```python
import torch

def _softmax(x, axis=-1):
    return torch.softmax(x, dim=axis)
def _gelu(x):
    return torch.nn.functional.gelu(x, approximate="tanh")
def _silu(x):
    return torch.nn.functional.silu(x)
def _sigmoid(x):
    return torch.sigmoid(x)
def _layernorm(x, axis=-1, eps=1e-5):
    m = x.mean(dim=axis, keepdim=True)
    v = x.var(dim=axis, keepdim=True, unbiased=False)
    return (x - m) / torch.sqrt(v + eps)
def _scatter_add(tgt, idx, src, axis=0):
    return tgt.clone().index_add_(axis, idx, src)
def _segment_sum(src, seg, n):
    out = torch.zeros((n,) + src.shape[1:], dtype=src.dtype)
    return out.index_add_(0, seg, src)
def _pad(x, pw):
    flat = tuple(v for pair in reversed(pw) for v in pair)
    return torch.nn.functional.pad(x, flat)

kernel_name = 'standalone__mha_block_cached__near_ridge'

def setup_inputs():

    _w = (lambda a: (lambda t: t.to(torch.bfloat16) if t.is_floating_point() else t)(torch.as_tensor(a)))
    B, Sq, D = 8, 192, 640
    H, Sk, Dh = 32, 512, 128
    return {
        "x": _w((lambda *s: torch.randn(*s, dtype=torch.bfloat16) * 0.8)(B, Sq, D)),
        "k_cache": _w((lambda *s: torch.randn(*s, dtype=torch.bfloat16) * 0.8)(B, H, Sk, Dh)),
        "v_cache": _w((lambda *s: torch.randn(*s, dtype=torch.bfloat16) * 0.05)(B, H, Sk, Dh)),
        "wq": _w((lambda *s: torch.randn(*s, dtype=torch.bfloat16) * 0.8)(D, H * Dh) * (D ** -0.5)),
        "wo": _w((lambda *s: torch.randn(*s, dtype=torch.bfloat16) * 0.05)(H * Dh, D) * ((H * Dh) ** -0.5)),
    }

def reference(x, k_cache, v_cache, wq, wo):
    B, Sq, D = x.shape
    H, Dh = 32, 128
    q = (lambda x, *d: x.permute(*d))((x @ wq).reshape(B, Sq, H, Dh), 0, 2, 1, 3)
    scores = torch.einsum("bhqd,bhkd->bhqk", q, k_cache) * (Dh ** -0.5)
    p = _softmax(scores, axis=-1)
    o = torch.einsum("bhqk,bhkd->bhqd", p, v_cache)
    return (lambda x, *d: x.permute(*d))(o, 0, 2, 1, 3).reshape(B, Sq, H * Dh) @ wo
```

```cpp
#include <hip/hip_runtime.h>
#include <hip/hip_bf16.h>
typedef __hip_bfloat16 bf16;
constexpr int B=8, SQ=192, DM=640, H=32, SK=512, DH=128;

__global__ void gemm_naive(const bf16* A, const bf16* Bm, bf16* C, int M, int N, int K) {
    int n = blockIdx.x * blockDim.x + threadIdx.x;
    int m = blockIdx.y;
    if (n >= N) return;
    float acc = 0.f;
    for (int k = 0; k < K; ++k) acc += __bfloat162float(A[(long)m*K+k]) * __bfloat162float(Bm[(long)k*N+n]);
    C[(long)m*N+n] = __float2bfloat16(acc);
}
__global__ void attn_naive(const bf16* Q, const bf16* Kc, const bf16* Vc, bf16* O) {
    int q = blockIdx.x, h = blockIdx.y, b = blockIdx.z, t = threadIdx.x;
    __shared__ float qs[DH]; __shared__ float s[SK]; __shared__ float red[128];
    qs[t] = __bfloat162float(Q[((long)(b*SQ+q))*(H*DH) + h*DH + t]);
    __syncthreads();
    const bf16* Kp = Kc + ((long)(b*H+h))*SK*DH; const bf16* Vp = Vc + ((long)(b*H+h))*SK*DH;
    float mx = -1e30f;
    for (int k = t; k < SK; k += 128) { float a = 0; for (int d = 0; d < DH; ++d) a += qs[d]*__bfloat162float(Kp[k*DH+d]); a *= 0.08838834764831845f; s[k] = a; mx = fmaxf(mx, a); }
    red[t] = mx; __syncthreads();
    for (int o = 64; o > 0; o >>= 1) { if (t < o) red[t] = fmaxf(red[t], red[t+o]); __syncthreads(); }
    mx = red[0]; __syncthreads();
    float sum = 0;
    for (int k = t; k < SK; k += 128) { float e = __expf(s[k]-mx); s[k] = e; sum += e; }
    red[t] = sum; __syncthreads();
    for (int o = 64; o > 0; o >>= 1) { if (t < o) red[t] += red[t+o]; __syncthreads(); }
    sum = red[0];
    float acc = 0;
    for (int k = 0; k < SK; ++k) acc += s[k]*__bfloat162float(Vp[k*DH+t]);
    O[((long)(b*SQ+q))*(H*DH) + h*DH + t] = __float2bfloat16(acc/sum);
}
extern "C" void kernel_launch(void* const* d_in, const int* in_sizes, int n_in,
                              void* d_out, int out_size, void* d_ws, size_t ws_size,
                              hipStream_t stream) {
    const bf16* x = (const bf16*)d_in[0]; const bf16* kc = (const bf16*)d_in[1]; const bf16* vc = (const bf16*)d_in[2];
    const bf16* wq = (const bf16*)d_in[3]; const bf16* wo = (const bf16*)d_in[4];
    bf16* Q = (bf16*)d_ws; bf16* O = Q + (long)B*SQ*H*DH;
    gemm_naive<<<dim3(H*DH/256, B*SQ), 256, 0, stream>>>(x, wq, Q, B*SQ, H*DH, DM);
    attn_naive<<<dim3(SQ, H, B), 128, 0, stream>>>(Q, kc, vc, O);
    gemm_naive<<<dim3((DM+127)/128, B*SQ), 128, 0, stream>>>(O, wo, (bf16*)d_out, B*SQ, DM, H*DH);
}
```

```cpp
#include <hip/hip_runtime.h>
#include <hip/hip_bf16.h>
#include <stdint.h>

typedef __hip_bfloat16 bf16;
using bf16x8 = __attribute__((ext_vector_type(8))) short;
using s16x4  = __attribute__((ext_vector_type(4))) short;
using f32x16 = __attribute__((ext_vector_type(16))) float;
using f32x4  = __attribute__((ext_vector_type(4))) float;
using u32x4  = __attribute__((ext_vector_type(4))) unsigned;
typedef short v4i16_t __attribute__((ext_vector_type(4)));
#define LAS __attribute__((address_space(3)))
typedef LAS const char* lds_cptr;
typedef LAS char* lds_ptr;
typedef __amdgpu_buffer_rsrc_t srd_t;
#define DI __device__ __forceinline__
#define SBAR() __builtin_amdgcn_sched_barrier(0)
#define PIN(x) asm volatile("" : "+v"(x))
#define MFMA(a, b, c) __builtin_amdgcn_mfma_f32_32x32x16_bf16(a, b, c, 0, 0, 0)

constexpr int NB = 8, SQ = 192, DM = 640, NH = 32, SK = 512, DH = 128, HD = NH * DH;
constexpr float QSCALE = 0.08838834764831845f * 1.4426950408889634f;
#define PSLIM 65536.0f

#define TS(i)

typedef float f32x2 __attribute__((ext_vector_type(2)));
typedef __bf16 bf16x2 __attribute__((ext_vector_type(2)));
DI unsigned cvtpk(float lo, float hi) { f32x2 v = {lo, hi}; bf16x2 b = __builtin_convertvector(v, bf16x2); return __builtin_bit_cast(unsigned, b); }
DI float fmul_s(float a, float b) { return a * b; }
template <bool NT>
DI void dma16(srd_t srd, unsigned voff, unsigned soff, unsigned lds_dst) {
    if constexpr (NT) asm volatile("s_mov_b32 m0, %3\n\ts_nop 0\n\tbuffer_load_dwordx4 %0, %1, %2 offen nt lds" :: "v"(voff), "s"(srd), "s"(soff), "s"(lds_dst) : "m0", "memory");
    else              asm volatile("s_mov_b32 m0, %3\n\ts_nop 0\n\tbuffer_load_dwordx4 %0, %1, %2 offen lds" :: "v"(voff), "s"(srd), "s"(soff), "s"(lds_dst) : "m0", "memory");
}
DI bf16x8 ldsr128(lds_cptr p) { return *(const LAS bf16x8*)p; }
DI s16x4 ldstr(lds_cptr p) { return __builtin_bit_cast(s16x4, __builtin_amdgcn_ds_read_tr16_b64_v4i16((LAS v4i16_t*)p)); }
DI bf16x8 cat8(s16x4 a, s16x4 b) { return __builtin_shufflevector(a, b, 0, 1, 2, 3, 4, 5, 6, 7); }
#define WAITV(N) asm volatile("s_waitcnt vmcnt(" #N ")" ::: "memory")
#define BAR_L() asm volatile("s_waitcnt lgkmcnt(0)\n\ts_barrier" ::: "memory")
DI float xhalf_max(float v) { auto rr = __builtin_amdgcn_permlane32_swap(__float_as_uint(v), __float_as_uint(v), false, false); return fmaxf(__uint_as_float(rr[0]), __uint_as_float(rr[1])); }
DI float xhalf_sum(float v) { auto rr = __builtin_amdgcn_permlane32_swap(__float_as_uint(v), __float_as_uint(v), false, false); return __uint_as_float(rr[0]) + __uint_as_float(rr[1]); }

#define KV_NT true
#define XW_NT false

constexpr int SLOT = 16384;
constexpr int RING0 = 5 * SLOT, RSTG = 20480, RXB = 12288;
constexpr int POFF = 6 * SLOT;
constexpr int MOFF = 9 * SLOT;
constexpr int LDS_BYTES = 10 * SLOT;
constexpr unsigned KSLOTS = 0x04215310u;
constexpr unsigned VSLOTS = 0x32153042u;
DI int kslot(int t) { return ((KSLOTS >> (4 * t)) & 15u) * SLOT; }
DI int vslot(int t) { return ((VSLOTS >> (4 * t)) & 15u) * SLOT; }

__global__ __launch_bounds__(512, 2) void k1_attn(const bf16* __restrict__ x, const bf16* __restrict__ kc, const bf16* __restrict__ vc,
                                                  const bf16* __restrict__ wq, bf16* __restrict__ ows) {
    extern __shared__ __attribute__((aligned(16))) char lds[];
    TS(0);
    const int tid = threadIdx.x, lane = tid & 63, r32 = lane & 31, hh = lane >> 5;
    const int wid = __builtin_amdgcn_readfirstlane(tid >> 6), w4 = wid & 3;
    const int xcd = blockIdx.x & 7, jj = blockIdx.x >> 3;
    const int bl = jj >> 3, hl = jj & 7;
    const int b = (xcd >> 2) * 4 + bl, h = (xcd & 3) * 8 + hl;
    const unsigned lds0 = (unsigned)(uintptr_t)lds;
    const lds_cptr L = (lds_cptr)lds;
    const lds_ptr LW = (lds_ptr)lds;
    const bf16* Kg = kc + (size_t)(b * NH + h) * SK * DH;
    const bf16* Vg = vc + (size_t)(b * NH + h) * SK * DH;
    const srd_t srdK = __builtin_amdgcn_make_buffer_rsrc((void*)Kg, (short)0, SK * DH * 2, 0x00020000);
    const srd_t srdV = __builtin_amdgcn_make_buffer_rsrc((void*)Vg, (short)0, SK * DH * 2, 0x00020000);
    const int qh = w4 & 1, kh = w4 >> 1;
    const int q4 = (lane & 15) >> 2, p4 = lane & 3, blk = (lane >> 4) & 1;
    const int arow = 8 * (w4 >> 1) + ((lane >> 2) & 7), ach = 4 * (2 * (w4 & 1) + hh) + (p4 ^ ((2 * (w4 >> 1) + blk) & 3));
    const unsigned kvoff = arow * 256 + ach * 16;
    auto kv_piece = [&](bool isV, int t, int i) {
        dma16<KV_NT>(isV ? srdV : srdK, kvoff, (unsigned)(t * 16384 + i * 4096), lds0 + (isV ? vslot(t) : kslot(t)) + (4 * i + w4) * 1024);
    };
    auto kv_unit = [&](bool isV, int t) {
#pragma unroll
        for (int i = 0; i < 4; ++i) kv_piece(isV, t, i);
    };
    LAS unsigned* flagw = (LAS unsigned*)(LW + MOFF);
    LAS float* prop = (LAS float*)(LW + MOFF + 256);
    LAS float* lsum = (LAS float*)(LW + MOFF + 2048);

    if (wid < 4) {
        bf16x8 qf[3][8];
        {
            f32x16 acc[3][2];
#pragma unroll
            for (int i = 0; i < 3; ++i) { acc[i][0] = f32x16{}; acc[i][1] = f32x16{}; }
            const int xsw = (r32 >> 2) & 3;
            const int xa0 = (96 * qh + r32) * 64 + ((hh ^ xsw) << 4), xa1 = (96 * qh + r32) * 64 + (((2 + hh) ^ xsw) << 4);
            const int ps = ((p4 & 1) << 1) | (p4 >> 1);
            const int wa0 = RXB + 2048 * hh + 1024 * kh + 64 * q4 + (((2 * blk + (ps >> 1)) ^ (2 * hh)) << 4) + 8 * (ps & 1);
            const int wa1 = RXB + 2048 * hh + 1024 * kh + 64 * q4 + 256 + (((2 * blk + (ps >> 1)) ^ (2 * hh + 1)) << 4) + 8 * (ps & 1);
            bf16x8 xf[2][3]; s16x4 wl[2][2], wh[2][2];
#define RDX(buf, base, ks, i) xf[buf][i] = ldsr128(base + (ks ? xa1 : xa0) + i * 2048)
#define RDW(buf, base, ks, db) do { wl[buf][db] = ldstr(base + wa0 + ks * 4096 + db * 512); wh[buf][db] = ldstr(base + wa1 + ks * 4096 + db * 512); } while (0)
#define MM(buf, i, db) acc[i][db] = MFMA(cat8(wl[buf][db], wh[buf][db]), xf[buf][i], acc[i][db])
#define HALF(cb, base, ks) do { \
                MM(cb, 0, 0); RDW(cb ^ 1, base, ks, 0); SBAR(); \
                MM(cb, 0, 1); RDX(cb ^ 1, base, ks, 0); SBAR(); \
                MM(cb, 1, 0); RDW(cb ^ 1, base, ks, 1); SBAR(); \
                MM(cb, 1, 1); RDX(cb ^ 1, base, ks, 1); SBAR(); \
                MM(cb, 2, 0); RDX(cb ^ 1, base, ks, 2); SBAR(); \
                MM(cb, 2, 1); SBAR(); } while (0)
            kv_piece(false, 0, 0);
            BAR_L();
            TS(4);
            { const lds_cptr base = L + RING0 + RSTG;
              RDW(0, base, 0, 0); RDX(0, base, 0, 0); RDW(0, base, 0, 1); RDX(0, base, 0, 1); RDX(0, base, 0, 2); SBAR();
              HALF(0, base, 1); }
#pragma unroll 1
            for (int s = 1; s < 20; ++s) {
                BAR_L();
                const lds_cptr base = L + RING0 + ((s + 1) & 3) * RSTG;
                HALF(1, base, 0);
                {
                    const int u = s >> 2, i = s & 3;
                    const bool isV = (u == 2) | (u == 4); const int t = (u == 0) ? 0 : (u == 1) ? 1 : (u == 2) ? 0 : (u == 3) ? 2 : 1;
                    kv_piece(isV, t, i); SBAR();
                }
                HALF(0, base, 1);
            }
            MM(1, 0, 0); MM(1, 0, 1); MM(1, 1, 0); MM(1, 1, 1); MM(1, 2, 0); MM(1, 2, 1);
            TS(5);
            BAR_L();
#pragma unroll
            for (int i = 0; i < 3; ++i)
#pragma unroll
                for (int db = 0; db < 2; ++db)
#pragma unroll
                    for (int s = 0; s < 2; ++s) {
                        u32x4 w;
#pragma unroll
                        for (int e = 0; e < 4; ++e) w[e] = cvtpk(fmul_s(acc[i][db][8 * s + 2 * e], QSCALE), fmul_s(acc[i][db][8 * s + 2 * e + 1], QSCALE));
                        *(LAS u32x4*)(LW + POFF + (w4 ^ 2) * 12288 + (i * 4 + db * 2 + s) * 1024 + lane * 16) = w;
                        qf[i][db * 2 + s] = __builtin_bit_cast(bf16x8, w);
                    }
            WAITV(12);
            BAR_L();
#pragma unroll
            for (int i = 0; i < 3; ++i)
#pragma unroll
                for (int j = 0; j < 4; ++j) qf[i][4 + j] = ldsr128(L + POFF + w4 * 12288 + (i * 4 + j) * 1024 + lane * 16);
        }
        TS(1);
        const int krow = 32 * kh + r32;
        const int kb0 = 2048 * (krow >> 3) + 64 * (krow & 7) + ((hh ^ ((krow >> 2) & 3)) << 4);
        const int kb1 = 2048 * (krow >> 3) + 64 * (krow & 7) + (((2 + hh) ^ ((krow >> 2) & 3)) << 4);
        const int kown = kh * 1024, koth = (1 - kh) * 1024;
        bf16x8 kf[8];
        auto kread = [&](int t) {
            const lds_cptr kb = L + kslot(t);
#pragma unroll
            for (int j = 0; j < 4; ++j) { kf[j] = ldsr128(kb + kown + ((j & 1) ? kb1 : kb0) + 512 * (j >> 1)); kf[4 + j] = ldsr128(kb + koth + ((j & 1) ? kb1 : kb0) + 512 * (j >> 1)); }
        };
        f32x16 negm[3];
        float l[3], m[3];
#pragma unroll
        for (int i = 0; i < 3; ++i) { negm[i] = f32x16{}; l[i] = 0.f; m[i] = 0.f; }
        kread(0);
        bool careful = true; int t = 0;
#pragma unroll 1
        while (t < 8) {
            const lds_ptr pw = LW + POFF + w4 * 12288 + (t & 1) * 6144 + lane * 16;
            float lnew[3];
            if (careful) {
                if (t > 0) {
#pragma unroll
                    for (int j = 0; j < 8; ++j) { const int s = (j < 4) ? 4 * kh + j : 4 * (1 - kh) + (j - 4);
                        kf[j] = *(const bf16x8*)(Kg + (size_t)(t * 64 + krow) * DH + s * 16 + hh * 8); }
                }
                f32x16 sc[3]; float d[3];
#pragma unroll
                for (int i = 0; i < 3; ++i) {
                    sc[i] = negm[i];
#pragma unroll
                    for (int j = 0; j < 8; ++j) sc[i] = MFMA(kf[j], qf[i][j], sc[i]);
                    float pm = sc[i][0];
#pragma unroll
                    for (int r = 1; r < 16; ++r) pm = fmaxf(pm, sc[i][r]);
                    pm = xhalf_max(pm);
                    d[i] = (t > 0) ? fmaxf(pm, 0.f) : pm;
                    if (hh == 0) prop[(w4 * 3 + i) * 32 + r32] = d[i];
                }
                BAR_L();
#pragma unroll
                for (int i = 0; i < 3; ++i) {
                    const float dl = fmaxf(d[i], prop[((w4 ^ 2) * 3 + i) * 32 + r32]);
                    m[i] += dl; l[i] = (t > 0) ? l[i] * __builtin_amdgcn_exp2f(-dl) : 0.f;
                    float psum = 0.f;
#pragma unroll
                    for (int r = 0; r < 16; ++r) { negm[i][r] = -m[i]; sc[i][r] = __builtin_amdgcn_exp2f(sc[i][r] - dl); psum += sc[i][r]; }
                    lnew[i] = l[i] + psum;
#pragma unroll
                    for (int s = 0; s < 2; ++s) { u32x4 w;
#pragma unroll
                        for (int e = 0; e < 4; ++e) w[e] = cvtpk(sc[i][8 * s + 2 * e], sc[i][8 * s + 2 * e + 1]);
                        *(LAS u32x4*)(pw + (i * 2 + s) * 1024) = w; }
                }
            } else {
                bool bad = false;
#pragma unroll
                for (int i = 0; i < 3; ++i) {
                    f32x16 sc = MFMA(kf[0], qf[i][0], negm[i]);
#pragma unroll
                    for (int j = 1; j < 8; ++j) sc = MFMA(kf[j], qf[i][j], sc);
                    float pa = 0.f;
#pragma unroll
                    for (int r = 0; r < 16; ++r) { sc[r] = __builtin_amdgcn_exp2f(sc[r]); pa = r ? pa + sc[r] : sc[r]; }
                    bad |= !(pa <= PSLIM);
                    lnew[i] = l[i] + pa;
#pragma unroll
                    for (int s = 0; s < 2; ++s) { u32x4 w;
#pragma unroll
                        for (int e = 0; e < 4; ++e) w[e] = cvtpk(sc[8 * s + 2 * e], sc[8 * s + 2 * e + 1]);
                        *(LAS u32x4*)(pw + (i * 2 + s) * 1024) = w; }
                }
                if (__builtin_expect(__any(bad), 0)) { if (lane == 0) flagw[t] = 1u; }
            }
            if (t < 7) kread(t + 1);
            else {
#pragma unroll
                for (int i = 0; i < 3; ++i) { const float ls = xhalf_sum(lnew[i]); if (hh == 0) lsum[(w4 * 3 + i) * 32 + r32] = ls; }
            }
            if (t == 0) WAITV(4); else WAITV(0);
            BAR_L();
            const unsigned fl = __builtin_amdgcn_readfirstlane(flagw[t]);
            if (!careful && fl) careful = true;
            else { careful = false; ++t;
#pragma unroll
                for (int i = 0; i < 3; ++i) l[i] = lnew[i]; }
            if (t == 1) TS(8); if (t == 2) TS(9); if (t == 4) TS(10); if (t == 6) TS(11);
        }
        TS(2);
    } else {
        {
            const int rot = (hl * 5 + bl * 10 + 1) / 2 % 20;
            const srd_t srdX = __builtin_amdgcn_make_buffer_rsrc((void*)(x + (size_t)b * SQ * DM), (short)0, SQ * DM * 2, 0x00020000);
            const srd_t srdW = __builtin_amdgcn_make_buffer_rsrc((void*)(wq + h * DH), (short)0, DM * HD * 2 - h * DH * 2, 0x00020000);
            const unsigned xvoff = (lane >> 2) * (DM * 2) + (((lane & 3) ^ ((lane >> 4) & 3)) << 4);
            const unsigned wvoff = arow * (HD * 2) + ach * 16;
            auto issue = [&](int s) {
                int cs = s + rot; if (cs >= 20) cs -= 20;
                const unsigned dst = lds0 + RING0 + ((s + 1) & 3) * RSTG;
#pragma unroll
                for (int i = 0; i < 3; ++i) dma16<XW_NT>(srdX, xvoff, (unsigned)((w4 * 3 + i) * 16 * DM * 2 + cs * 64), dst + (w4 * 3 + i) * 1024);
#pragma unroll
                for (int i = 0; i < 2; ++i) dma16<XW_NT>(srdW, wvoff, (unsigned)((cs * 32 + i * 16) * HD * 2), dst + RXB + (4 * i + w4) * 1024);
            };
            issue(0); issue(1); issue(2);
#pragma unroll 1
            for (int s = 0; s < 17; ++s) { WAITV(10); BAR_L(); issue(s + 3); }
            WAITV(10); BAR_L(); WAITV(5); BAR_L(); WAITV(0); BAR_L();
            BAR_L();
            if (wid == 4 && lane < 16) flagw[lane] = 0u;
            kv_unit(false, 3);
            BAR_L();
        }
        f32x16 o[3][2];
#pragma unroll
        for (int i = 0; i < 3; ++i) { o[i][0] = f32x16{}; o[i][1] = f32x16{}; }
        const int vb0 = 1024 * kh + 64 * (4 * hh + q4) + (((2 * blk + (p4 >> 1)) ^ hh) << 4) + 8 * (p4 & 1);
        const int vb1 = 1024 * kh + 2048 + 64 * (4 * hh + q4) + (((2 * blk + (p4 >> 1)) ^ (2 + hh)) << 4) + 8 * (p4 & 1);
        auto pv = [&](int t) {
            const lds_cptr vb = L + vslot(t);
            const lds_cptr pb = L + POFF + qh * 12288 + (t & 1) * 6144 + lane * 16;
#pragma unroll
            for (int ks = 0; ks < 4; ++ks) {
                bf16x8 vf[2], pf[3];
#pragma unroll
                for (int db = 0; db < 2; ++db) vf[db] = cat8(ldstr(vb + vb0 + ks * 4096 + db * 512), ldstr(vb + vb1 + ks * 4096 + db * 512));
#pragma unroll
                for (int i = 0; i < 3; ++i) pf[i] = ldsr128(pb + (ks >> 1) * 24576 + (i * 2 + (ks & 1)) * 1024);
#pragma unroll
                for (int i = 0; i < 3; ++i)
#pragma unroll
                    for (int db = 0; db < 2; ++db) o[i][db] = MFMA(vf[db], pf[i], o[i][db]);
            }
        };
        bool careful = true, redo = false; int t = 0;
#pragma unroll 1
        while (t < 8) {
            if (!redo) {
                if (t == 1) kv_unit(false, 4);
                else if (t >= 2 && t <= 4) { kv_unit(true, t + 1); kv_unit(false, t + 3); }
                else if (t == 5) { kv_unit(true, 6); kv_unit(true, 7); }
                if (t > 0) pv(t - 1);
            }
            if (careful) {
                BAR_L();
                if (redo) {
#pragma unroll
                    for (int i = 0; i < 3; ++i) {
                        const float dl = fmaxf(prop[(qh * 3 + i) * 32 + r32], prop[((qh + 2) * 3 + i) * 32 + r32]);
                        const float f = __builtin_amdgcn_exp2f(-dl);
#pragma unroll
                        for (int r = 0; r < 16; ++r) { o[i][0][r] *= f; o[i][1][r] *= f; }
                    }
                } else kv_unit(true, 2);
            }
            if (t == 0) { } else if (t == 1) WAITV(8); else if (t <= 5) WAITV(8); else if (t == 6) WAITV(4); else WAITV(0);
            BAR_L();
            const unsigned fl = __builtin_amdgcn_readfirstlane(flagw[t]);
            if (!careful && fl) { careful = true; redo = true; }
            else { careful = false; redo = false; ++t; }
        }
        pv(7);
        TS(6);
        char* og = (char*)ows + ((size_t)((b * NH + h) * 6 + 3 * qh) * 8 + kh * 4) * 1024 + lane * 16;
#pragma unroll
        for (int i = 0; i < 3; ++i) {
            const float rl = 1.0f / (lsum[(qh * 3 + i) * 32 + r32] + lsum[((qh + 2) * 3 + i) * 32 + r32]);
#pragma unroll
            for (int db = 0; db < 2; ++db)
#pragma unroll
                for (int s = 0; s < 2; ++s) { u32x4 w;
#pragma unroll
                    for (int e = 0; e < 4; ++e) w[e] = cvtpk(o[i][db][8 * s + 2 * e] * rl, o[i][db][8 * s + 2 * e + 1] * rl);
                    *(u32x4*)(og + (i * 8 + db * 2 + s) * 1024) = w; }
        }
        TS(3);
    }
}

#define MFMA16(a, b, c) __builtin_amdgcn_mfma_f32_16x16x32_bf16(a, b, c, 0, 0, 0)
__global__ __launch_bounds__(512, 2) void k2_oproj(const bf16* __restrict__ ows, const bf16* __restrict__ wo, float* __restrict__ part) {
    extern __shared__ __attribute__((aligned(16))) char lds[];
    TS(12);
    const int tid = threadIdx.x, lane = tid & 63;
    const int wid = __builtin_amdgcn_readfirstlane(tid >> 6);
    const int xcd = blockIdx.x & 7, jj = blockIdx.x >> 3;
    const int b = (xcd >> 2) * 4 + (jj >> 3), hg = xcd & 3, c = jj & 7;
    const unsigned lds0 = (unsigned)(uintptr_t)lds;
    const lds_cptr L = (lds_cptr)lds; const lds_ptr LW = (lds_ptr)lds;
    const srd_t srdW = __builtin_amdgcn_make_buffer_rsrc((void*)(wo + (size_t)hg * 1024 * DM + c * 80), (short)0, 1024 * DM * 2 - c * 160, 0x00020000);
    const int qg = wid & 3, kh2 = wid >> 2;
    unsigned wv[5];
#pragma unroll
    for (int p = 0; p < 5; ++p) { const int o = p * 1024 + lane * 16, row = o / 160, ch = (o % 160) >> 4; wv[p] = row * (DM * 2) + ch * 16; }
#pragma unroll
    for (int i = 0; i < 4; ++i) {
        const int gi = kh2 * 16 + i * 4 + qg;
#pragma unroll
        for (int p = 0; p < 5; ++p) dma16<false>(srdW, wv[p], (unsigned)(gi * 32 * DM * 2), lds0 + gi * 5120 + p * 1024);
    }
    const int g = lane >> 4, qq = lane & 15;
    const char* obase = (const char*)ows + (size_t)(b * NH + hg * 8 + kh2 * 4) * 6 * 8192 + (g >> 1) * 1024 + (g & 1) * 512 + qq * 16;
    int ooff[3];
#pragma unroll
    for (int i = 0; i < 3; ++i) { const int t16 = 3 * qg + i; ooff[i] = (t16 >> 1) * 8192 + (t16 & 1) * 256; }
    bf16x8 of[16][3];
#pragma unroll
    for (int ks = 0; ks < 16; ++ks)
#pragma unroll
        for (int i = 0; i < 3; ++i) of[ks][i] = *(const bf16x8*)(obase + (ks >> 2) * 49152 + (ks & 3) * 2048 + ooff[i]);
    f32x4 acc[5][3];
#pragma unroll
    for (int n = 0; n < 5; ++n)
#pragma unroll
        for (int i = 0; i < 3; ++i) acc[n][i] = f32x4{};
    const int q4 = (lane & 15) >> 2, p4 = lane & 3;
    const int wa = (kh2 * 512 + 16 * (g >> 1) + 4 * (g & 1) + q4) * 160 + p4 * 8;
    asm volatile("s_waitcnt vmcnt(0)" ::: "memory");
    BAR_L();
    TS(13);
#pragma unroll
    for (int ks = 0; ks < 16; ++ks) {
#pragma unroll
        for (int n = 0; n < 5; ++n) {
            const bf16x8 wf = cat8(ldstr(L + wa + ks * 5120 + n * 32), ldstr(L + wa + ks * 5120 + 1280 + n * 32));
#pragma unroll
            for (int i = 0; i < 3; ++i) acc[n][i] = MFMA16(wf, of[ks][i], acc[n][i]);
        }
    }
    BAR_L();
    if (kh2) {
#pragma unroll
        for (int n = 0; n < 5; ++n)
#pragma unroll
            for (int i = 0; i < 3; ++i) *(LAS f32x4*)(LW + ((qg * 15 + n * 3 + i) * 64 + lane) * 16) = acc[n][i];
    }
    BAR_L();
    if (!kh2) {
        f32x4* pp = (f32x4*)part + ((size_t)(((hg * NB + b) * 8 + c) * 4 + qg) * 15) * 64 + lane;
#pragma unroll
        for (int n = 0; n < 5; ++n)
#pragma unroll
            for (int i = 0; i < 3; ++i) pp[(n * 3 + i) * 64] = acc[n][i] + *(const LAS f32x4*)(L + ((qg * 15 + n * 3 + i) * 64 + lane) * 16);
    }
    TS(14);
}
__global__ __launch_bounds__(256) void k3_reduce(const float* __restrict__ part, bf16* __restrict__ out) {
    const int idx = blockIdx.x * 256 + threadIdx.x;
    const int lane = idx & 63, f = idx >> 6, ni = f % 15, qg = (f / 15) & 3, c = (f / 60) & 7, b = f / 480;
    const int n = ni / 3, i = ni % 3;
    const f32x4* pp = (const f32x4*)part + idx;
    const f32x4 v = pp[0] + pp[245760] + pp[2 * 245760] + pp[3 * 245760];
    const int q = (3 * qg + i) * 16 + (lane & 15), col = c * 80 + n * 16 + (lane >> 4) * 4;
    uint2 w; w.x = cvtpk(v[0], v[1]); w.y = cvtpk(v[2], v[3]);
    *(uint2*)(out + (size_t)(b * SQ + q) * DM + col) = w;
}

extern "C" void kernel_launch(void* const* d_in, const int* in_sizes, int n_in, void* d_out, int out_size, void* d_ws, size_t ws_size, hipStream_t stream) {
    const bf16* x = (const bf16*)d_in[0]; const bf16* kc = (const bf16*)d_in[1]; const bf16* vc = (const bf16*)d_in[2];
    const bf16* wq = (const bf16*)d_in[3]; const bf16* wo = (const bf16*)d_in[4];
    bf16* ows = (bf16*)d_ws;
    float* part = (float*)((char*)d_ws + (16 << 20));
    (void)hipFuncSetAttribute((const void*)k1_attn, hipFuncAttributeMaxDynamicSharedMemorySize, LDS_BYTES);
    (void)hipFuncSetAttribute((const void*)k2_oproj, hipFuncAttributeMaxDynamicSharedMemorySize, LDS_BYTES);
    k1_attn<<<256, 512, LDS_BYTES, stream>>>(x, kc, vc, wq, ows);
    k2_oproj<<<256, 512, LDS_BYTES, stream>>>(ows, wo, part);
    k3_reduce<<<960, 256, 0, stream>>>(part, (bf16*)d_out);
}
```

```cpp
#include <hip/hip_runtime.h>
#include <hip/hip_bf16.h>
#include <stdint.h>
#include <type_traits>

typedef __hip_bfloat16 bf16;
using bf16x8 = __attribute__((ext_vector_type(8))) short;
using s16x4  = __attribute__((ext_vector_type(4))) short;
using f32x16 = __attribute__((ext_vector_type(16))) float;
using f32x4  = __attribute__((ext_vector_type(4))) float;
using u32x4  = __attribute__((ext_vector_type(4))) unsigned;
typedef short v4i16_t __attribute__((ext_vector_type(4)));
#define LAS __attribute__((address_space(3)))
typedef LAS const char* lds_cptr;
typedef LAS char* lds_ptr;
typedef __amdgpu_buffer_rsrc_t srd_t;
#define DI __device__ __forceinline__
#define SBAR() __builtin_amdgcn_sched_barrier(0)
#define PIN(x) asm volatile("" : "+v"(x))
#define MFMA(a, b, c) __builtin_amdgcn_mfma_f32_32x32x16_bf16(a, b, c, 0, 0, 0)

constexpr int NB = 8, SQ = 192, DM = 640, NH = 32, SK = 512, DH = 128, HD = NH * DH;
constexpr float QSCALE = 0.08838834764831845f * 1.4426950408889634f;
#define PSLIM 65536.0f

#define TS(i)

typedef float f32x2 __attribute__((ext_vector_type(2)));
typedef __bf16 bf16x2 __attribute__((ext_vector_type(2)));
DI unsigned cvtpk(float lo, float hi) { f32x2 v = {lo, hi}; bf16x2 b = __builtin_convertvector(v, bf16x2); return __builtin_bit_cast(unsigned, b); }
DI float fmul_s(float a, float b) { return a * b; }
template <bool NT>
DI void dma16(srd_t srd, unsigned voff, unsigned soff, unsigned lds_dst_) {
    const unsigned lds_dst = __builtin_amdgcn_readfirstlane(lds_dst_);
    if constexpr (NT) asm volatile("s_mov_b32 m0, %3\n\ts_nop 0\n\tbuffer_load_dwordx4 %0, %1, %2 offen nt lds" :: "v"(voff), "s"(srd), "s"(soff), "s"(lds_dst) : "m0", "memory");
    else              asm volatile("s_mov_b32 m0, %3\n\ts_nop 0\n\tbuffer_load_dwordx4 %0, %1, %2 offen lds" :: "v"(voff), "s"(srd), "s"(soff), "s"(lds_dst) : "m0", "memory");
}
DI bf16x8 ldsr128(lds_cptr p) { return *(const LAS bf16x8*)p; }
DI s16x4 ldstr(lds_cptr p) { return __builtin_bit_cast(s16x4, __builtin_amdgcn_ds_read_tr16_b64_v4i16((LAS v4i16_t*)p)); }
DI bf16x8 cat8(s16x4 a, s16x4 b) { return __builtin_shufflevector(a, b, 0, 1, 2, 3, 4, 5, 6, 7); }
#define WAITV(N) asm volatile("s_waitcnt vmcnt(" #N ")" ::: "memory")
#define BAR_L() asm volatile("s_waitcnt lgkmcnt(0)\n\ts_barrier" ::: "memory")
DI float xhalf_max(float v) { auto rr = __builtin_amdgcn_permlane32_swap(__float_as_uint(v), __float_as_uint(v), false, false); return fmaxf(__uint_as_float(rr[0]), __uint_as_float(rr[1])); }
DI float xhalf_sum(float v) { auto rr = __builtin_amdgcn_permlane32_swap(__float_as_uint(v), __float_as_uint(v), false, false); return __uint_as_float(rr[0]) + __uint_as_float(rr[1]); }

#define KV_NT true
#define XW_NT false

#define GAS __attribute__((address_space(1)))
typedef GAS unsigned gu32;
#define RLX_AGENT __ATOMIC_RELAXED, __HIP_MEMORY_SCOPE_AGENT
#define MFMA16(a, b, c) __builtin_amdgcn_mfma_f32_16x16x32_bf16(a, b, c, 0, 0, 0)
DI void st16_wt(u32x4 v, srd_t srd, unsigned voff, unsigned soff) {
    __builtin_amdgcn_raw_buffer_store_b128(v, srd, voff, soff, 16); SBAR(); asm volatile("s_nop 1"); SBAR();
}
constexpr unsigned ECHO_KEY = 0x5A17C3E9u;
constexpr unsigned SPIN_CAP = 1u << 20;
constexpr size_t WS_O = 0, WS_PART = 16u << 20, WS_SYNC = 48u << 20;
constexpr int SLOT = 16384;
constexpr int MOFF = 32768;
constexpr int RING0 = 40960, RSTG = 40960, RXB = 24576;
constexpr int POFF = 106496;
constexpr int LDS_BYTES = 163840;
constexpr unsigned KSLOTS = 0x04215310u;
constexpr unsigned VSLOTS = 0x32153042u;
DI int slot_addr(unsigned s) { return s * SLOT + (s >= 2 ? 8192 : 0); }
DI int kslot(int t) { return slot_addr((KSLOTS >> (4 * t)) & 15u); }
DI int vslot(int t) { return slot_addr((VSLOTS >> (4 * t)) & 15u); }
#define GROUPB(bl, hl) (((bl) + (hl)) & 1)

__global__ __launch_bounds__(512, 2) void k1_attn(const bf16* __restrict__ x, const bf16* __restrict__ kc, const bf16* __restrict__ vc,
                                                  const bf16* __restrict__ wq, const bf16* __restrict__ wo, char* __restrict__ ws, bf16* __restrict__ out) {
    extern __shared__ __attribute__((aligned(16))) char lds[];
    TS(0);
    const int tid = threadIdx.x, lane = tid & 63, r32 = lane & 31, hh = lane >> 5;
    const int wid = __builtin_amdgcn_readfirstlane(tid >> 6), w4 = wid & 3;
    const int xcd = blockIdx.x & 7, jj = blockIdx.x >> 3;
    const int bl = jj >> 3, hl = jj & 7;
    const int b = (xcd >> 2) * 4 + bl, h = (xcd & 3) * 8 + hl;
    const unsigned lds0 = (unsigned)(uintptr_t)lds;
    const lds_cptr L = (lds_cptr)lds;
    const lds_ptr LW = (lds_ptr)lds;
    const bf16* Kg = kc + (size_t)(b * NH + h) * SK * DH;
    const bf16* Vg = vc + (size_t)(b * NH + h) * SK * DH;
    const int qh = w4 & 1, kh = w4 >> 1;
    const int q4 = (lane & 15) >> 2, p4 = lane & 3, blk = (lane >> 4) & 1;
    LAS unsigned* flagw = (LAS unsigned*)(LW + MOFF);
    LAS unsigned* basew = (LAS unsigned*)(LW + MOFF + 128);
    const int hg = xcd & 3;
    gu32* const F = (gu32*)(ws + WS_SYNC);
    gu32* const E = (gu32*)(ws + WS_SYNC + 65536);
#define PEER(l_) ((l_) < 8 ? xcd + 8 * (bl * 8 + (l_)) : ((xcd & 4) | ((l_) & 3)) + 8 * jj)
#define PFLAG(l_) (F + (((l_) < 8 ? 0 : 256) + PEER(l_)) * 32)
    LAS float* prop = (LAS float*)(LW + MOFF + 256);
    LAS float* lsum = (LAS float*)(LW + MOFF + 2048);

    if (wid < 4) {
        if (wid == 0 && lane < 12) {
            const unsigned base = __hip_atomic_load(PFLAG(lane), RLX_AGENT);
            basew[lane] = base;
            __hip_atomic_store(E + blockIdx.x * 32 + lane, base ^ ECHO_KEY, RLX_AGENT);
        }
        bf16x8 qf[3][8];
        {
            f32x16 acc[3][2];
#pragma unroll
            for (int i = 0; i < 3; ++i) { acc[i][0] = f32x16{}; acc[i][1] = f32x16{}; }
            const int xsw = (r32 >> 1) & 7;
            int xa[4];
#pragma unroll
            for (int ks = 0; ks < 4; ++ks) xa[ks] = (96 * qh + r32) * 128 + (((2 * ks + hh) ^ xsw) << 4);
            const int ps = ((p4 & 1) << 1) | (p4 >> 1);
            const int wa0 = RXB + 2048 * hh + 1024 * kh + 64 * q4 + (((2 * blk + (ps >> 1)) ^ (2 * hh)) << 4) + 8 * (ps & 1);
            const int wa1 = RXB + 2048 * hh + 1024 * kh + 64 * q4 + 256 + (((2 * blk + (ps >> 1)) ^ (2 * hh + 1)) << 4) + 8 * (ps & 1);
            bf16x8 xf[2][3]; s16x4 wl[2][2], wh[2][2];
#define RDX(buf, base, ks, i) xf[buf][i] = ldsr128(base + xa[ks] + i * 4096)
#define RDW(buf, base, ks, db) do { wl[buf][db] = ldstr(base + wa0 + ks * 4096 + db * 512); wh[buf][db] = ldstr(base + wa1 + ks * 4096 + db * 512); } while (0)
#define MM(buf, i, db) acc[i][db] = MFMA(cat8(wl[buf][db], wh[buf][db]), xf[buf][i], acc[i][db])
#define HALF(cb, base, ks) do { \
                MM(cb, 0, 0); RDW(cb ^ 1, base, ks, 0); SBAR(); \
                MM(cb, 0, 1); RDX(cb ^ 1, base, ks, 0); SBAR(); \
                MM(cb, 1, 0); RDW(cb ^ 1, base, ks, 1); SBAR(); \
                MM(cb, 1, 1); RDX(cb ^ 1, base, ks, 1); SBAR(); \
                MM(cb, 2, 0); RDX(cb ^ 1, base, ks, 2); SBAR(); \
                MM(cb, 2, 1); SBAR(); } while (0)
            BAR_L();
            TS(4);
            { const lds_cptr base = L + RING0;
              RDW(0, base, 0, 0); RDX(0, base, 0, 0); RDW(0, base, 0, 1); RDX(0, base, 0, 1); RDX(0, base, 0, 2); SBAR();
              HALF(0, base, 1); HALF(1, base, 2); HALF(0, base, 3); }
            int rb = RSTG;
#pragma unroll 1
            for (int s = 1; s < 10; ++s) {
                SBAR(); BAR_L(); SBAR();
                const lds_cptr base = L + RING0 + rb;
                HALF(1, base, 0); HALF(0, base, 1); HALF(1, base, 2); HALF(0, base, 3);
                rb = (rb == 2 * RSTG) ? 0 : rb + RSTG;
            }
            BAR_L();
            MM(1, 0, 0); MM(1, 0, 1); MM(1, 1, 0); MM(1, 1, 1); MM(1, 2, 0); MM(1, 2, 1);
            TS(5);
#pragma unroll
            for (int i = 0; i < 3; ++i)
#pragma unroll
                for (int db = 0; db < 2; ++db)
#pragma unroll
                    for (int s = 0; s < 2; ++s) {
                        u32x4 w;
#pragma unroll
                        for (int e = 0; e < 4; ++e) w[e] = cvtpk(acc[i][db][8 * s + 2 * e] * QSCALE, acc[i][db][8 * s + 2 * e + 1] * QSCALE);
                        *(LAS u32x4*)(LW + POFF + (w4 ^ 2) * 12288 + (i * 4 + db * 2 + s) * 1024 + lane * 16) = w;
                        qf[i][db * 2 + s] = __builtin_bit_cast(bf16x8, w);
                    }
            BAR_L();
#pragma unroll
            for (int i = 0; i < 3; ++i)
#pragma unroll
                for (int j = 0; j < 4; ++j) qf[i][4 + j] = ldsr128(L + POFF + w4 * 12288 + (i * 4 + j) * 1024 + lane * 16);
        }
        TS(1);
        const int krow = 32 * kh + r32;
        const int kb0 = 2048 * (krow >> 3) + 64 * (krow & 7) + ((hh ^ ((krow >> 2) & 3)) << 4);
        const int kb1 = 2048 * (krow >> 3) + 64 * (krow & 7) + (((2 + hh) ^ ((krow >> 2) & 3)) << 4);
        const int kown = kh * 1024, koth = (1 - kh) * 1024;
        bf16x8 kf[8];
        unsigned ka[4];
#define KRD(j) asm volatile("ds_read_b128 %0, %1 offset:%2" : "+v"(kf[j]) : "v"(ka[((j) >> 2) * 2 + ((j) & 1)]), "i"(512 * (((j) >> 1) & 1)) : "memory")
#define KADDR(t_) do { const unsigned kb_ = lds0 + kslot(t_); ka[0] = kb_ + kown + kb0; ka[1] = kb_ + kown + kb1; ka[2] = kb_ + koth + kb0; ka[3] = kb_ + koth + kb1; } while (0)
#define KRDALL() do { KRD(0); KRD(1); KRD(2); KRD(3); KRD(4); KRD(5); KRD(6); KRD(7); } while (0)
        f32x16 negm[3];
        float l[3], m[3], lold[3];
#pragma unroll
        for (int i = 0; i < 3; ++i) { negm[i] = f32x16{}; l[i] = 0.f; m[i] = 0.f; lold[i] = 0.f; }
#pragma unroll
        for (int j = 0; j < 8; ++j) kf[j] = bf16x8{};
        auto careful = [&](int t, bool first) __attribute__((always_inline)) {
            const lds_ptr pw = LW + POFF + w4 * 12288 + (t & 1) * 6144 + lane * 16;
            f32x16 sc[3]; float d[3];
#pragma unroll
            for (int i = 0; i < 3; ++i) {
                sc[i] = negm[i];
#pragma unroll
                for (int j = 0; j < 8; ++j) sc[i] = MFMA(kf[j], qf[i][j], sc[i]);
            }
            SBAR();
            if (first) { KADDR(1); KRDALL(); }
#pragma unroll
            for (int i = 0; i < 3; ++i) {
                float pm = sc[i][0];
#pragma unroll
                for (int r = 1; r < 16; ++r) pm = fmaxf(pm, sc[i][r]);
                pm = xhalf_max(pm);
                d[i] = first ? pm : fmaxf(pm, 0.f);
                prop[(w4 * 3 + i) * 32 + r32] = d[i];
            }
            BAR_L();
#pragma unroll
            for (int i = 0; i < 3; ++i) {
                const float dl = fmaxf(d[i], prop[((w4 ^ 2) * 3 + i) * 32 + r32]);
                m[i] += dl;
                float psum = first ? 0.f : lold[i] * __builtin_amdgcn_exp2f(-dl);
#pragma unroll
                for (int r = 0; r < 16; ++r) { negm[i][r] = -m[i]; sc[i][r] = __builtin_amdgcn_exp2f(sc[i][r] - dl); psum += sc[i][r]; }
                l[i] = psum;
#pragma unroll
                for (int s = 0; s < 2; ++s) { u32x4 w;
#pragma unroll
                    for (int e = 0; e < 4; ++e) w[e] = cvtpk(sc[i][8 * s + 2 * e], sc[i][8 * s + 2 * e + 1]);
                    *(LAS u32x4*)(pw + (i * 2 + s) * 1024) = w; }
            }
            if (t == 7) {
#pragma unroll
                for (int i = 0; i < 3; ++i) lsum[(w4 * 3 + i) * 32 + r32] = xhalf_sum(l[i]);
            }
            BAR_L(); SBAR();
        };
        auto redo = [&](int t) __attribute__((always_inline)) {
#pragma unroll
            for (int j = 0; j < 8; ++j) { const int s = (j < 4) ? 4 * kh + j : 4 * (1 - kh) + (j - 4);
                kf[j] = *(const bf16x8*)(Kg + (size_t)(t * 64 + krow) * DH + s * 16 + hh * 8); }
            careful(t, false);
        };
        KADDR(0); KRDALL();
        asm volatile("s_waitcnt lgkmcnt(0)" ::: "memory"); SBAR();
        careful(0, true);
        TS(8);
#define EX2(X, g) do { X[2 * (g)] = __builtin_amdgcn_exp2f(X[2 * (g)]); X[2 * (g) + 1] = __builtin_amdgcn_exp2f(X[2 * (g) + 1]); } while (0)
#define AP(X, g) do { if ((g) == 0) pa = X[0] + X[1]; else { pa += X[2 * (g)]; pa += X[2 * (g) + 1]; } w[(g) & 3] = cvtpk(X[2 * (g)], X[2 * (g) + 1]); PIN(pa); PIN(w); } while (0)
#define PST(i, s) *(LAS u32x4*)(pw + ((i) * 2 + (s)) * 1024) = w
#define SMS(X, i, g) do { EX2(X, g); if ((g) > 0) AP(X, (g) - 1); if ((g) == 4) PST(i, 0); PIN(X); } while (0)
#define SMFIN(X, i) do { AP(X, 7); PST(i, 1); bad |= !(pa <= PSLIM); lold[i] = l[i]; l[i] += pa; PIN(l[i]); } while (0)
#define QK(S, i, j) S = MFMA(kf[j], qf[i][j], (j) ? S : negm[i])
        u32x4 w = {};
        int tf = 8;
#pragma unroll 1
        for (int t = 1; t < 8; ++t) {
            const lds_ptr pw = LW + POFF + w4 * 12288 + (t & 1) * 6144 + lane * 16;
            f32x16 sa, sb; float pa;
            const unsigned flv = flagw[t - 1];
            SBAR();
            QK(sa, 0, 0); QK(sa, 0, 1); QK(sa, 0, 2); QK(sa, 0, 3); QK(sa, 0, 4); QK(sa, 0, 5); QK(sa, 0, 6); QK(sa, 0, 7); SBAR();
            if (__builtin_expect(__builtin_amdgcn_readfirstlane(flv) != 0u, 0)) { tf = t - 1; break; }
            bool bad = false;
            QK(sb, 1, 0); KADDR(t < 7 ? t + 1 : 7); SBAR(); QK(sb, 1, 1); SBAR();
            SMS(sa, 0, 0); SBAR(); QK(sb, 1, 2); SMS(sa, 0, 1); SBAR(); QK(sb, 1, 3); SMS(sa, 0, 2); SBAR(); QK(sb, 1, 4); SMS(sa, 0, 3); SBAR();
            QK(sb, 1, 5); SMS(sa, 0, 4); SBAR(); QK(sb, 1, 6); SMS(sa, 0, 5); SBAR(); QK(sb, 1, 7); SMS(sa, 0, 6); SBAR();
            f32x16 sc;
            QK(sc, 2, 0); SMS(sa, 0, 7); SBAR(); QK(sc, 2, 1); SMFIN(sa, 0); SBAR();
            SMS(sb, 1, 0); SBAR(); QK(sc, 2, 2); SMS(sb, 1, 1); SBAR(); QK(sc, 2, 3); SMS(sb, 1, 2); SBAR(); QK(sc, 2, 4); SMS(sb, 1, 3); SBAR();
            QK(sc, 2, 5); SMS(sb, 1, 4); SBAR(); QK(sc, 2, 6); SMS(sb, 1, 5); SBAR(); QK(sc, 2, 7); SMS(sb, 1, 6); SBAR();
            SMS(sb, 1, 7); KRD(0); SBAR(); SMFIN(sb, 1); KRD(1); SBAR();
            SMS(sc, 2, 0); KRD(2); SBAR(); SMS(sc, 2, 1); KRD(3); SBAR(); SMS(sc, 2, 2); KRD(4); SBAR(); SMS(sc, 2, 3); KRD(5); SBAR();
            SMS(sc, 2, 4); KRD(6); SBAR(); SMS(sc, 2, 5); KRD(7); SBAR(); SMS(sc, 2, 6); SBAR(); SMS(sc, 2, 7); SBAR(); SMFIN(sc, 2); SBAR();
            if (__builtin_expect(__any(bad), 0)) { if (lane == 0) flagw[t] = 1u; }
            if (t == 7) {
#pragma unroll
                for (int i = 0; i < 3; ++i) lsum[(w4 * 3 + i) * 32 + r32] = xhalf_sum(l[i]);
            }
            BAR_L(); SBAR();
            if (t == 1) TS(9); if (t == 3) TS(10); if (t == 5) TS(11);
        }
        if (tf == 8 && __builtin_amdgcn_readfirstlane(flagw[7]) != 0u) tf = 7;
#pragma unroll 1
        for (int t = tf; t < 8; ++t) {
            redo(t);
#pragma unroll
            for (int i = 0; i < 3; ++i) lold[i] = l[i];
        }
        TS(2);
    } else {
        const srd_t srdK = __builtin_amdgcn_make_buffer_rsrc((void*)Kg, (short)0, SK * DH * 2, 0x00020000);
        const srd_t srdV = __builtin_amdgcn_make_buffer_rsrc((void*)Vg, (short)0, SK * DH * 2, 0x00020000);
        const int arow = 8 * (w4 >> 1) + ((lane >> 2) & 7), ach = 4 * (2 * (w4 & 1) + hh) + (p4 ^ ((2 * (w4 >> 1) + blk) & 3));
        const unsigned kvoff = arow * 256 + ach * 16;
        auto kv_unit = [&](bool isV, int t) {
#pragma unroll
            for (int i = 0; i < 4; ++i)
                dma16<KV_NT>(isV ? srdV : srdK, kvoff, (unsigned)(t * 16384 + i * 4096), lds0 + (isV ? vslot(t) : kslot(t)) + (4 * i + w4) * 1024);
        };
        {
            const int rot = (hl * 5 + bl * 10 + 2) / 4 % 10;
            const srd_t srdX = __builtin_amdgcn_make_buffer_rsrc((void*)(x + (size_t)b * SQ * DM), (short)0, SQ * DM * 2, 0x00020000);
            const srd_t srdW = __builtin_amdgcn_make_buffer_rsrc((void*)(wq + h * DH), (short)0, DM * HD * 2 - h * DH * 2, 0x00020000);
            const unsigned xvoff = (8 * w4 + (lane >> 3)) * (DM * 2) + (((lane & 7) ^ ((4 * (w4 & 1) + (lane >> 4)) & 7)) << 4);
            const unsigned wvoff = arow * (HD * 2) + ach * 16;
            auto issue = [&](int s, int buf) __attribute__((always_inline)) {
                int cs = s + rot; if (cs >= 10) cs -= 10;
                const unsigned dst = lds0 + RING0 + buf;
#pragma unroll
                for (int i = 0; i < 4; ++i) dma16<XW_NT>(srdW, wvoff, (unsigned)((cs * 64 + i * 16) * HD * 2), dst + RXB + (4 * i + w4) * 1024);
#pragma unroll
                for (int i = 0; i < 6; ++i) dma16<XW_NT>(srdX, xvoff, (unsigned)(i * 32 * DM * 2 + cs * 128), dst + (4 * i + w4) * 1024);
            };
            if (wid == 4 && lane < 16) flagw[lane] = 0u;
            u32x4 pre[16];
            auto kvpre = [&](int n) __attribute__((always_inline)) {
                if (n < 8) dma16<KV_NT>(srdK, kvoff, (unsigned)(n * 4096), lds0 + (n >> 2) * SLOT + (4 * (n & 3) + w4) * 1024);
                else { const int u = (n - 8) >> 2, i = n & 3;
                       pre[n - 8] = __builtin_amdgcn_raw_buffer_load_b128((u & 1) ? srdK : srdV, kvoff, (unsigned)(((u & 1) ? 2 + (u >> 1) : (u >> 1)) * 16384 + i * 4096), 2); }
            };
            issue(0, 0); issue(1, RSTG);
            WAITV(10); BAR_L(); issue(2, 2 * RSTG); kvpre(0); kvpre(1); kvpre(2);
            WAITV(13); BAR_L(); issue(3, 0); kvpre(3); kvpre(4); kvpre(5);
#pragma unroll
            for (int s = 2; s < 8; ++s) { WAITV(16); BAR_L(); issue(s + 2, ((s + 2) % 3) * RSTG); kvpre(3 * s); kvpre(3 * s + 1); kvpre(3 * s + 2); }
            WAITV(16); BAR_L(); WAITV(3); BAR_L();
            BAR_L();
#pragma unroll
            for (int n = 0; n < 16; ++n) *(LAS u32x4*)(LW + slot_addr(2 + (n >> 2)) + (4 * (n & 3) + w4) * 1024 + lane * 16) = pre[n];
            BAR_L();
        }
        f32x16 o[3][2];
#pragma unroll
        for (int i = 0; i < 3; ++i) { o[i][0] = f32x16{}; o[i][1] = f32x16{}; }
        const int vb0 = 1024 * kh + 64 * (4 * hh + q4) + (((2 * blk + (p4 >> 1)) ^ hh) << 4) + 8 * (p4 & 1);
        const int vb1 = 1024 * kh + 2048 + 64 * (4 * hh + q4) + (((2 * blk + (p4 >> 1)) ^ (2 + hh)) << 4) + 8 * (p4 & 1);
        auto pv = [&](int t, auto dma_tag, srd_t srdB, int ta, int tb, int slotB, bool chk) __attribute__((always_inline)) -> bool {
            constexpr bool DMA = decltype(dma_tag)::value;
            const lds_cptr vb = L + vslot(t);
            const lds_cptr pb = L + POFF + qh * 12288 + (t & 1) * 6144 + lane * 16;
            const unsigned dA = lds0 + vslot(ta) + w4 * 1024, dB = lds0 + slotB + w4 * 1024;
            s16x4 vl[2][2], vh[2][2]; bf16x8 pf[2][3];
#define RDV(buf, ks, db) do { vl[buf][db] = ldstr(vb + vb0 + (ks) * 4096 + (db) * 512); vh[buf][db] = ldstr(vb + vb1 + (ks) * 4096 + (db) * 512); } while (0)
#define RDP(buf, ks, i) pf[buf][i] = ldsr128(pb + ((ks) >> 1) * 24576 + ((i) * 2 + ((ks) & 1)) * 1024)
#define PVM(buf, i, db) o[i][db] = MFMA(cat8(vl[buf][db], vh[buf][db]), pf[buf][i], o[i][db])
#define DM_(n) do { if (DMA) { if ((n) < 4) dma16<KV_NT>(srdV, kvoff, (unsigned)(ta * 16384 + (n) * 4096), dA + (n) * 4096); \
                               else dma16<KV_NT>(srdB, kvoff, (unsigned)(tb * 16384 + ((n) - 4) * 4096), dB + ((n) - 4) * 4096); } } while (0)
#define PVH(cb, ks, more, n) do { \
                PVM(cb, 0, 0); if (more) RDV(cb ^ 1, ks, 0); SBAR(); \
                PVM(cb, 0, 1); if (more) RDP(cb ^ 1, ks, 0); SBAR(); \
                PVM(cb, 1, 0); if (more) RDV(cb ^ 1, ks, 1); SBAR(); \
                PVM(cb, 1, 1); if (more) RDP(cb ^ 1, ks, 1); DM_(n); SBAR(); \
                PVM(cb, 2, 0); if (more) RDP(cb ^ 1, ks, 2); SBAR(); \
                PVM(cb, 2, 1); DM_(n + 1); SBAR(); } while (0)
            const unsigned flv = chk ? flagw[t] : 0u;
            RDV(0, 0, 0); RDP(0, 0, 0); RDV(0, 0, 1); RDP(0, 0, 1); RDP(0, 0, 2); SBAR();
            if (__builtin_expect(__builtin_amdgcn_readfirstlane(flv) != 0u, 0)) return true;
            PVH(0, 1, true, 0); PVH(1, 2, true, 2); PVH(0, 3, true, 4); PVH(1, 0, false, 6);
            return false;
        };
        BAR_L();
        BAR_L();
        int tf = 8;
#pragma unroll 1
        for (int t = 1; t < 6; ++t) {
            if (pv(t - 1, std::true_type{}, t < 5 ? srdK : srdV, t + 1, t < 5 ? t + 3 : 7, t < 5 ? kslot(t + 3) : vslot(7), true)) { tf = t - 1; break; }
            WAITV(8);
            BAR_L();
        }
        if (tf == 8) {
#pragma unroll 1
            for (int t = 6; t < 9; ++t) {
                if (pv(t - 1, std::false_type{}, srdV, 0, 0, 0, true)) { tf = t - 1; break; }
                if (t == 6) WAITV(4); else WAITV(0);
                if (t < 8) BAR_L();
            }
        }
#pragma unroll 1
        for (int t = tf; t < 8; ++t) {
            BAR_L();
#pragma unroll
            for (int i = 0; i < 3; ++i) {
                const float dl = fmaxf(prop[(qh * 3 + i) * 32 + r32], prop[((qh + 2) * 3 + i) * 32 + r32]);
                const float f = __builtin_amdgcn_exp2f(-dl);
#pragma unroll
                for (int r = 0; r < 16; ++r) { o[i][0][r] *= f; o[i][1][r] *= f; }
            }
            WAITV(0);
            BAR_L();
            if (t < 5) kv_unit(true, t + 2);
            if (t == 4) kv_unit(true, 7);
            pv(t, std::false_type{}, srdV, 0, 0, 0, false);
        }
        TS(6);
        float rl[3];
#pragma unroll
        for (int i = 0; i < 3; ++i) rl[i] = 1.0f / (lsum[(qh * 3 + i) * 32 + r32] + lsum[((qh + 2) * 3 + i) * 32 + r32]);
        const srd_t srdO = __builtin_amdgcn_make_buffer_rsrc((void*)(ws + WS_O + (size_t)(b * NH + h) * 49152), (short)0, 49152, 0x00020000);
#pragma unroll
        for (int i = 0; i < 3; ++i)
#pragma unroll
            for (int db = 0; db < 2; ++db)
#pragma unroll
                for (int s = 0; s < 2; ++s) { u32x4 w;
#pragma unroll
                    for (int e = 0; e < 4; ++e) w[e] = cvtpk(o[i][db][8 * s + 2 * e] * rl[i], o[i][db][8 * s + 2 * e + 1] * rl[i]);
                    st16_wt(w, srdO, lane * 16, ((3 * qh + i) * 8 + kh * 4 + db * 2 + s) * 1024); }
        TS(3);
    }
    int ln; asm volatile("v_mbcnt_lo_u32_b32 %0, -1, 0\n\tv_mbcnt_hi_u32_b32 %0, -1, %0" : "=v"(ln));
    const int qg = wid & 3, kh2 = wid >> 2;
    unsigned base = 0, echo_want = 0, echo = 0;
    gu32* const pflag = PFLAG(ln);
    gu32* const pecho = E + PEER(ln) * 32 + (ln < 8 ? hl : 8 + hg);
    if (wid == 4) {
        base = basew[ln < 12 ? ln : 0];
        const unsigned my1 = basew[hl], my2 = basew[8 + hg];
        echo_want = (ln < 8 ? my1 : my2) ^ ECHO_KEY;
        echo = __hip_atomic_load(pecho, RLX_AGENT);
    }
    BAR_L();
    if (wid < 4) {
        const srd_t srdWo = __builtin_amdgcn_make_buffer_rsrc((void*)(wo + (size_t)hg * 1024 * DM + hl * 80), (short)0, 1024 * DM * 2 - hl * 160, 0x00020000);
        unsigned wv[5];
#pragma unroll
        for (int p = 0; p < 5; ++p) { const int o_ = p * 1024 + ln * 16, row = o_ / 160, ch = (o_ % 160) >> 4; wv[p] = row * (DM * 2) + ch * 16; }
#pragma unroll 1
        for (int i = 0; i < 8; ++i) {
            const int gi = 4 * i + w4, st = gi >> 1, hf = gi & 1;
            const int head = (hl + 2 * (st >> 2) + hf) & 7;
#pragma unroll
            for (int p = 0; p < 5; ++p) dma16<false>(srdWo, wv[p], (unsigned)((head * 128 + (st & 3) * 32) * DM * 2), lds0 + gi * 5120 + p * 1024);
        }
    } else WAITV(0);
    BAR_L();
    TS(7);
    if (wid == 4) {
#pragma unroll 1
        for (unsigned sp = 0; __any(ln < 12 && echo != echo_want) && sp < SPIN_CAP; ++sp) {
            __builtin_amdgcn_s_sleep(2);
            echo = __hip_atomic_load(pecho, RLX_AGENT);
        }
        if (ln == hl) __hip_atomic_store(pflag, base + 1u, RLX_AGENT);
#pragma unroll 1
        for (unsigned sp = 0; sp < SPIN_CAP; ++sp) {
            const unsigned v = __hip_atomic_load(pflag, RLX_AGENT);
            if (!__any(ln < 8 && v == base)) break;
            __builtin_amdgcn_s_sleep(1);
        }
    }
    if (wid < 4) WAITV(0);
    BAR_L();
    TS(12);
    {
        const int g = ln >> 4, qq = ln & 15;
        const srd_t srdOg = __builtin_amdgcn_make_buffer_rsrc((void*)(ws + WS_O + (size_t)(b * NH + hg * 8) * 49152), (short)0, 8 * 49152, 0x00020000);
        const unsigned ov = (g >> 1) * 1024 + (g & 1) * 512 + qq * 16;
        u32x4 of[12][3];
#define LDO(st) do { const int head_ = (hl + 2 * ((st) >> 2) + kh2) & 7; _Pragma("unroll") for (int i = 0; i < 3; ++i) { const int t16 = 3 * qg + i; \
                of[(st) % 12][i] = __builtin_amdgcn_raw_buffer_load_b128(srdOg, ov, head_ * 49152 + (t16 >> 1) * 8192 + (t16 & 1) * 256 + ((st) & 3) * 2048, 16); } } while (0)
#pragma unroll
        for (int st = 0; st < 12; ++st) LDO(st);
        f32x4 acc[5][3];
#pragma unroll
        for (int n = 0; n < 5; ++n)
#pragma unroll
            for (int i = 0; i < 3; ++i) acc[n][i] = f32x4{};
        const int wa = kh2 * 5120 + (16 * (g >> 1) + 4 * (g & 1) + (qq >> 2)) * 160 + (ln & 3) * 8;
        s16x4 wl[2][5], wh[2][5];
#define RDWO(buf, st, n) do { wl[buf][n] = ldstr(L + wa + (st) * 10240 + (n) * 32); wh[buf][n] = ldstr(L + wa + (st) * 10240 + 1280 + (n) * 32); } while (0)
#pragma unroll
        for (int n = 0; n < 5; ++n) RDWO(0, 0, n);
#pragma unroll
        for (int st = 0; st < 16; ++st) {
#pragma unroll
            for (int n = 0; n < 5; ++n) {
#pragma unroll
                for (int i = 0; i < 3; ++i) acc[n][i] = MFMA16(cat8(wl[st & 1][n], wh[st & 1][n]), __builtin_bit_cast(bf16x8, of[st % 12][i]), acc[n][i]);
                if (st < 15) RDWO((st + 1) & 1, st + 1, n);
                SBAR();
            }
            if (st < 4) { LDO(st + 12); SBAR(); }
        }
        TS(13);
        BAR_L();
#pragma unroll
        for (int tl = 0; tl < 15; ++tl)
            if ((tl < 8) != (kh2 == 0)) *(LAS f32x4*)(LW + ((qg * 15 + tl) * 64 + ln) * 16) = acc[tl / 3][tl % 3];
        BAR_L();
        const int dst = ((xcd & 4) | qg) + 8 * jj;
        const srd_t srdP = __builtin_amdgcn_make_buffer_rsrc((void*)(ws + WS_PART + ((size_t)dst * 4 + hg) * 15360), (short)0, 15360, 0x00020000);
#pragma unroll
        for (int tl = 0; tl < 15; ++tl)
            if ((tl < 8) == (kh2 == 0)) {
                const f32x4 v = acc[tl / 3][tl % 3] + *(const LAS f32x4*)(L + ((qg * 15 + tl) * 64 + ln) * 16);
                if (qg == hg) *(LAS f32x4*)(LW + ((qg * 15 + tl) * 64 + ln) * 16) = v;
                else st16_wt(__builtin_bit_cast(u32x4, v), srdP, ln * 16, tl * 1024);
            }
        WAITV(0);
        BAR_L();
        if (wid == 4) {
            if (ln == 8 + hg) __hip_atomic_store(pflag, base + 1u, RLX_AGENT);
#pragma unroll 1
            for (unsigned sp = 0; sp < SPIN_CAP; ++sp) {
                const unsigned v = __hip_atomic_load(pflag, RLX_AGENT);
                if (!__any(ln >= 8 && ln < 12 && v == base)) break;
                __builtin_amdgcn_s_sleep(1);
            }
        }
        BAR_L();
        TS(14);
        const srd_t srdI = __builtin_amdgcn_make_buffer_rsrc((void*)(ws + WS_PART + (size_t)blockIdx.x * 4 * 15360), (short)0, 4 * 15360, 0x00020000);
#pragma unroll
        for (int k = 0; k < 2; ++k) {
            const int tl = wid + 8 * k;
            if (tl < 15) {
                f32x4 v = *(const LAS f32x4*)(L + ((hg * 15 + tl) * 64 + ln) * 16);
                f32x4 pv_[3];
#pragma unroll
                for (int j = 0; j < 3; ++j) pv_[j] = __builtin_bit_cast(f32x4, __builtin_amdgcn_raw_buffer_load_b128(srdI, ln * 16, ((hg + 1 + j) & 3) * 15360 + tl * 1024, 16));
                const f32x4 sum = (v + pv_[0]) + (pv_[1] + pv_[2]);
                const int n = tl / 3, i = tl % 3;
                const int q = (3 * hg + i) * 16 + qq, col = hl * 80 + n * 16 + g * 4;
                uint2 w; w.x = cvtpk(sum[0], sum[1]); w.y = cvtpk(sum[2], sum[3]);
                *(uint2*)(out + (size_t)(b * SQ + q) * DM + col) = w;
            }
        }
        TS(15);
    }
}

extern "C" void kernel_launch(void* const* d_in, const int* in_sizes, int n_in, void* d_out, int out_size, void* d_ws, size_t ws_size, hipStream_t stream) {
    const bf16* x = (const bf16*)d_in[0]; const bf16* kc = (const bf16*)d_in[1]; const bf16* vc = (const bf16*)d_in[2];
    const bf16* wq = (const bf16*)d_in[3]; const bf16* wo = (const bf16*)d_in[4];
    (void)hipFuncSetAttribute((const void*)k1_attn, hipFuncAttributeMaxDynamicSharedMemorySize, LDS_BYTES);
    k1_attn<<<256, 512, LDS_BYTES, stream>>>(x, kc, vc, wq, wo, (char*)d_ws, (bf16*)d_out);
}
```

```cpp
#include <hip/hip_runtime.h>
#include <hip/hip_bf16.h>
#include <stdint.h>
#include <type_traits>

typedef __hip_bfloat16 bf16;
using bf16x8 = __attribute__((ext_vector_type(8))) short;
using s16x4  = __attribute__((ext_vector_type(4))) short;
using f32x16 = __attribute__((ext_vector_type(16))) float;
using f32x4  = __attribute__((ext_vector_type(4))) float;
using u32x4  = __attribute__((ext_vector_type(4))) unsigned;
typedef short v4i16_t __attribute__((ext_vector_type(4)));
#define LAS __attribute__((address_space(3)))
typedef LAS const char* lds_cptr;
typedef LAS char* lds_ptr;
typedef __amdgpu_buffer_rsrc_t srd_t;
#define DI __device__ __forceinline__
#define SBAR() __builtin_amdgcn_sched_barrier(0)
#define PIN(x) asm volatile("" : "+v"(x))
#define MFMA(a, b, c) __builtin_amdgcn_mfma_f32_32x32x16_bf16(a, b, c, 0, 0, 0)

constexpr int NB = 8, SQ = 192, DM = 640, NH = 32, SK = 512, DH = 128, HD = NH * DH;
constexpr float QSCALE = 0.08838834764831845f * 1.4426950408889634f;
#define PSLIM 65536.0f

#define TS(i)

typedef float f32x2 __attribute__((ext_vector_type(2)));
typedef __bf16 bf16x2 __attribute__((ext_vector_type(2)));
DI unsigned cvtpk(float lo, float hi) { f32x2 v = {lo, hi}; bf16x2 b = __builtin_convertvector(v, bf16x2); return __builtin_bit_cast(unsigned, b); }
DI float fmul_s(float a, float b) { return a * b; }
template <bool NT>
DI void dma16(srd_t srd, unsigned voff, unsigned soff, unsigned lds_dst_) {
    const unsigned lds_dst = __builtin_amdgcn_readfirstlane(lds_dst_);
    if constexpr (NT) asm volatile("s_mov_b32 m0, %3\n\ts_nop 0\n\tbuffer_load_dwordx4 %0, %1, %2 offen nt lds" :: "v"(voff), "s"(srd), "s"(soff), "s"(lds_dst) : "m0", "memory");
    else              asm volatile("s_mov_b32 m0, %3\n\ts_nop 0\n\tbuffer_load_dwordx4 %0, %1, %2 offen lds" :: "v"(voff), "s"(srd), "s"(soff), "s"(lds_dst) : "m0", "memory");
}
template <bool NT>
DI void touch4(srd_t srd, unsigned voff, unsigned soff, unsigned lds_dst) {
    if constexpr (NT) asm volatile("s_mov_b32 m0, %3\n\ts_nop 0\n\tbuffer_load_dword %0, %1, %2 offen nt lds" :: "v"(voff), "s"(srd), "s"(soff), "s"(lds_dst) : "m0", "memory");
    else              asm volatile("s_mov_b32 m0, %3\n\ts_nop 0\n\tbuffer_load_dword %0, %1, %2 offen lds" :: "v"(voff), "s"(srd), "s"(soff), "s"(lds_dst) : "m0", "memory");
}
DI bf16x8 ldsr128(lds_cptr p) { return *(const LAS bf16x8*)p; }
DI s16x4 ldstr(lds_cptr p) { return __builtin_bit_cast(s16x4, __builtin_amdgcn_ds_read_tr16_b64_v4i16((LAS v4i16_t*)p)); }
DI bf16x8 cat8(s16x4 a, s16x4 b) { return __builtin_shufflevector(a, b, 0, 1, 2, 3, 4, 5, 6, 7); }
#define WAITV(N) asm volatile("s_waitcnt vmcnt(" #N ")" ::: "memory")
#define BAR_L() asm volatile("s_waitcnt lgkmcnt(0)\n\ts_barrier" ::: "memory")
DI float xhalf_max(float v) { auto rr = __builtin_amdgcn_permlane32_swap(__float_as_uint(v), __float_as_uint(v), false, false); return fmaxf(__uint_as_float(rr[0]), __uint_as_float(rr[1])); }
DI float xhalf_sum(float v) { auto rr = __builtin_amdgcn_permlane32_swap(__float_as_uint(v), __float_as_uint(v), false, false); return __uint_as_float(rr[0]) + __uint_as_float(rr[1]); }

#define KV_NT true
#define XW_NT false

#define GAS __attribute__((address_space(1)))
typedef GAS unsigned gu32;
#define RLX_AGENT __ATOMIC_RELAXED, __HIP_MEMORY_SCOPE_AGENT
#define MFMA16(a, b, c) __builtin_amdgcn_mfma_f32_16x16x32_bf16(a, b, c, 0, 0, 0)
DI void st16_wt(u32x4 v, srd_t srd, unsigned voff, unsigned soff) {
    __builtin_amdgcn_raw_buffer_store_b128(v, srd, voff, soff, 16); SBAR(); asm volatile("s_nop 1"); SBAR();
}
constexpr unsigned ECHO_KEY = 0x5A17C3E9u;
constexpr unsigned SPIN_CAP = 1u << 20;
constexpr size_t WS_O = 0, WS_PART = 16u << 20, WS_SYNC = 48u << 20;
constexpr int PAIR_OF_STEP[16] = {8, 9, 0, 1, 2, 4, 5, 6, 7, 3, 10, 11, 12, 13, 14, 15};
constexpr unsigned long long STEP_OF_PAIR = 0xFEDCBA1087659432ull;
#define WO_NT false
#define TOUCH_NT false
#define CODE_TOUCH 30720
#define WRS 2
#define TOUCH_P 99
constexpr int SLOT = 16384;
constexpr int MOFF = 32768;
constexpr int RING0 = 40960, RSTG = 40960, RXB = 24576;
constexpr int POFF = 106496;
constexpr int LDS_BYTES = 163840;
constexpr unsigned KSLOTS = 0x04215310u;
constexpr unsigned VSLOTS = 0x32153042u;
DI int slot_addr(unsigned s) { return s * SLOT + (s >= 2 ? 8192 : 0); }
DI int own_addr(int pr) { return pr < 20 ? POFF + (pr / 5) * 12288 + (pr % 5) * 2048 : 155648 + (pr - 20) * 2048; }
DI int kslot(int t) { return slot_addr((KSLOTS >> (4 * t)) & 15u); }
DI int vslot(int t) { return slot_addr((VSLOTS >> (4 * t)) & 15u); }
#define GROUPB(bl, hl) (((bl) + (hl)) & 1)

__global__ __launch_bounds__(512, 2) void k1_attn(const bf16* __restrict__ x, const bf16* __restrict__ kc, const bf16* __restrict__ vc,
                                                  const bf16* __restrict__ wq, const bf16* __restrict__ wo, char* __restrict__ ws, bf16* __restrict__ out) {
    extern __shared__ __attribute__((aligned(16))) char lds[];
    TS(0);
    unsigned long long pc0; asm volatile("s_getpc_b64 %0" : "=s"(pc0));
    const int tid = threadIdx.x, lane = tid & 63, r32 = lane & 31, hh = lane >> 5;
    const int wid = __builtin_amdgcn_readfirstlane(tid >> 6), w4 = wid & 3;
    const int xcd = blockIdx.x & 7, jj = blockIdx.x >> 3;
    const int bl = jj >> 3, hl = jj & 7;
    const int b = (xcd >> 2) * 4 + bl, h = (xcd & 3) * 8 + hl;
    const unsigned lds0 = (unsigned)(uintptr_t)lds;
    const lds_cptr L = (lds_cptr)lds;
    const lds_ptr LW = (lds_ptr)lds;
    const bf16* Kg = kc + (size_t)(b * NH + h) * SK * DH;
    const bf16* Vg = vc + (size_t)(b * NH + h) * SK * DH;
    const int qh = w4 & 1, kh = w4 >> 1;
    const int q4 = (lane & 15) >> 2, p4 = lane & 3, blk = (lane >> 4) & 1;
    LAS unsigned* flagw = (LAS unsigned*)(LW + MOFF);
    LAS unsigned* basew = (LAS unsigned*)(LW + MOFF + 128);
    const int hg = xcd & 3;
    gu32* const F = (gu32*)(ws + WS_SYNC);
    gu32* const E = (gu32*)(ws + WS_SYNC + 65536);
#define PEER(l_) ((l_) < 8 ? xcd + 8 * (bl * 8 + (l_)) : ((xcd & 4) | ((l_) & 3)) + 8 * jj)
#define PFLAG(l_) (F + (((l_) < 8 ? 0 : 256) + PEER(l_)) * 32)
    LAS float* prop = (LAS float*)(LW + MOFF + 256);
    LAS float* lsum = (LAS float*)(LW + MOFF + 2048);

    const srd_t srdWo = __builtin_amdgcn_make_buffer_rsrc((void*)(wo + (size_t)hg * 1024 * DM + hl * 80), (short)0, 1024 * DM * 2 - hl * 160, 0x00020000);
    auto wo_soff = [&](int g) __attribute__((always_inline)) -> unsigned {
        const int st = (int)((STEP_OF_PAIR >> (4 * (g >> 1))) & 15u);
        return (unsigned)((((hl + (st >> 1)) & 7) * 128 + (2 * (g & 1) + (st & 1)) * 32) * DM * 2);
    };
    auto wo_groups = [&](int g0, int g1, int gs) __attribute__((always_inline)) {
        int l_; asm volatile("v_mbcnt_lo_u32_b32 %0, -1, 0\n\tv_mbcnt_hi_u32_b32 %0, -1, %0" : "=v"(l_));
        unsigned wv[5];
#pragma unroll
        for (int p = 0; p < 5; ++p) { const int o_ = p * 1024 + l_ * 16, row = o_ / 160, ch = (o_ % 160) >> 4; wv[p] = row * (DM * 2) + ch * 16; }
#pragma unroll 1
        for (int g = g0; g < g1; g += gs) {
            const unsigned so = wo_soff(g);
#pragma unroll
            for (int p = 0; p < 5; ++p) dma16<WO_NT>(srdWo, wv[p], so, lds0 + g * 5120 + p * 1024);
        }
    };
    bool slow = false;
    u32x4 wr[25];
    auto late_group = [&](int k) __attribute__((always_inline)) -> int { const int j = 4 * k + w4; return j < 6 ? 10 + j : j < 8 ? j : 12 + j; };

    auto wr_load = [&](int k0, int k1) __attribute__((always_inline)) {
#pragma unroll
        for (int k = k0; k < k1; ++k) { const unsigned so = wo_soff(late_group(k));
#pragma unroll
            for (int p = 0; p < 5; ++p) { const int o_ = p * 1024 + lane * 16, row = o_ / 160, ch = (o_ % 160) >> 4;
                wr[k * 5 + p] = __builtin_amdgcn_raw_buffer_load_b128(srdWo, row * (DM * 2) + ch * 16, so, 0); } }
    };

    if (wid < 4) {
        if (wid == 0) {
            if (lane < 12) {
                const unsigned base = __hip_atomic_load(PFLAG(lane), RLX_AGENT);
                basew[lane] = base;
                __hip_atomic_store(E + blockIdx.x * 32 + lane, base ^ ECHO_KEY, RLX_AGENT);
            }
        }
        if (wid == 1) {
            const srd_t srdC = __builtin_amdgcn_make_buffer_rsrc((void*)(pc0 & ~127ull), (short)0, CODE_TOUCH, 0x00020000);
#pragma unroll
            for (int k = 0; k < 4; ++k) touch4<false>(srdC, lane * 128, (unsigned)(k * 8192), lds0 + MOFF + 4096u);
        }
        bf16x8 qf[3][8];
        {
            f32x16 acc[3][2];
#pragma unroll
            for (int i = 0; i < 3; ++i) { acc[i][0] = f32x16{}; acc[i][1] = f32x16{}; }
            const int xsw = (r32 >> 1) & 7;
            int xa[4];
#pragma unroll
            for (int ks = 0; ks < 4; ++ks) xa[ks] = (96 * qh + r32) * 128 + (((2 * ks + hh) ^ xsw) << 4);
            const int ps = ((p4 & 1) << 1) | (p4 >> 1);
            const int wa0 = RXB + 2048 * hh + 1024 * kh + 64 * q4 + (((2 * blk + (ps >> 1)) ^ (2 * hh)) << 4) + 8 * (ps & 1);
            const int wa1 = RXB + 2048 * hh + 1024 * kh + 64 * q4 + 256 + (((2 * blk + (ps >> 1)) ^ (2 * hh + 1)) << 4) + 8 * (ps & 1);
            bf16x8 xf[2][3]; s16x4 wl[2][2], wh[2][2];
#define RDX(buf, base, ks, i) xf[buf][i] = ldsr128(base + xa[ks] + i * 4096)
#define RDW(buf, base, ks, db) do { wl[buf][db] = ldstr(base + wa0 + ks * 4096 + db * 512); wh[buf][db] = ldstr(base + wa1 + ks * 4096 + db * 512); } while (0)
#define MM(buf, i, db) acc[i][db] = MFMA(cat8(wl[buf][db], wh[buf][db]), xf[buf][i], acc[i][db])
#define HALF(cb, base, ks) do { \
                MM(cb, 0, 0); RDW(cb ^ 1, base, ks, 0); SBAR(); \
                MM(cb, 0, 1); RDX(cb ^ 1, base, ks, 0); SBAR(); \
                MM(cb, 1, 0); RDW(cb ^ 1, base, ks, 1); SBAR(); \
                MM(cb, 1, 1); RDX(cb ^ 1, base, ks, 1); SBAR(); \
                MM(cb, 2, 0); RDX(cb ^ 1, base, ks, 2); SBAR(); \
                MM(cb, 2, 1); SBAR(); } while (0)
            BAR_L();
            TS(4);
            { const lds_cptr base = L + RING0;
              RDW(0, base, 0, 0); RDX(0, base, 0, 0); RDW(0, base, 0, 1); RDX(0, base, 0, 1); RDX(0, base, 0, 2); SBAR();
              HALF(0, base, 1); HALF(1, base, 2); HALF(0, base, 3); }
            int rb = RSTG;
#pragma unroll 1
            for (int s = 1; s < 10; ++s) {
                SBAR(); BAR_L(); SBAR();
                const lds_cptr base = L + RING0 + rb;
                HALF(1, base, 0); HALF(0, base, 1); HALF(1, base, 2); HALF(0, base, 3);
                rb = (rb == 2 * RSTG) ? 0 : rb + RSTG;
            }
            BAR_L();
            MM(1, 0, 0); MM(1, 0, 1); MM(1, 1, 0); MM(1, 1, 1); MM(1, 2, 0); MM(1, 2, 1);
            TS(5);
#pragma unroll
            for (int i = 0; i < 3; ++i)
#pragma unroll
                for (int db = 0; db < 2; ++db)
#pragma unroll
                    for (int s = 0; s < 2; ++s) {
                        u32x4 w;
#pragma unroll
                        for (int e = 0; e < 4; ++e) w[e] = cvtpk(acc[i][db][8 * s + 2 * e] * QSCALE, acc[i][db][8 * s + 2 * e + 1] * QSCALE);
                        *(LAS u32x4*)(LW + POFF + (w4 ^ 2) * 12288 + (i * 4 + db * 2 + s) * 1024 + lane * 16) = w;
                        qf[i][db * 2 + s] = __builtin_bit_cast(bf16x8, w);
                    }
            BAR_L();
#pragma unroll
            for (int i = 0; i < 3; ++i)
#pragma unroll
                for (int j = 0; j < 4; ++j) qf[i][4 + j] = ldsr128(L + POFF + w4 * 12288 + (i * 4 + j) * 1024 + lane * 16);
        }
        TS(1);
        const int krow = 32 * kh + r32;
        const int kb0 = 2048 * (krow >> 3) + 64 * (krow & 7) + ((hh ^ ((krow >> 2) & 3)) << 4);
        const int kb1 = 2048 * (krow >> 3) + 64 * (krow & 7) + (((2 + hh) ^ ((krow >> 2) & 3)) << 4);
        const int kown = kh * 1024, koth = (1 - kh) * 1024;
        bf16x8 kf[8];
        unsigned ka[4];
#define KRD(j) asm volatile("ds_read_b128 %0, %1 offset:%2" : "+v"(kf[j]) : "v"(ka[((j) >> 2) * 2 + ((j) & 1)]), "i"(512 * (((j) >> 1) & 1)) : "memory")
#define KADDR(t_) do { const unsigned kb_ = lds0 + kslot(t_); ka[0] = kb_ + kown + kb0; ka[1] = kb_ + kown + kb1; ka[2] = kb_ + koth + kb0; ka[3] = kb_ + koth + kb1; } while (0)
#define KRDALL() do { KRD(0); KRD(1); KRD(2); KRD(3); KRD(4); KRD(5); KRD(6); KRD(7); } while (0)
        f32x16 negm[3], sc;
        float l[3], m[3], lold[3];
#pragma unroll
        for (int j = 0; j < 8; ++j) kf[j] = bf16x8{};
        KADDR(0); KRDALL();
        asm volatile("s_waitcnt lgkmcnt(0)" ::: "memory"); SBAR();
        {
            const lds_ptr pw = LW + POFF + w4 * 12288 + lane * 16;
            f32x16 s0[3]; float d[3];
#pragma unroll
            for (int i = 0; i < 3; ++i) {
                s0[i] = f32x16{};
#pragma unroll
                for (int j = 0; j < 8; ++j) s0[i] = MFMA(kf[j], qf[i][j], s0[i]);
            }
            SBAR();
            KADDR(1); KRDALL();
#pragma unroll
            for (int i = 0; i < 3; ++i) {
                float pm = s0[i][0];
#pragma unroll
                for (int r = 1; r < 16; ++r) pm = fmaxf(pm, s0[i][r]);
                d[i] = xhalf_max(pm);
                prop[(w4 * 3 + i) * 32 + r32] = d[i];
            }
            BAR_L();
#pragma unroll
            for (int i = 0; i < 3; ++i) {
                m[i] = fmaxf(d[i], prop[((w4 ^ 2) * 3 + i) * 32 + r32]);
                float psum = 0.f;
#pragma unroll
                for (int r = 0; r < 16; ++r) { negm[i][r] = -m[i]; s0[i][r] -= m[i]; }
                if (i < 2) {
#pragma unroll
                    for (int r = 0; r < 16; ++r) { s0[i][r] = __builtin_amdgcn_exp2f(s0[i][r]); psum += s0[i][r]; }
#pragma unroll
                    for (int s = 0; s < 2; ++s) { u32x4 w;
#pragma unroll
                        for (int e = 0; e < 4; ++e) w[e] = cvtpk(s0[i][8 * s + 2 * e], s0[i][8 * s + 2 * e + 1]);
                        *(LAS u32x4*)(pw + (i * 2 + s) * 1024) = w; }
                }
                l[i] = psum; lold[i] = psum;
            }
            sc = s0[2];
            BAR_L(); SBAR();
        }
        TS(8);
#define EX2(X, g) do { X[2 * (g)] = __builtin_amdgcn_exp2f(X[2 * (g)]); X[2 * (g) + 1] = __builtin_amdgcn_exp2f(X[2 * (g) + 1]); } while (0)
#define AP(X, g) do { if ((g) == 0) pa = X[0] + X[1]; else { pa += X[2 * (g)]; pa += X[2 * (g) + 1]; } w[(g) & 3] = cvtpk(X[2 * (g)], X[2 * (g) + 1]); PIN(pa); PIN(w); } while (0)
#define PST(PW, i, s) *(LAS u32x4*)(PW + ((i) * 2 + (s)) * 1024) = w
#define SMS(X, PW, i, g) do { EX2(X, g); if ((g) > 0) AP(X, (g) - 1); if ((g) == 4) PST(PW, i, 0); PIN(X); } while (0)
#define SMFIN(X, PW, i) do { AP(X, 7); PST(PW, i, 1); bad |= !(pa <= PSLIM); lold[i] = l[i]; l[i] += pa; PIN(l[i]); } while (0)
#define QK(S, i, j) S = MFMA(kf[j], qf[i][j], (j) ? S : negm[i])
        u32x4 w = {};
        int tf = 9;
#pragma unroll 1
        for (int t = 1; t < 8; ++t) {
            const lds_ptr pw = LW + POFF + w4 * 12288 + (t & 1) * 6144 + lane * 16;
            const lds_ptr pc = LW + POFF + w4 * 12288 + ((t & 1) ^ 1) * 6144 + lane * 16;
            f32x16 sa, sb; float pa; bool bad = false;
            const unsigned flv = flagw[t - 1];
            SBAR();
            QK(sa, 0, 0); SMS(sc, pc, 2, 0); SBAR(); QK(sa, 0, 1); SMS(sc, pc, 2, 1); SBAR(); QK(sa, 0, 2); SMS(sc, pc, 2, 2); SBAR();
            if (__builtin_expect(__builtin_amdgcn_readfirstlane(flv) != 0u, 0)) { tf = t - 1; break; }
            QK(sa, 0, 3); SMS(sc, pc, 2, 3); SBAR(); QK(sa, 0, 4); SMS(sc, pc, 2, 4); SBAR(); QK(sa, 0, 5); SMS(sc, pc, 2, 5); SBAR();
            QK(sa, 0, 6); SMS(sc, pc, 2, 6); SBAR(); QK(sa, 0, 7); SMS(sc, pc, 2, 7); SBAR();
            QK(sb, 1, 0); SMFIN(sc, pc, 2); KADDR(t < 7 ? t + 1 : 7); SBAR();
            QK(sb, 1, 1); SBAR(); SMS(sa, pw, 0, 0); SBAR(); QK(sb, 1, 2); SMS(sa, pw, 0, 1); SBAR(); QK(sb, 1, 3); SMS(sa, pw, 0, 2); SBAR();
            QK(sb, 1, 4); SMS(sa, pw, 0, 3); SBAR(); QK(sb, 1, 5); SMS(sa, pw, 0, 4); SBAR(); QK(sb, 1, 6); SMS(sa, pw, 0, 5); SBAR(); QK(sb, 1, 7); SMS(sa, pw, 0, 6); SBAR();
            QK(sc, 2, 0); SBAR(); KRD(0); SMS(sa, pw, 0, 7); SBAR(); QK(sc, 2, 1); SBAR(); KRD(1); SMFIN(sa, pw, 0); SBAR(); SMS(sb, pw, 1, 0); SBAR();
            QK(sc, 2, 2); SBAR(); KRD(2); SMS(sb, pw, 1, 1); SBAR(); QK(sc, 2, 3); SBAR(); KRD(3); SMS(sb, pw, 1, 2); SBAR();
            QK(sc, 2, 4); SBAR(); KRD(4); SMS(sb, pw, 1, 3); SBAR(); QK(sc, 2, 5); SBAR(); KRD(5); SMS(sb, pw, 1, 4); SBAR();
            QK(sc, 2, 6); SBAR(); KRD(6); SMS(sb, pw, 1, 5); SBAR(); QK(sc, 2, 7); SBAR(); KRD(7); SMS(sb, pw, 1, 6); SBAR();
            SMS(sb, pw, 1, 7); SBAR(); SMFIN(sb, pw, 1); SBAR();
            if (__builtin_expect(__any(bad), 0)) { if (lane == 0) flagw[t] = 1u; }
            if (t == 7) { lsum[(w4 * 3 + 0) * 32 + r32] = xhalf_sum(l[0]); lsum[(w4 * 3 + 1) * 32 + r32] = xhalf_sum(l[1]); }
            BAR_L(); SBAR();
            if (t == 1) TS(9); if (t == 3) TS(10); if (t == 5) TS(11);
        }
        if (tf == 9) {
            if (__builtin_amdgcn_readfirstlane(flagw[7]) != 0u) tf = 7;
            else {
                const lds_ptr pc = LW + POFF + w4 * 12288 + 6144 + lane * 16;
                float pa; bool bad = false;
                lold[0] = l[0]; lold[1] = l[1];
                SMS(sc, pc, 2, 0); SMS(sc, pc, 2, 1); SMS(sc, pc, 2, 2); SMS(sc, pc, 2, 3); SMS(sc, pc, 2, 4); SMS(sc, pc, 2, 5); SMS(sc, pc, 2, 6); SMS(sc, pc, 2, 7); SMFIN(sc, pc, 2);
                if (__builtin_expect(__any(bad), 0)) { if (lane == 0) flagw[8] = 1u; }
                lsum[(w4 * 3 + 2) * 32 + r32] = xhalf_sum(l[2]);
#pragma unroll
                for (int j = 0; j < 8; ++j) { qf[0][j] = qf[2][j]; qf[1][j] = qf[2][j]; }
                SBAR();
                wr_load(0, WRS);
                BAR_L();
                if (__builtin_amdgcn_readfirstlane(flagw[8]) != 0u) tf = 8;
            }
        }
        slow = tf < 9;
        if (__builtin_expect(slow, 0)) {
#pragma unroll
            for (int i = 0; i < 3; ++i) l[i] = lold[i];
            BAR_L();
#pragma unroll 1
            for (int t = tf - 1; t < 8; ++t) {
                const bool all = t >= tf;
                const lds_ptr pw = LW + POFF + w4 * 12288 + (t & 1) * 6144 + lane * 16;
#pragma unroll
                for (int j = 0; j < 8; ++j) { const int s = (j < 4) ? 4 * kh + j : 4 * (1 - kh) + (j - 4);
                    kf[j] = *(const bf16x8*)(Kg + (size_t)(t * 64 + krow) * DH + s * 16 + hh * 8); }
                f32x16 s0[3]; float d[3];
#pragma unroll
                for (int i = 0; i < 3; ++i) {
                    s0[i] = f32x16{};
#pragma unroll
                    for (int j = 0; j < 8; ++j) s0[i] = MFMA(kf[j], qf[i][j], s0[i]);
                    float pm = s0[i][0];
#pragma unroll
                    for (int r = 1; r < 16; ++r) pm = fmaxf(pm, s0[i][r]);
                    d[i] = (all || i == 2) ? fmaxf(xhalf_max(pm) - m[i], 0.f) : 0.f;
                    prop[(w4 * 3 + i) * 32 + r32] = d[i];
                }
                BAR_L();
#pragma unroll
                for (int i = 0; i < 3; ++i) {
                    const float dl = fmaxf(d[i], prop[((w4 ^ 2) * 3 + i) * 32 + r32]);
                    m[i] += dl;
                    float psum = l[i] * __builtin_amdgcn_exp2f(-dl);
#pragma unroll
                    for (int r = 0; r < 16; ++r) { s0[i][r] = (all || i == 2) ? __builtin_amdgcn_exp2f(s0[i][r] - m[i]) : 0.f; psum += s0[i][r]; }
                    l[i] = psum;
#pragma unroll
                    for (int s = 0; s < 2; ++s) { u32x4 w2;
#pragma unroll
                        for (int e = 0; e < 4; ++e) w2[e] = cvtpk(s0[i][8 * s + 2 * e], s0[i][8 * s + 2 * e + 1]);
                        *(LAS u32x4*)(pw + (i * 2 + s) * 1024) = w2; }
                    lsum[(w4 * 3 + i) * 32 + r32] = xhalf_sum(psum);
                }
                BAR_L();
            }
            BAR_L();
            wr_load(0, WRS);
        }
        wr_load(WRS, 5);
        TS(2);
    } else {
        const srd_t srdK = __builtin_amdgcn_make_buffer_rsrc((void*)Kg, (short)0, SK * DH * 2, 0x00020000);
        const srd_t srdV = __builtin_amdgcn_make_buffer_rsrc((void*)Vg, (short)0, SK * DH * 2, 0x00020000);
        const int arow = 8 * (w4 >> 1) + ((lane >> 2) & 7), ach = 4 * (2 * (w4 & 1) + hh) + (p4 ^ ((2 * (w4 >> 1) + blk) & 3));
        const unsigned kvoff = arow * 256 + ach * 16;
        auto kv_unit = [&](bool isV, int t) {
#pragma unroll
            for (int i = 0; i < 4; ++i)
                dma16<KV_NT>(isV ? srdV : srdK, kvoff, (unsigned)(t * 16384 + i * 4096), lds0 + (isV ? vslot(t) : kslot(t)) + (4 * i + w4) * 1024);
        };
        {
            const int rot = (hl * 5 + bl * 10 + 2) / 4 % 10;
            const srd_t srdX = __builtin_amdgcn_make_buffer_rsrc((void*)(x + (size_t)b * SQ * DM), (short)0, SQ * DM * 2, 0x00020000);
            const srd_t srdW = __builtin_amdgcn_make_buffer_rsrc((void*)(wq + h * DH), (short)0, DM * HD * 2 - h * DH * 2, 0x00020000);
            const unsigned xvoff = (8 * w4 + (lane >> 3)) * (DM * 2) + (((lane & 7) ^ ((4 * (w4 & 1) + (lane >> 4)) & 7)) << 4);
            const unsigned wvoff = arow * (HD * 2) + ach * 16;
            auto issue = [&](int s, int buf) __attribute__((always_inline)) {
                int cs = s + rot; if (cs >= 10) cs -= 10;
                const unsigned dst = lds0 + RING0 + buf;
#pragma unroll
                for (int i = 0; i < 4; ++i) dma16<XW_NT>(srdW, wvoff, (unsigned)((cs * 64 + i * 16) * HD * 2), dst + RXB + (4 * i + w4) * 1024);
#pragma unroll
                for (int i = 0; i < 6; ++i) dma16<XW_NT>(srdX, xvoff, (unsigned)(i * 32 * DM * 2 + cs * 128), dst + (4 * i + w4) * 1024);
            };
            if (wid == 4 && lane < 16) flagw[lane] = 0u;
            u32x4 pre[16];
            auto kvpre = [&](int n) __attribute__((always_inline)) {
                if (n < 8) dma16<KV_NT>(srdK, kvoff, (unsigned)(n * 4096), lds0 + (n >> 2) * SLOT + (4 * (n & 3) + w4) * 1024);
                else { const int u = (n - 8) >> 2, i = n & 3;
                       pre[n - 8] = __builtin_amdgcn_raw_buffer_load_b128((u & 1) ? srdK : srdV, kvoff, (unsigned)(((u & 1) ? 2 + (u >> 1) : (u >> 1)) * 16384 + i * 4096), 2); }
            };
            issue(0, 0); issue(1, RSTG);
            WAITV(10); BAR_L(); issue(2, 2 * RSTG); kvpre(0); kvpre(1); kvpre(2);
            WAITV(13); BAR_L(); issue(3, 0); kvpre(3); kvpre(4); kvpre(5);
#pragma unroll
            for (int s = 2; s < 8; ++s) { WAITV(16); BAR_L(); issue(s + 2, ((s + 2) % 3) * RSTG); kvpre(3 * s); kvpre(3 * s + 1); kvpre(3 * s + 2); }
            WAITV(16); BAR_L(); WAITV(3); BAR_L();
            BAR_L();
#pragma unroll
            for (int n = 0; n < 16; ++n) *(LAS u32x4*)(LW + slot_addr(2 + (n >> 2)) + (4 * (n & 3) + w4) * 1024 + lane * 16) = pre[n];
            BAR_L();
        }
        f32x16 o[3][2];
#pragma unroll
        for (int i = 0; i < 3; ++i) { o[i][0] = f32x16{}; o[i][1] = f32x16{}; }
        const int vb0 = 1024 * kh + 64 * (4 * hh + q4) + (((2 * blk + (p4 >> 1)) ^ hh) << 4) + 8 * (p4 & 1);
        const int vb1 = 1024 * kh + 2048 + 64 * (4 * hh + q4) + (((2 * blk + (p4 >> 1)) ^ (2 + hh)) << 4) + 8 * (p4 & 1);
        const lds_cptr pb0 = L + POFF + qh * 12288 + lane * 16;
        s16x4 vl[2][4][2], vh[2][4][2];
        bf16x8 pf[2][3];
        unsigned wv[5];
#pragma unroll
        for (int p = 0; p < 5; ++p) { const int o_ = p * 1024 + lane * 16, row = o_ / 160, ch = (o_ % 160) >> 4; wv[p] = row * (DM * 2) + ch * 16; }
        const unsigned tvoff = r32 * (DM * 2) + hh * 156;
        const srd_t srdO = __builtin_amdgcn_make_buffer_rsrc((void*)(ws + WS_O + (size_t)(b * NH + h) * 49152), (short)0, 49152, 0x00020000);
        u32x4 ow[3][4];
        auto opack = [&](int i) __attribute__((always_inline)) {
            const float rl = 1.0f / (lsum[(qh * 3 + i) * 32 + r32] + lsum[((qh + 2) * 3 + i) * 32 + r32]);
#pragma unroll
            for (int db = 0; db < 2; ++db)
#pragma unroll
                for (int s = 0; s < 2; ++s) {
#pragma unroll
                    for (int e = 0; e < 4; ++e) ow[i][db * 2 + s][e] = cvtpk(o[i][db][8 * s + 2 * e] * rl, o[i][db][8 * s + 2 * e + 1] * rl);
                    st16_wt(ow[i][db * 2 + s], srdO, lane * 16, ((3 * qh + i) * 8 + kh * 4 + db * 2 + s) * 1024); }
        };
        auto olds = [&](int i) __attribute__((always_inline)) {
#pragma unroll
            for (int db = 0; db < 2; ++db) { const lds_ptr p_ = LW + own_addr((3 * qh + i) * 4 + kh * 2 + db) + lane * 16;
                *(LAS u32x4*)p_ = ow[i][db * 2]; *(LAS u32x4*)(p_ + 1024) = ow[i][db * 2 + 1]; }
        };
#define RDV(X, vb, ks, db) do { vl[X][ks][db] = ldstr(vb + vb0 + (ks) * 4096 + (db) * 512); vh[X][ks][db] = ldstr(vb + vb1 + (ks) * 4096 + (db) * 512); } while (0)
#define RDP(buf, blk_, ks, i) pf[buf][i] = ldsr128(pb0 + ((blk_) & 1) * 6144 + ((ks) >> 1) * 24576 + ((i) * 2 + ((ks) & 1)) * 1024)
#define PVM(X, buf, ks, i, db) o[i][db] = MFMA(cat8(vl[X][ks][db], vh[X][ks][db]), pf[buf][i], o[i][db])
        auto period = [&](auto tag) __attribute__((always_inline)) -> bool {
            constexpr int P = decltype(tag)::value, X = P & 1;
            constexpr bool AB = P <= 8, C = P >= 2;
            const lds_cptr vb = L + vslot(AB ? P - 1 : 0);
            auto dm = [&](int n) __attribute__((always_inline)) {
                if constexpr (P <= 5) {
                    if (n < 4) dma16<KV_NT>(srdV, kvoff, (unsigned)((P + 1) * 16384 + n * 4096), lds0 + vslot(P + 1) + (4 * n + w4) * 1024);
                    else if constexpr (P < 5) dma16<KV_NT>(srdK, kvoff, (unsigned)((P + 3) * 16384 + (n - 4) * 4096), lds0 + kslot(P + 3) + (4 * (n - 4) + w4) * 1024);
                    else dma16<KV_NT>(srdV, kvoff, (unsigned)(7 * 16384 + (n - 4) * 4096), lds0 + vslot(7) + (4 * (n - 4) + w4) * 1024);
                } else if constexpr (P <= 8) {
                    const int g = (P == 6) ? 16 + w4 : (P == 7) ? w4 : (w4 < 2 ? 4 + w4 : 6 + w4);
                    if (n < 5) dma16<WO_NT>(srdWo, wv[n], wo_soff(g), lds0 + g * 5120 + n * 1024);
                }
            };
            auto rd = [&](int buf, int ks, int part) __attribute__((always_inline)) {
                if (part == 0) { if (C) RDP(buf, P - 2, ks, 2); }
                if (part == 1) { if (AB) RDV(X, vb, ks, 0); }
                if (part == 2) { if (AB) RDP(buf, P - 1, ks, 0); }
                if (part == 3) { if (AB) RDV(X, vb, ks, 1); }
                if (part == 4) { if (AB) RDP(buf, P - 1, ks, 1); }
            };
            auto touch = [&](int n) __attribute__((always_inline)) {
                if constexpr (P == TOUCH_P) touch4<TOUCH_NT>(srdWo, tvoff, (unsigned)((bl * 256 + w4 * 64 + n * 32) * DM * 2), lds0 + 155648u);
            };
            const unsigned flv = flagw[P - 1];
            rd(0, 0, 0); rd(0, 0, 1); rd(0, 0, 2); rd(0, 0, 3); rd(0, 0, 4); SBAR();
            if (__builtin_expect(__builtin_amdgcn_readfirstlane(flv) != 0u, 0)) return true;
            if constexpr (P == 9) { olds(0); olds(1); }
#pragma unroll
            for (int ks = 0; ks < 4; ++ks) {
                const int cb = ks & 1; const bool more = ks < 3;
                if (C) PVM(X ^ 1, cb, ks, 2, 0); if (more) rd(cb ^ 1, ks + 1, 0); SBAR();
                if (C) PVM(X ^ 1, cb, ks, 2, 1); if (more) rd(cb ^ 1, ks + 1, 1); SBAR();
                if (AB) PVM(X, cb, ks, 0, 0); if (more) rd(cb ^ 1, ks + 1, 2); SBAR();
                if (AB) PVM(X, cb, ks, 0, 1); if (more) rd(cb ^ 1, ks + 1, 3); dm(2 * ks); SBAR();
                if (AB) PVM(X, cb, ks, 1, 0); if (more) rd(cb ^ 1, ks + 1, 4); if (ks >= 2) touch(ks - 2); SBAR();
                if (AB) PVM(X, cb, ks, 1, 1); dm(2 * ks + 1); SBAR();
            }
            {
                constexpr int T0 = (P == TOUCH_P) ? 2 : 0, T1 = (P - 1 == TOUCH_P) ? 2 : 0;
                constexpr int N = P <= 5 ? 8 + T0 : P == 6 ? 9 + T0 : 10 + T0 + T1;
                if constexpr (P <= 8) asm volatile("s_waitcnt vmcnt(%0)" :: "i"(N) : "memory");
            }
            if constexpr (P == 8) { opack(0); opack(1); }
            if constexpr (P <= 8) BAR_L();
            return false;
        };
        BAR_L();
        BAR_L();
        int tf = 9;
#define PERIOD(P) if (tf == 9) { if (period(std::integral_constant<int, P>{})) tf = P - 1; }
        PERIOD(1) PERIOD(2) PERIOD(3) PERIOD(4) PERIOD(5) PERIOD(6) PERIOD(7) PERIOD(8) PERIOD(9)
        slow = tf < 9;
        if (__builtin_expect(slow, 0)) {
            WAITV(0); BAR_L();
#pragma unroll 1
            for (int t = tf - 1; t < 8; ++t) {
                const int sl = slot_addr(2 + (t & 1));
#pragma unroll
                for (int n = 0; n < 4; ++n) dma16<KV_NT>(srdV, kvoff, (unsigned)(t * 16384 + n * 4096), lds0 + sl + (4 * n + w4) * 1024);
                BAR_L();
#pragma unroll
                for (int i = 0; i < 3; ++i) {
                    const float dl = fmaxf(prop[(qh * 3 + i) * 32 + r32], prop[((qh + 2) * 3 + i) * 32 + r32]);
                    const float f = __builtin_amdgcn_exp2f(-dl);
#pragma unroll
                    for (int r = 0; r < 16; ++r) { o[i][0][r] *= f; o[i][1][r] *= f; }
                }
                WAITV(0);
                BAR_L();
                const lds_cptr vb = L + sl;
#pragma unroll
                for (int ks = 0; ks < 4; ++ks) {
                    RDV(0, vb, ks, 0); RDV(0, vb, ks, 1); RDP(0, t, ks, 0); RDP(0, t, ks, 1); RDP(0, t, ks, 2);
#pragma unroll
                    for (int i = 0; i < 3; ++i) { PVM(0, 0, ks, i, 0); PVM(0, 0, ks, i, 1); }
                }
            }
            BAR_L();
            opack(0); opack(1); olds(0); olds(1);
        }
        TS(6);
        opack(2); olds(2);
        TS(3);
    }
    int ln; asm volatile("v_mbcnt_lo_u32_b32 %0, -1, 0\n\tv_mbcnt_hi_u32_b32 %0, -1, %0" : "=v"(ln));
    const int qg = wid & 3, kh2 = wid >> 2;
    unsigned base = 0, echo_want = 0, echo = 0;
    gu32* const pflag = PFLAG(ln);
    gu32* const pecho = E + PEER(ln) * 32 + (ln < 8 ? hl : 8 + hg);
    if (wid == 4) {
        base = basew[ln < 12 ? ln : 0];
        const unsigned my1 = basew[hl], my2 = basew[8 + hg];
        echo_want = (ln < 8 ? my1 : my2) ^ ECHO_KEY;
        echo = __hip_atomic_load(pecho, RLX_AGENT);
    }
    TS(16);
    BAR_L(); TS(17);
    if (__builtin_expect(slow, 0)) { if (wid < 4) wo_groups(w4, 20, 4); WAITV(0); BAR_L(); }
    {
        const int g = ln >> 4, qq = ln & 15;
        const srd_t srdOg = __builtin_amdgcn_make_buffer_rsrc((void*)(ws + WS_O + (size_t)(b * NH + hg * 8) * 49152), (short)0, 8 * 49152, 0x00020000);
        const unsigned ov0 = (g >> 1) * 1024 + (g & 1) * 512 + qq * 16, ov = ov0 + kh2 * 4096;
        constexpr int OW = 6;
        u32x4 of[OW][3];
#define LDO1(st, i) do { const int head_ = (hl + ((st) >> 1)) & 7, t16 = 3 * qg + (i); \
                of[(st) % OW][i] = __builtin_amdgcn_raw_buffer_load_b128(srdOg, ov, head_ * 49152 + (t16 >> 1) * 8192 + (t16 & 1) * 256 + ((st) & 1) * 2048, 16); } while (0)
#define LDO(st) do { LDO1(st, 0); LDO1(st, 1); LDO1(st, 2); } while (0)
#pragma unroll
        for (int st = 0; st < 2; ++st)
#pragma unroll
            for (int i = 0; i < 3; ++i) { const int t16 = 3 * qg + i;
                of[st][i] = *(const LAS u32x4*)(L + own_addr((t16 >> 1) * 4 + kh2 * 2 + st) + (t16 & 1) * 256 + ov0); }
        f32x4 acc[5][3];
#pragma unroll
        for (int n = 0; n < 5; ++n)
#pragma unroll
            for (int i = 0; i < 3; ++i) acc[n][i] = f32x4{};
        lds_cptr wb[3];
        wb[0] = L + kh2 * 5120 + (16 * (g >> 1) + 4 * (g & 1) + (qq >> 2)) * 160 + (ln & 3) * 8;
        wb[1] = wb[0] + 61440; wb[2] = wb[0] + 122880;
        PIN(wb[1]); PIN(wb[2]);
        s16x4 wl[2][5], wh[2][5];
#define RDWO(buf, st, n) do { const lds_cptr p_ = wb[PAIR_OF_STEP[st] / 6] + (PAIR_OF_STEP[st] % 6) * 10240 + (n) * 32; wl[buf][n] = ldstr(p_); wh[buf][n] = ldstr(p_ + 1280); } while (0)
#pragma unroll
        for (int n = 0; n < 5; ++n) RDWO(0, 0, n);
#pragma unroll
        for (int st = 0; st < 16; ++st) {
            if (st == 1) {
                TS(18); WAITV(0); TS(19);
                BAR_L();
                TS(7);
                if (wid == 4) {
#pragma unroll 1
                    for (unsigned sp = 0; __any(ln < 12 && echo != echo_want) && sp < SPIN_CAP; ++sp) {
                        __builtin_amdgcn_s_sleep(2);
                        echo = __hip_atomic_load(pecho, RLX_AGENT);
                    }
                    if (ln == hl) __hip_atomic_store(pflag, base + 1u, RLX_AGENT);
                } else if (wid == 5) {
#pragma unroll
                    for (int k = 0; k < 6; ++k) touch4<false>(srdOg, ln * 128, (unsigned)(hl * 49152 + k * 8192), lds0 + 81920u);
                } else if (wid < 4) {
#pragma unroll
                    for (int k = 0; k < 5; ++k)
#pragma unroll
                        for (int p = 0; p < 5; ++p) *(LAS u32x4*)(LW + late_group(k) * 5120 + p * 1024 + ln * 16) = wr[k * 5 + p];
                }
                SBAR(); TS(20);
            }
            if (st == 2) {
                TS(21);
                if (wid == 4) {
#pragma unroll 1
                    for (unsigned sp = 0; sp < SPIN_CAP; ++sp) {
                        const unsigned v = __hip_atomic_load(pflag, RLX_AGENT);
                        if (!__any(ln < 8 && v == base)) break;
                        __builtin_amdgcn_s_sleep(1);
                    }
                }
                TS(22); WAITV(0); TS(23);
                BAR_L();
                TS(12);
                if (kh2) __builtin_amdgcn_s_setprio(1);
#pragma unroll
                for (int s2 = 2; s2 < 2 + OW - 1; ++s2) LDO(s2);
                SBAR();
            }
#pragma unroll
            for (int n = 0; n < 5; ++n) {
#pragma unroll
                for (int i = 0; i < 3; ++i) acc[n][i] = MFMA16(cat8(wl[st & 1][n], wh[st & 1][n]), __builtin_bit_cast(bf16x8, of[st % OW][i]), acc[n][i]);
                if (st < 15) RDWO((st + 1) & 1, st + 1, n);
                if (st >= 2 && st + OW - 1 < 16 && n < 3) LDO1(st + OW - 1, n);
                SBAR();
            }
            if (st == 3) TS(24);
            if (st == 10) { TS(25); BAR_L(); TS(26); }
            if (0) BAR_L();
        }
        TS(13);
#pragma unroll
        for (int tl = 0; tl < 15; ++tl)
            if ((tl < 8) != (kh2 == 0)) *(LAS f32x4*)(LW + ((qg * 15 + tl) * 64 + ln) * 16) = acc[tl / 3][tl % 3];
        BAR_L(); TS(27);
        const int dst = ((xcd & 4) | qg) + 8 * jj;
        const srd_t srdP = __builtin_amdgcn_make_buffer_rsrc((void*)(ws + WS_PART + (size_t)dst * 24576 + ((hg - qg - 1) & 3) * 8192), (short)0, 8192, 0x00020000);
#pragma unroll
        for (int u = 0; u < 8; ++u)
            if ((u < 4) == (kh2 == 0)) {
                u32x4 w = {};
#pragma unroll
                for (int k = 0; k < 2; ++k) { const int tl = 2 * u + k;
                    if (tl < 15) {
                        const f32x4 v = acc[tl / 3][tl % 3] + *(const LAS f32x4*)(L + ((qg * 15 + tl) * 64 + ln) * 16);
                        if (qg == hg) *(LAS f32x4*)(LW + ((qg * 15 + tl) * 64 + ln) * 16) = v;
                        w[2 * k] = cvtpk(v[0], v[1]); w[2 * k + 1] = cvtpk(v[2], v[3]);
                    } }
                if (qg != hg) st16_wt(w, srdP, ln * 16, u * 1024);
            }
        TS(28);
        WAITV(0);
        BAR_L();
        TS(14);
        if (wid == 4) {
            if (ln == 8 + hg) __hip_atomic_store(pflag, base + 1u, RLX_AGENT);
#pragma unroll 1
            for (unsigned sp = 0; sp < SPIN_CAP; ++sp) {
                const unsigned v = __hip_atomic_load(pflag, RLX_AGENT);
                if (!__any(ln >= 8 && ln < 12 && v == base)) break;
                __builtin_amdgcn_s_sleep(1);
            }
        }
        BAR_L();
        TS(29);
        const srd_t srdI = __builtin_amdgcn_make_buffer_rsrc((void*)(ws + WS_PART + (size_t)blockIdx.x * 24576), (short)0, 24576, 0x00020000);
        u32x4 pv_[3];
#pragma unroll
        for (int j = 0; j < 3; ++j) pv_[j] = __builtin_amdgcn_raw_buffer_load_b128(srdI, ln * 16, j * 8192 + wid * 1024, 16);
#pragma unroll
        for (int k = 0; k < 2; ++k) {
            const int tl = 2 * wid + k;
            if (tl < 15) {
                f32x4 sum = *(const LAS f32x4*)(L + ((hg * 15 + tl) * 64 + ln) * 16);
#pragma unroll
                for (int j = 0; j < 3; ++j) {
                    sum[0] += __uint_as_float(pv_[j][2 * k] << 16); sum[1] += __uint_as_float(pv_[j][2 * k] & 0xffff0000u);
                    sum[2] += __uint_as_float(pv_[j][2 * k + 1] << 16); sum[3] += __uint_as_float(pv_[j][2 * k + 1] & 0xffff0000u);
                }
                const int n = tl / 3, i = tl % 3;
                const int q = (3 * hg + i) * 16 + qq, col = hl * 80 + n * 16 + g * 4;
                uint2 w; w.x = cvtpk(sum[0], sum[1]); w.y = cvtpk(sum[2], sum[3]);
                *(uint2*)(out + (size_t)(b * SQ + q) * DM + col) = w;
            }
        }
        TS(15);
    }
}

extern "C" void kernel_launch(void* const* d_in, const int* in_sizes, int n_in, void* d_out, int out_size, void* d_ws, size_t ws_size, hipStream_t stream) {
    const bf16* x = (const bf16*)d_in[0]; const bf16* kc = (const bf16*)d_in[1]; const bf16* vc = (const bf16*)d_in[2];
    const bf16* wq = (const bf16*)d_in[3]; const bf16* wo = (const bf16*)d_in[4];
    (void)hipFuncSetAttribute((const void*)k1_attn, hipFuncAttributeMaxDynamicSharedMemorySize, LDS_BYTES);
    k1_attn<<<256, 512, LDS_BYTES, stream>>>(x, kc, vc, wq, wo, (char*)d_ws, (bf16*)d_out);
}
```

```cpp
#include <hip/hip_runtime.h>
#include <hip/hip_bf16.h>
#include <stdint.h>
#include <type_traits>

typedef __hip_bfloat16 bf16;
using bf16x8 = __attribute__((ext_vector_type(8))) short;
using s16x4  = __attribute__((ext_vector_type(4))) short;
using f32x16 = __attribute__((ext_vector_type(16))) float;
using f32x4  = __attribute__((ext_vector_type(4))) float;
using u32x4  = __attribute__((ext_vector_type(4))) unsigned;
typedef short v4i16_t __attribute__((ext_vector_type(4)));
#define LAS __attribute__((address_space(3)))
typedef LAS const char* lds_cptr;
typedef LAS char* lds_ptr;
typedef __amdgpu_buffer_rsrc_t srd_t;
#define DI __device__ __forceinline__
#define SBAR() __builtin_amdgcn_sched_barrier(0)
#define PIN(x) asm volatile("" : "+v"(x))
#define MFMA(a, b, c) __builtin_amdgcn_mfma_f32_32x32x16_bf16(a, b, c, 0, 0, 0)

constexpr int NB = 8, SQ = 192, DM = 640, NH = 32, SK = 512, DH = 128, HD = NH * DH;
constexpr float QSCALE = 0.08838834764831845f * 1.4426950408889634f;
#define PSLIM 65536.0f

#define TS(i)

typedef float f32x2 __attribute__((ext_vector_type(2)));
typedef __bf16 bf16x2 __attribute__((ext_vector_type(2)));
DI unsigned cvtpk(float lo, float hi) { f32x2 v = {lo, hi}; bf16x2 b = __builtin_convertvector(v, bf16x2); return __builtin_bit_cast(unsigned, b); }
DI float fmul_s(float a, float b) { return a * b; }
template <bool NT>
DI void dma16(srd_t srd, unsigned voff, unsigned soff, unsigned lds_dst_) {
    const unsigned lds_dst = __builtin_amdgcn_readfirstlane(lds_dst_);
    if constexpr (NT) asm volatile("s_mov_b32 m0, %3\n\ts_nop 0\n\tbuffer_load_dwordx4 %0, %1, %2 offen nt lds" :: "v"(voff), "s"(srd), "s"(soff), "s"(lds_dst) : "m0", "memory");
    else              asm volatile("s_mov_b32 m0, %3\n\ts_nop 0\n\tbuffer_load_dwordx4 %0, %1, %2 offen lds" :: "v"(voff), "s"(srd), "s"(soff), "s"(lds_dst) : "m0", "memory");
}
template <bool NT>
DI void touch4(srd_t srd, unsigned voff, unsigned soff, unsigned lds_dst) {
    if constexpr (NT) asm volatile("s_mov_b32 m0, %3\n\ts_nop 0\n\tbuffer_load_dword %0, %1, %2 offen nt lds" :: "v"(voff), "s"(srd), "s"(soff), "s"(lds_dst) : "m0", "memory");
    else              asm volatile("s_mov_b32 m0, %3\n\ts_nop 0\n\tbuffer_load_dword %0, %1, %2 offen lds" :: "v"(voff), "s"(srd), "s"(soff), "s"(lds_dst) : "m0", "memory");
}
DI bf16x8 ldsr128(lds_cptr p) { return *(const LAS bf16x8*)p; }
DI s16x4 ldstr(lds_cptr p) { return __builtin_bit_cast(s16x4, __builtin_amdgcn_ds_read_tr16_b64_v4i16((LAS v4i16_t*)p)); }
DI bf16x8 cat8(s16x4 a, s16x4 b) { return __builtin_shufflevector(a, b, 0, 1, 2, 3, 4, 5, 6, 7); }
#define WAITV(N) asm volatile("s_waitcnt vmcnt(" #N ")" ::: "memory")
#define BAR_L() asm volatile("s_waitcnt lgkmcnt(0)\n\ts_barrier" ::: "memory")
DI float xhalf_max(float v) { auto rr = __builtin_amdgcn_permlane32_swap(__float_as_uint(v), __float_as_uint(v), false, false); return fmaxf(__uint_as_float(rr[0]), __uint_as_float(rr[1])); }
DI float xhalf_sum(float v) { auto rr = __builtin_amdgcn_permlane32_swap(__float_as_uint(v), __float_as_uint(v), false, false); return __uint_as_float(rr[0]) + __uint_as_float(rr[1]); }

#define KV_NT true
#define XW_NT false

#define GAS __attribute__((address_space(1)))
typedef GAS unsigned gu32;
#define RLX_AGENT __ATOMIC_RELAXED, __HIP_MEMORY_SCOPE_AGENT
#define MFMA16(a, b, c) __builtin_amdgcn_mfma_f32_16x16x32_bf16(a, b, c, 0, 0, 0)
DI void st16_wt(u32x4 v, srd_t srd, unsigned voff, unsigned soff) {
    __builtin_amdgcn_raw_buffer_store_b128(v, srd, voff, soff, 16); SBAR(); asm volatile("s_nop 1"); SBAR();
}
constexpr unsigned ECHO_KEY = 0x5A17C3E9u;
constexpr unsigned SPIN_CAP = 1u << 20;
constexpr size_t WS_O = 0, WS_PART = 16u << 20, WS_SYNC = 48u << 20;
constexpr int PAIR_OF_STEP[16] = {8, 9, 0, 1, 2, 4, 5, 6, 7, 3, 10, 11, 12, 13, 14, 15};
constexpr unsigned long long STEP_OF_PAIR = 0xFEDCBA1087659432ull;
#define WO_NT false
#define TOUCH_NT false
#define CODE_TOUCH 30720
#define TLB_TOUCH
#define WRS 2
#define TOUCH_P 99
constexpr int SLOT = 16384;
constexpr int MOFF = 32768;
constexpr int RING0 = 40960, RSTG = 40960, RXB = 24576;
constexpr int POFF = 106496;
constexpr int LDS_BYTES = 163840;
constexpr unsigned KSLOTS = 0x04215310u;
constexpr unsigned VSLOTS = 0x32153042u;
DI int slot_addr(unsigned s) { return s * SLOT + (s >= 2 ? 8192 : 0); }
DI int own_addr(int pr) { return pr < 20 ? POFF + (pr / 5) * 12288 + (pr % 5) * 2048 : 155648 + (pr - 20) * 2048; }
DI int kslot(int t) { return slot_addr((KSLOTS >> (4 * t)) & 15u); }
DI int vslot(int t) { return slot_addr((VSLOTS >> (4 * t)) & 15u); }
#define GROUPB(bl, hl) (((bl) + (hl)) & 1)

__global__ __launch_bounds__(512, 2) void k1_attn(const bf16* __restrict__ x, const bf16* __restrict__ kc, const bf16* __restrict__ vc,
                                                  const bf16* __restrict__ wq, const bf16* __restrict__ wo, char* __restrict__ ws, bf16* __restrict__ out) {
    extern __shared__ __attribute__((aligned(16))) char lds[];
    TS(0);
    unsigned long long pc0; asm volatile("s_getpc_b64 %0" : "=s"(pc0));
    const int tid = threadIdx.x, lane = tid & 63, r32 = lane & 31, hh = lane >> 5;
    const int wid = __builtin_amdgcn_readfirstlane(tid >> 6), w4 = wid & 3;
    const int xcd = blockIdx.x & 7, jj = blockIdx.x >> 3;
    const int bl = jj >> 3, hl = jj & 7;
    const int b = (xcd >> 2) * 4 + bl, h = (xcd & 3) * 8 + hl;
    const unsigned lds0 = (unsigned)(uintptr_t)lds;
    const lds_cptr L = (lds_cptr)lds;
    const lds_ptr LW = (lds_ptr)lds;
    const bf16* Kg = kc + (size_t)(b * NH + h) * SK * DH;
    const bf16* Vg = vc + (size_t)(b * NH + h) * SK * DH;
    const int qh = w4 & 1, kh = w4 >> 1;
    const int q4 = (lane & 15) >> 2, p4 = lane & 3, blk = (lane >> 4) & 1;
    LAS unsigned* flagw = (LAS unsigned*)(LW + MOFF);
    LAS unsigned* basew = (LAS unsigned*)(LW + MOFF + 128);
    const int hg = xcd & 3;
    gu32* const F = (gu32*)(ws + WS_SYNC);
    gu32* const E = (gu32*)(ws + WS_SYNC + 65536);
#define PEER(l_) ((l_) < 8 ? xcd + 8 * (bl * 8 + (l_)) : ((xcd & 4) | ((l_) & 3)) + 8 * jj)
#define PFLAG(l_) (F + (((l_) < 8 ? 0 : 256) + PEER(l_)) * 32)
    LAS float* prop = (LAS float*)(LW + MOFF + 256);
    LAS float* lsum = (LAS float*)(LW + MOFF + 2048);

    const srd_t srdWo = __builtin_amdgcn_make_buffer_rsrc((void*)(wo + (size_t)hg * 1024 * DM + hl * 80), (short)0, 1024 * DM * 2 - hl * 160, 0x00020000);
    auto wo_soff = [&](int g) __attribute__((always_inline)) -> unsigned {
        const int st = (int)((STEP_OF_PAIR >> (4 * (g >> 1))) & 15u);
        return (unsigned)((((hl + (st >> 1)) & 7) * 128 + (2 * (g & 1) + (st & 1)) * 32) * DM * 2);
    };
    auto wo_groups = [&](int g0, int g1, int gs) __attribute__((always_inline)) {
        int l_; asm volatile("v_mbcnt_lo_u32_b32 %0, -1, 0\n\tv_mbcnt_hi_u32_b32 %0, -1, %0" : "=v"(l_));
        unsigned wv[5];
#pragma unroll
        for (int p = 0; p < 5; ++p) { const int o_ = p * 1024 + l_ * 16, row = o_ / 160, ch = (o_ % 160) >> 4; wv[p] = row * (DM * 2) + ch * 16; }
#pragma unroll 1
        for (int g = g0; g < g1; g += gs) {
            const unsigned so = wo_soff(g);
#pragma unroll
            for (int p = 0; p < 5; ++p) dma16<WO_NT>(srdWo, wv[p], so, lds0 + g * 5120 + p * 1024);
        }
    };
    bool slow = false;
    u32x4 wr[25];
    auto late_group = [&](int k) __attribute__((always_inline)) -> int { const int j = 4 * k + w4; return j < 6 ? 10 + j : j < 8 ? j : 12 + j; };

    auto wr_load = [&](int k0, int k1) __attribute__((always_inline)) {
#pragma unroll
        for (int k = k0; k < k1; ++k) { const unsigned so = wo_soff(late_group(k));
#pragma unroll
            for (int p = 0; p < 5; ++p) { const int o_ = p * 1024 + lane * 16, row = o_ / 160, ch = (o_ % 160) >> 4;
                wr[k * 5 + p] = __builtin_amdgcn_raw_buffer_load_b128(srdWo, row * (DM * 2) + ch * 16, so, 0); } }
    };

    if (wid < 4) {
        if (wid == 0) {
            if (lane < 12) {
                const unsigned base = __hip_atomic_load(PFLAG(lane), RLX_AGENT);
                basew[lane] = base;
                __hip_atomic_store(E + blockIdx.x * 32 + lane, base ^ ECHO_KEY, RLX_AGENT);
            }
        }
        if (wid == 1) {
            const srd_t srdC = __builtin_amdgcn_make_buffer_rsrc((void*)(pc0 & ~127ull), (short)0, CODE_TOUCH, 0x00020000);
#pragma unroll
            for (int k = 0; k < 4; ++k) touch4<false>(srdC, lane * 128, (unsigned)(k * 8192), lds0 + MOFF + 4096u);
        }
        if (wid == 2) {
            const char* p = (const char*)Kg;
            if (lane == 1) p = (const char*)Vg;
            if (lane == 2) p = (const char*)Kg + SK * DH * 2 - 4;
            if (lane == 3) p = (const char*)Vg + SK * DH * 2 - 4;
            if (lane == 4) p = ws + WS_O + (size_t)(b * NH + h) * 49152;
            if (lane == 5) p = ws + WS_O + (size_t)(b * NH + h) * 49152 + 49148;
            if (lane >= 8 && lane < 12) p = ws + WS_PART + (size_t)PEER(lane) * 24576 + (((hg - lane - 1) & 3) == 3 ? 0 : ((hg - lane - 1) & 3) * 8192 + 7168 + 1020);
            if (lane >= 16 && lane < 36) p = (const char*)(wo + (size_t)hg * 1024 * DM + hl * 80) + (size_t)(lane - 16) * 65536;
            if (lane == 36) p = (const char*)(out + (size_t)(b * SQ + 48 * hg) * DM + hl * 80);
            if (lane == 37) p = (const char*)(out + (size_t)(b * SQ + 48 * hg + 47) * DM + hl * 80);
            if (lane == 8 + hg) p = (const char*)Kg;
            unsigned v = *(const volatile unsigned*)p;
            asm volatile("" :: "v"(v));
        }
        bf16x8 qf[3][8];
        {
            f32x16 acc[3][2];
#pragma unroll
            for (int i = 0; i < 3; ++i) { acc[i][0] = f32x16{}; acc[i][1] = f32x16{}; }
            const int xsw = (r32 >> 1) & 7;
            int xa[4];
#pragma unroll
            for (int ks = 0; ks < 4; ++ks) xa[ks] = (96 * qh + r32) * 128 + (((2 * ks + hh) ^ xsw) << 4);
            const int ps = ((p4 & 1) << 1) | (p4 >> 1);
            const int wa0 = RXB + 2048 * hh + 1024 * kh + 64 * q4 + (((2 * blk + (ps >> 1)) ^ (2 * hh)) << 4) + 8 * (ps & 1);
            const int wa1 = RXB + 2048 * hh + 1024 * kh + 64 * q4 + 256 + (((2 * blk + (ps >> 1)) ^ (2 * hh + 1)) << 4) + 8 * (ps & 1);
            bf16x8 xf[2][3]; s16x4 wl[2][2], wh[2][2];
#define RDX(buf, base, ks, i) xf[buf][i] = ldsr128(base + xa[ks] + i * 4096)
#define RDW(buf, base, ks, db) do { wl[buf][db] = ldstr(base + wa0 + ks * 4096 + db * 512); wh[buf][db] = ldstr(base + wa1 + ks * 4096 + db * 512); } while (0)
#define MM(buf, i, db) acc[i][db] = MFMA(cat8(wl[buf][db], wh[buf][db]), xf[buf][i], acc[i][db])
#define HALF(cb, base, ks) do { \
                MM(cb, 0, 0); RDW(cb ^ 1, base, ks, 0); SBAR(); \
                MM(cb, 0, 1); RDX(cb ^ 1, base, ks, 0); SBAR(); \
                MM(cb, 1, 0); RDW(cb ^ 1, base, ks, 1); SBAR(); \
                MM(cb, 1, 1); RDX(cb ^ 1, base, ks, 1); SBAR(); \
                MM(cb, 2, 0); RDX(cb ^ 1, base, ks, 2); SBAR(); \
                MM(cb, 2, 1); SBAR(); } while (0)
            BAR_L();
            TS(4);
            { const lds_cptr base = L + RING0;
              RDW(0, base, 0, 0); RDX(0, base, 0, 0); RDW(0, base, 0, 1); RDX(0, base, 0, 1); RDX(0, base, 0, 2); SBAR();
              HALF(0, base, 1); HALF(1, base, 2); HALF(0, base, 3); }
            int rb = RSTG;
#pragma unroll 1
            for (int s = 1; s < 10; ++s) {
                SBAR(); BAR_L(); SBAR();
                const lds_cptr base = L + RING0 + rb;
                HALF(1, base, 0); HALF(0, base, 1); HALF(1, base, 2); HALF(0, base, 3);
                rb = (rb == 2 * RSTG) ? 0 : rb + RSTG;
            }
            BAR_L();
            MM(1, 0, 0); MM(1, 0, 1); MM(1, 1, 0); MM(1, 1, 1); MM(1, 2, 0); MM(1, 2, 1);
            TS(5);
#pragma unroll
            for (int i = 0; i < 3; ++i)
#pragma unroll
                for (int db = 0; db < 2; ++db)
#pragma unroll
                    for (int s = 0; s < 2; ++s) {
                        u32x4 w;
#pragma unroll
                        for (int e = 0; e < 4; ++e) w[e] = cvtpk(acc[i][db][8 * s + 2 * e] * QSCALE, acc[i][db][8 * s + 2 * e + 1] * QSCALE);
                        *(LAS u32x4*)(LW + POFF + (w4 ^ 2) * 12288 + (i * 4 + db * 2 + s) * 1024 + lane * 16) = w;
                        qf[i][db * 2 + s] = __builtin_bit_cast(bf16x8, w);
                    }
            BAR_L();
#pragma unroll
            for (int i = 0; i < 3; ++i)
#pragma unroll
                for (int j = 0; j < 4; ++j) qf[i][4 + j] = ldsr128(L + POFF + w4 * 12288 + (i * 4 + j) * 1024 + lane * 16);
        }
        TS(1);
        const int krow = 32 * kh + r32;
        const int kb0 = 2048 * (krow >> 3) + 64 * (krow & 7) + ((hh ^ ((krow >> 2) & 3)) << 4);
        const int kb1 = 2048 * (krow >> 3) + 64 * (krow & 7) + (((2 + hh) ^ ((krow >> 2) & 3)) << 4);
        const int kown = kh * 1024, koth = (1 - kh) * 1024;
        bf16x8 kf[8];
        unsigned ka[4];
#define KRD(j) asm volatile("ds_read_b128 %0, %1 offset:%2" : "+v"(kf[j]) : "v"(ka[((j) >> 2) * 2 + ((j) & 1)]), "i"(512 * (((j) >> 1) & 1)) : "memory")
#define KADDR(t_) do { const unsigned kb_ = lds0 + kslot(t_); ka[0] = kb_ + kown + kb0; ka[1] = kb_ + kown + kb1; ka[2] = kb_ + koth + kb0; ka[3] = kb_ + koth + kb1; } while (0)
#define KRDALL() do { KRD(0); KRD(1); KRD(2); KRD(3); KRD(4); KRD(5); KRD(6); KRD(7); } while (0)
        f32x16 negm[3], sc;
        float l[3], m[3], lold[3];
#pragma unroll
        for (int j = 0; j < 8; ++j) kf[j] = bf16x8{};
        KADDR(0); KRDALL();
        asm volatile("s_waitcnt lgkmcnt(0)" ::: "memory"); SBAR();
        {
            const lds_ptr pw = LW + POFF + w4 * 12288 + lane * 16;
            f32x16 s0[3]; float d[3];
#pragma unroll
            for (int i = 0; i < 3; ++i) {
                s0[i] = f32x16{};
#pragma unroll
                for (int j = 0; j < 8; ++j) s0[i] = MFMA(kf[j], qf[i][j], s0[i]);
            }
            SBAR();
            KADDR(1); KRDALL();
#pragma unroll
            for (int i = 0; i < 3; ++i) {
                float pm = s0[i][0];
#pragma unroll
                for (int r = 1; r < 16; ++r) pm = fmaxf(pm, s0[i][r]);
                d[i] = xhalf_max(pm);
                prop[(w4 * 3 + i) * 32 + r32] = d[i];
            }
            BAR_L();
#pragma unroll
            for (int i = 0; i < 3; ++i) {
                m[i] = fmaxf(d[i], prop[((w4 ^ 2) * 3 + i) * 32 + r32]);
                float psum = 0.f;
#pragma unroll
                for (int r = 0; r < 16; ++r) { negm[i][r] = -m[i]; s0[i][r] -= m[i]; }
                if (i < 2) {
#pragma unroll
                    for (int r = 0; r < 16; ++r) { s0[i][r] = __builtin_amdgcn_exp2f(s0[i][r]); psum += s0[i][r]; }
#pragma unroll
                    for (int s = 0; s < 2; ++s) { u32x4 w;
#pragma unroll
                        for (int e = 0; e < 4; ++e) w[e] = cvtpk(s0[i][8 * s + 2 * e], s0[i][8 * s + 2 * e + 1]);
                        *(LAS u32x4*)(pw + (i * 2 + s) * 1024) = w; }
                }
                l[i] = psum; lold[i] = psum;
            }
            sc = s0[2];
            BAR_L(); SBAR();
        }
        TS(8);
#define EX2(X, g) do { X[2 * (g)] = __builtin_amdgcn_exp2f(X[2 * (g)]); X[2 * (g) + 1] = __builtin_amdgcn_exp2f(X[2 * (g) + 1]); } while (0)
#define AP(X, g) do { if ((g) == 0) pa = X[0] + X[1]; else { pa += X[2 * (g)]; pa += X[2 * (g) + 1]; } w[(g) & 3] = cvtpk(X[2 * (g)], X[2 * (g) + 1]); PIN(pa); PIN(w); } while (0)
#define PST(PW, i, s) *(LAS u32x4*)(PW + ((i) * 2 + (s)) * 1024) = w
#define SMS(X, PW, i, g) do { EX2(X, g); if ((g) > 0) AP(X, (g) - 1); if ((g) == 4) PST(PW, i, 0); PIN(X); } while (0)
#define SMFIN(X, PW, i) do { AP(X, 7); PST(PW, i, 1); bad |= !(pa <= PSLIM); lold[i] = l[i]; l[i] += pa; PIN(l[i]); } while (0)
#define QK(S, i, j) S = MFMA(kf[j], qf[i][j], (j) ? S : negm[i])
        u32x4 w = {};
        int tf = 9;
#pragma unroll 1
        for (int t = 1; t < 8; ++t) {
            const lds_ptr pw = LW + POFF + w4 * 12288 + (t & 1) * 6144 + lane * 16;
            const lds_ptr pc = LW + POFF + w4 * 12288 + ((t & 1) ^ 1) * 6144 + lane * 16;
            f32x16 sa, sb; float pa; bool bad = false;
            const unsigned flv = flagw[t - 1];
            SBAR();
            QK(sa, 0, 0); SMS(sc, pc, 2, 0); SBAR(); QK(sa, 0, 1); SMS(sc, pc, 2, 1); SBAR(); QK(sa, 0, 2); SMS(sc, pc, 2, 2); SBAR();
            if (__builtin_expect(__builtin_amdgcn_readfirstlane(flv) != 0u, 0)) { tf = t - 1; break; }
            QK(sa, 0, 3); SMS(sc, pc, 2, 3); SBAR(); QK(sa, 0, 4); SMS(sc, pc, 2, 4); SBAR(); QK(sa, 0, 5); SMS(sc, pc, 2, 5); SBAR();
            QK(sa, 0, 6); SMS(sc, pc, 2, 6); SBAR(); QK(sa, 0, 7); SMS(sc, pc, 2, 7); SBAR();
            QK(sb, 1, 0); SMFIN(sc, pc, 2); KADDR(t < 7 ? t + 1 : 7); SBAR();
            QK(sb, 1, 1); SBAR(); SMS(sa, pw, 0, 0); SBAR(); QK(sb, 1, 2); SMS(sa, pw, 0, 1); SBAR(); QK(sb, 1, 3); SMS(sa, pw, 0, 2); SBAR();
            QK(sb, 1, 4); SMS(sa, pw, 0, 3); SBAR(); QK(sb, 1, 5); SMS(sa, pw, 0, 4); SBAR(); QK(sb, 1, 6); SMS(sa, pw, 0, 5); SBAR(); QK(sb, 1, 7); SMS(sa, pw, 0, 6); SBAR();
            QK(sc, 2, 0); SBAR(); KRD(0); SMS(sa, pw, 0, 7); SBAR(); QK(sc, 2, 1); SBAR(); KRD(1); SMFIN(sa, pw, 0); SBAR(); SMS(sb, pw, 1, 0); SBAR();
            QK(sc, 2, 2); SBAR(); KRD(2); SMS(sb, pw, 1, 1); SBAR(); QK(sc, 2, 3); SBAR(); KRD(3); SMS(sb, pw, 1, 2); SBAR();
            QK(sc, 2, 4); SBAR(); KRD(4); SMS(sb, pw, 1, 3); SBAR(); QK(sc, 2, 5); SBAR(); KRD(5); SMS(sb, pw, 1, 4); SBAR();
            QK(sc, 2, 6); SBAR(); KRD(6); SMS(sb, pw, 1, 5); SBAR(); QK(sc, 2, 7); SBAR(); KRD(7); SMS(sb, pw, 1, 6); SBAR();
            SMS(sb, pw, 1, 7); SBAR(); SMFIN(sb, pw, 1); SBAR();
            if (__builtin_expect(__any(bad), 0)) { if (lane == 0) flagw[t] = 1u; }
            if (t == 7) { lsum[(w4 * 3 + 0) * 32 + r32] = xhalf_sum(l[0]); lsum[(w4 * 3 + 1) * 32 + r32] = xhalf_sum(l[1]); }
            BAR_L(); SBAR();
            if (t == 1) TS(9); if (t == 3) TS(10); if (t == 5) TS(11);
        }
        if (tf == 9) {
            if (__builtin_amdgcn_readfirstlane(flagw[7]) != 0u) tf = 7;
            else {
                const lds_ptr pc = LW + POFF + w4 * 12288 + 6144 + lane * 16;
                float pa; bool bad = false;
                lold[0] = l[0]; lold[1] = l[1];
                SMS(sc, pc, 2, 0); SMS(sc, pc, 2, 1); SMS(sc, pc, 2, 2); SMS(sc, pc, 2, 3); SMS(sc, pc, 2, 4); SMS(sc, pc, 2, 5); SMS(sc, pc, 2, 6); SMS(sc, pc, 2, 7); SMFIN(sc, pc, 2);
                if (__builtin_expect(__any(bad), 0)) { if (lane == 0) flagw[8] = 1u; }
                lsum[(w4 * 3 + 2) * 32 + r32] = xhalf_sum(l[2]);
#pragma unroll
                for (int j = 0; j < 8; ++j) { qf[0][j] = qf[2][j]; qf[1][j] = qf[2][j]; }
                SBAR();
                wr_load(0, WRS);
                BAR_L();
                if (__builtin_amdgcn_readfirstlane(flagw[8]) != 0u) tf = 8;
            }
        }
        slow = tf < 9;
        if (__builtin_expect(slow, 0)) {
#pragma unroll
            for (int i = 0; i < 3; ++i) l[i] = lold[i];
            BAR_L();
#pragma unroll 1
            for (int t = tf - 1; t < 8; ++t) {
                const bool all = t >= tf;
                const lds_ptr pw = LW + POFF + w4 * 12288 + (t & 1) * 6144 + lane * 16;
#pragma unroll
                for (int j = 0; j < 8; ++j) { const int s = (j < 4) ? 4 * kh + j : 4 * (1 - kh) + (j - 4);
                    kf[j] = *(const bf16x8*)(Kg + (size_t)(t * 64 + krow) * DH + s * 16 + hh * 8); }
                f32x16 s0[3]; float d[3];
#pragma unroll
                for (int i = 0; i < 3; ++i) {
                    s0[i] = f32x16{};
#pragma unroll
                    for (int j = 0; j < 8; ++j) s0[i] = MFMA(kf[j], qf[i][j], s0[i]);
                    float pm = s0[i][0];
#pragma unroll
                    for (int r = 1; r < 16; ++r) pm = fmaxf(pm, s0[i][r]);
                    d[i] = (all || i == 2) ? fmaxf(xhalf_max(pm) - m[i], 0.f) : 0.f;
                    prop[(w4 * 3 + i) * 32 + r32] = d[i];
                }
                BAR_L();
#pragma unroll
                for (int i = 0; i < 3; ++i) {
                    const float dl = fmaxf(d[i], prop[((w4 ^ 2) * 3 + i) * 32 + r32]);
                    m[i] += dl;
                    float psum = l[i] * __builtin_amdgcn_exp2f(-dl);
#pragma unroll
                    for (int r = 0; r < 16; ++r) { s0[i][r] = (all || i == 2) ? __builtin_amdgcn_exp2f(s0[i][r] - m[i]) : 0.f; psum += s0[i][r]; }
                    l[i] = psum;
#pragma unroll
                    for (int s = 0; s < 2; ++s) { u32x4 w2;
#pragma unroll
                        for (int e = 0; e < 4; ++e) w2[e] = cvtpk(s0[i][8 * s + 2 * e], s0[i][8 * s + 2 * e + 1]);
                        *(LAS u32x4*)(pw + (i * 2 + s) * 1024) = w2; }
                    lsum[(w4 * 3 + i) * 32 + r32] = xhalf_sum(psum);
                }
                BAR_L();
            }
            BAR_L();
            wr_load(0, WRS);
        }
        wr_load(WRS, 5);
        TS(2);
    } else {
        const srd_t srdK = __builtin_amdgcn_make_buffer_rsrc((void*)Kg, (short)0, SK * DH * 2, 0x00020000);
        const srd_t srdV = __builtin_amdgcn_make_buffer_rsrc((void*)Vg, (short)0, SK * DH * 2, 0x00020000);
        const int arow = 8 * (w4 >> 1) + ((lane >> 2) & 7), ach = 4 * (2 * (w4 & 1) + hh) + (p4 ^ ((2 * (w4 >> 1) + blk) & 3));
        const unsigned kvoff = arow * 256 + ach * 16;
        auto kv_unit = [&](bool isV, int t) {
#pragma unroll
            for (int i = 0; i < 4; ++i)
                dma16<KV_NT>(isV ? srdV : srdK, kvoff, (unsigned)(t * 16384 + i * 4096), lds0 + (isV ? vslot(t) : kslot(t)) + (4 * i + w4) * 1024);
        };
        {
            const int rot = (hl * 5 + bl * 10 + 2) / 4 % 10;
            const srd_t srdX = __builtin_amdgcn_make_buffer_rsrc((void*)(x + (size_t)b * SQ * DM), (short)0, SQ * DM * 2, 0x00020000);
            const srd_t srdW = __builtin_amdgcn_make_buffer_rsrc((void*)(wq + h * DH), (short)0, DM * HD * 2 - h * DH * 2, 0x00020000);
            const unsigned xvoff = (8 * w4 + (lane >> 3)) * (DM * 2) + (((lane & 7) ^ ((4 * (w4 & 1) + (lane >> 4)) & 7)) << 4);
            const unsigned wvoff = arow * (HD * 2) + ach * 16;
            auto issue = [&](int s, int buf) __attribute__((always_inline)) {
                int cs = s + rot; if (cs >= 10) cs -= 10;
                const unsigned dst = lds0 + RING0 + buf;
#pragma unroll
                for (int i = 0; i < 4; ++i) dma16<XW_NT>(srdW, wvoff, (unsigned)((cs * 64 + i * 16) * HD * 2), dst + RXB + (4 * i + w4) * 1024);
#pragma unroll
                for (int i = 0; i < 6; ++i) dma16<XW_NT>(srdX, xvoff, (unsigned)(i * 32 * DM * 2 + cs * 128), dst + (4 * i + w4) * 1024);
            };
            if (wid == 4 && lane < 16) flagw[lane] = 0u;
            u32x4 pre[16];
            auto kvpre = [&](int n) __attribute__((always_inline)) {
                if (n < 8) dma16<KV_NT>(srdK, kvoff, (unsigned)(n * 4096), lds0 + (n >> 2) * SLOT + (4 * (n & 3) + w4) * 1024);
                else { const int u = (n - 8) >> 2, i = n & 3;
                       pre[n - 8] = __builtin_amdgcn_raw_buffer_load_b128((u & 1) ? srdK : srdV, kvoff, (unsigned)(((u & 1) ? 2 + (u >> 1) : (u >> 1)) * 16384 + i * 4096), 2); }
            };
            issue(0, 0); issue(1, RSTG);
            WAITV(10); BAR_L(); issue(2, 2 * RSTG); kvpre(0); kvpre(1); kvpre(2);
            WAITV(13); BAR_L(); issue(3, 0); kvpre(3); kvpre(4); kvpre(5);
#pragma unroll
            for (int s = 2; s < 8; ++s) { WAITV(16); BAR_L(); issue(s + 2, ((s + 2) % 3) * RSTG); kvpre(3 * s); kvpre(3 * s + 1); kvpre(3 * s + 2); }
            WAITV(16); BAR_L(); WAITV(3); BAR_L();
            BAR_L();
#pragma unroll
            for (int n = 0; n < 16; ++n) *(LAS u32x4*)(LW + slot_addr(2 + (n >> 2)) + (4 * (n & 3) + w4) * 1024 + lane * 16) = pre[n];
            BAR_L();
        }
        f32x16 o[3][2];
#pragma unroll
        for (int i = 0; i < 3; ++i) { o[i][0] = f32x16{}; o[i][1] = f32x16{}; }
        const int vb0 = 1024 * kh + 64 * (4 * hh + q4) + (((2 * blk + (p4 >> 1)) ^ hh) << 4) + 8 * (p4 & 1);
        const int vb1 = 1024 * kh + 2048 + 64 * (4 * hh + q4) + (((2 * blk + (p4 >> 1)) ^ (2 + hh)) << 4) + 8 * (p4 & 1);
        const lds_cptr pb0 = L + POFF + qh * 12288 + lane * 16;
        s16x4 vl[2][4][2], vh[2][4][2];
        bf16x8 pf[2][3];
        unsigned wv[5];
#pragma unroll
        for (int p = 0; p < 5; ++p) { const int o_ = p * 1024 + lane * 16, row = o_ / 160, ch = (o_ % 160) >> 4; wv[p] = row * (DM * 2) + ch * 16; }
        const unsigned tvoff = r32 * (DM * 2) + hh * 156;
        const srd_t srdO = __builtin_amdgcn_make_buffer_rsrc((void*)(ws + WS_O + (size_t)(b * NH + h) * 49152), (short)0, 49152, 0x00020000);
        u32x4 ow[3][4];
        auto opack = [&](int i) __attribute__((always_inline)) {
            const float rl = 1.0f / (lsum[(qh * 3 + i) * 32 + r32] + lsum[((qh + 2) * 3 + i) * 32 + r32]);
#pragma unroll
            for (int db = 0; db < 2; ++db)
#pragma unroll
                for (int s = 0; s < 2; ++s) {
#pragma unroll
                    for (int e = 0; e < 4; ++e) ow[i][db * 2 + s][e] = cvtpk(o[i][db][8 * s + 2 * e] * rl, o[i][db][8 * s + 2 * e + 1] * rl);
                    st16_wt(ow[i][db * 2 + s], srdO, lane * 16, ((3 * qh + i) * 8 + kh * 4 + db * 2 + s) * 1024); }
        };
        auto olds = [&](int i) __attribute__((always_inline)) {
#pragma unroll
            for (int db = 0; db < 2; ++db) { const lds_ptr p_ = LW + own_addr((3 * qh + i) * 4 + kh * 2 + db) + lane * 16;
                *(LAS u32x4*)p_ = ow[i][db * 2]; *(LAS u32x4*)(p_ + 1024) = ow[i][db * 2 + 1]; }
        };
#define RDV(X, vb, ks, db) do { vl[X][ks][db] = ldstr(vb + vb0 + (ks) * 4096 + (db) * 512); vh[X][ks][db] = ldstr(vb + vb1 + (ks) * 4096 + (db) * 512); } while (0)
#define RDP(buf, blk_, ks, i) pf[buf][i] = ldsr128(pb0 + ((blk_) & 1) * 6144 + ((ks) >> 1) * 24576 + ((i) * 2 + ((ks) & 1)) * 1024)
#define PVM(X, buf, ks, i, db) o[i][db] = MFMA(cat8(vl[X][ks][db], vh[X][ks][db]), pf[buf][i], o[i][db])
        auto period = [&](auto tag) __attribute__((always_inline)) -> bool {
            constexpr int P = decltype(tag)::value, X = P & 1;
            constexpr bool AB = P <= 8, C = P >= 2;
            const lds_cptr vb = L + vslot(AB ? P - 1 : 0);
            auto dm = [&](int n) __attribute__((always_inline)) {
                if constexpr (P <= 5) {
                    if (n < 4) dma16<KV_NT>(srdV, kvoff, (unsigned)((P + 1) * 16384 + n * 4096), lds0 + vslot(P + 1) + (4 * n + w4) * 1024);
                    else if constexpr (P < 5) dma16<KV_NT>(srdK, kvoff, (unsigned)((P + 3) * 16384 + (n - 4) * 4096), lds0 + kslot(P + 3) + (4 * (n - 4) + w4) * 1024);
                    else dma16<KV_NT>(srdV, kvoff, (unsigned)(7 * 16384 + (n - 4) * 4096), lds0 + vslot(7) + (4 * (n - 4) + w4) * 1024);
                } else if constexpr (P <= 8) {
                    const int g = (P == 6) ? 16 + w4 : (P == 7) ? w4 : (w4 < 2 ? 4 + w4 : 6 + w4);
                    if (n < 5) dma16<WO_NT>(srdWo, wv[n], wo_soff(g), lds0 + g * 5120 + n * 1024);
                }
            };
            auto rd = [&](int buf, int ks, int part) __attribute__((always_inline)) {
                if (part == 0) { if (C) RDP(buf, P - 2, ks, 2); }
                if (part == 1) { if (AB) RDV(X, vb, ks, 0); }
                if (part == 2) { if (AB) RDP(buf, P - 1, ks, 0); }
                if (part == 3) { if (AB) RDV(X, vb, ks, 1); }
                if (part == 4) { if (AB) RDP(buf, P - 1, ks, 1); }
            };
            auto touch = [&](int n) __attribute__((always_inline)) {
                if constexpr (P == TOUCH_P) touch4<TOUCH_NT>(srdWo, tvoff, (unsigned)((bl * 256 + w4 * 64 + n * 32) * DM * 2), lds0 + 155648u);
            };
            const unsigned flv = flagw[P - 1];
            rd(0, 0, 0); rd(0, 0, 1); rd(0, 0, 2); rd(0, 0, 3); rd(0, 0, 4); SBAR();
            if (__builtin_expect(__builtin_amdgcn_readfirstlane(flv) != 0u, 0)) return true;
            if constexpr (P == 9) { olds(0); olds(1); }
#pragma unroll
            for (int ks = 0; ks < 4; ++ks) {
                const int cb = ks & 1; const bool more = ks < 3;
                if (C) PVM(X ^ 1, cb, ks, 2, 0); if (more) rd(cb ^ 1, ks + 1, 0); SBAR();
                if (C) PVM(X ^ 1, cb, ks, 2, 1); if (more) rd(cb ^ 1, ks + 1, 1); SBAR();
                if (AB) PVM(X, cb, ks, 0, 0); if (more) rd(cb ^ 1, ks + 1, 2); SBAR();
                if (AB) PVM(X, cb, ks, 0, 1); if (more) rd(cb ^ 1, ks + 1, 3); dm(2 * ks); SBAR();
                if (AB) PVM(X, cb, ks, 1, 0); if (more) rd(cb ^ 1, ks + 1, 4); if (ks >= 2) touch(ks - 2); SBAR();
                if (AB) PVM(X, cb, ks, 1, 1); dm(2 * ks + 1); SBAR();
            }
            {
                constexpr int T0 = (P == TOUCH_P) ? 2 : 0, T1 = (P - 1 == TOUCH_P) ? 2 : 0;
                constexpr int N = P <= 5 ? 8 + T0 : P == 6 ? 9 + T0 : 10 + T0 + T1;
                if constexpr (P <= 8) asm volatile("s_waitcnt vmcnt(%0)" :: "i"(N) : "memory");
            }
            if constexpr (P == 8) { opack(0); opack(1); }
            if constexpr (P <= 8) BAR_L();
            return false;
        };
        BAR_L();
        BAR_L();
        int tf = 9;
#define PERIOD(P) if (tf == 9) { if (period(std::integral_constant<int, P>{})) tf = P - 1; }
        PERIOD(1) PERIOD(2) PERIOD(3) PERIOD(4) PERIOD(5) PERIOD(6) PERIOD(7) PERIOD(8) PERIOD(9)
        slow = tf < 9;
        if (__builtin_expect(slow, 0)) {
            WAITV(0); BAR_L();
#pragma unroll 1
            for (int t = tf - 1; t < 8; ++t) {
                const int sl = slot_addr(2 + (t & 1));
#pragma unroll
                for (int n = 0; n < 4; ++n) dma16<KV_NT>(srdV, kvoff, (unsigned)(t * 16384 + n * 4096), lds0 + sl + (4 * n + w4) * 1024);
                BAR_L();
#pragma unroll
                for (int i = 0; i < 3; ++i) {
                    const float dl = fmaxf(prop[(qh * 3 + i) * 32 + r32], prop[((qh + 2) * 3 + i) * 32 + r32]);
                    const float f = __builtin_amdgcn_exp2f(-dl);
#pragma unroll
                    for (int r = 0; r < 16; ++r) { o[i][0][r] *= f; o[i][1][r] *= f; }
                }
                WAITV(0);
                BAR_L();
                const lds_cptr vb = L + sl;
#pragma unroll
                for (int ks = 0; ks < 4; ++ks) {
                    RDV(0, vb, ks, 0); RDV(0, vb, ks, 1); RDP(0, t, ks, 0); RDP(0, t, ks, 1); RDP(0, t, ks, 2);
#pragma unroll
                    for (int i = 0; i < 3; ++i) { PVM(0, 0, ks, i, 0); PVM(0, 0, ks, i, 1); }
                }
            }
            BAR_L();
            opack(0); opack(1); olds(0); olds(1);
        }
        TS(6);
        opack(2); olds(2);
        TS(3);
    }
    int ln; asm volatile("v_mbcnt_lo_u32_b32 %0, -1, 0\n\tv_mbcnt_hi_u32_b32 %0, -1, %0" : "=v"(ln));
    const int qg = wid & 3, kh2 = wid >> 2;
    unsigned base = 0, echo_want = 0, echo = 0;
    gu32* const pflag = PFLAG(ln);
    gu32* const pecho = E + PEER(ln) * 32 + (ln < 8 ? hl : 8 + hg);
    if (wid == 4) {
        base = basew[ln < 12 ? ln : 0];
        const unsigned my1 = basew[hl], my2 = basew[8 + hg];
        echo_want = (ln < 8 ? my1 : my2) ^ ECHO_KEY;
        echo = __hip_atomic_load(pecho, RLX_AGENT);
    }
    TS(16);
    BAR_L(); TS(17);
    if (__builtin_expect(slow, 0)) { if (wid < 4) wo_groups(w4, 20, 4); WAITV(0); BAR_L(); }
    {
        const int g = ln >> 4, qq = ln & 15;
        const srd_t srdOg = __builtin_amdgcn_make_buffer_rsrc((void*)(ws + WS_O + (size_t)(b * NH + hg * 8) * 49152), (short)0, 8 * 49152, 0x00020000);
        const unsigned ov0 = (g >> 1) * 1024 + (g & 1) * 512 + qq * 16, ov = ov0 + kh2 * 4096;
        constexpr int OW = 6;
        u32x4 of[OW][3];
#define LDO1(st, i) do { const int head_ = (hl + ((st) >> 1)) & 7, t16 = 3 * qg + (i); \
                of[(st) % OW][i] = __builtin_amdgcn_raw_buffer_load_b128(srdOg, ov, head_ * 49152 + (t16 >> 1) * 8192 + (t16 & 1) * 256 + ((st) & 1) * 2048, 16); } while (0)
#define LDO(st) do { LDO1(st, 0); LDO1(st, 1); LDO1(st, 2); } while (0)
#pragma unroll
        for (int st = 0; st < 2; ++st)
#pragma unroll
            for (int i = 0; i < 3; ++i) { const int t16 = 3 * qg + i;
                of[st][i] = *(const LAS u32x4*)(L + own_addr((t16 >> 1) * 4 + kh2 * 2 + st) + (t16 & 1) * 256 + ov0); }
        f32x4 acc[5][3];
#pragma unroll
        for (int n = 0; n < 5; ++n)
#pragma unroll
            for (int i = 0; i < 3; ++i) acc[n][i] = f32x4{};
        lds_cptr wb[3];
        wb[0] = L + kh2 * 5120 + (16 * (g >> 1) + 4 * (g & 1) + (qq >> 2)) * 160 + (ln & 3) * 8;
        wb[1] = wb[0] + 61440; wb[2] = wb[0] + 122880;
        PIN(wb[1]); PIN(wb[2]);
        s16x4 wl[2][5], wh[2][5];
#define RDWO(buf, st, n) do { const lds_cptr p_ = wb[PAIR_OF_STEP[st] / 6] + (PAIR_OF_STEP[st] % 6) * 10240 + (n) * 32; wl[buf][n] = ldstr(p_); wh[buf][n] = ldstr(p_ + 1280); } while (0)
#pragma unroll
        for (int n = 0; n < 5; ++n) RDWO(0, 0, n);
#pragma unroll
        for (int st = 0; st < 16; ++st) {
            if (st == 1) {
                TS(18); WAITV(0); TS(19);
                BAR_L();
                TS(7);
                if (wid == 4) {
#pragma unroll 1
                    for (unsigned sp = 0; __any(ln < 12 && echo != echo_want) && sp < SPIN_CAP; ++sp) {
                        __builtin_amdgcn_s_sleep(2);
                        echo = __hip_atomic_load(pecho, RLX_AGENT);
                    }
                    if (ln == hl) __hip_atomic_store(pflag, base + 1u, RLX_AGENT);
                } else if (wid == 5) {
#pragma unroll
                    for (int k = 0; k < 6; ++k) touch4<false>(srdOg, ln * 128, (unsigned)(hl * 49152 + k * 8192), lds0 + 81920u);
                } else if (wid < 4) {
#pragma unroll
                    for (int k = 0; k < 5; ++k)
#pragma unroll
                        for (int p = 0; p < 5; ++p) *(LAS u32x4*)(LW + late_group(k) * 5120 + p * 1024 + ln * 16) = wr[k * 5 + p];
                }
                SBAR(); TS(20);
            }
            if (st == 2) {
                TS(21);
                if (wid == 4) {
#pragma unroll 1
                    for (unsigned sp = 0; sp < SPIN_CAP; ++sp) {
                        const unsigned v = __hip_atomic_load(pflag, RLX_AGENT);
                        if (!__any(ln < 8 && v == base)) break;
                        __builtin_amdgcn_s_sleep(1);
                    }
                }
                TS(22); WAITV(0); TS(23);
                BAR_L();
                TS(12);
                if (kh2) __builtin_amdgcn_s_setprio(1);
#pragma unroll
                for (int s2 = 2; s2 < 2 + OW - 1; ++s2) LDO(s2);
                SBAR();
            }
#pragma unroll
            for (int n = 0; n < 5; ++n) {
#pragma unroll
                for (int i = 0; i < 3; ++i) acc[n][i] = MFMA16(cat8(wl[st & 1][n], wh[st & 1][n]), __builtin_bit_cast(bf16x8, of[st % OW][i]), acc[n][i]);
                if (st < 15) RDWO((st + 1) & 1, st + 1, n);
                if (st >= 2 && st + OW - 1 < 16 && n < 3) LDO1(st + OW - 1, n);
                SBAR();
            }
            if (st == 3) TS(24);
            if (st == 10) { TS(25); BAR_L(); TS(26); }
            if (0) BAR_L();
        }
        TS(13);
#pragma unroll
        for (int tl = 0; tl < 15; ++tl)
            if ((tl < 8) != (kh2 == 0)) *(LAS f32x4*)(LW + ((qg * 15 + tl) * 64 + ln) * 16) = acc[tl / 3][tl % 3];
        BAR_L(); TS(27);
        const int dst = ((xcd & 4) | qg) + 8 * jj;
        const srd_t srdP = __builtin_amdgcn_make_buffer_rsrc((void*)(ws + WS_PART + (size_t)dst * 24576 + ((hg - qg - 1) & 3) * 8192), (short)0, 8192, 0x00020000);
#pragma unroll
        for (int u = 0; u < 8; ++u)
            if ((u < 4) == (kh2 == 0)) {
                u32x4 w = {};
#pragma unroll
                for (int k = 0; k < 2; ++k) { const int tl = 2 * u + k;
                    if (tl < 15) {
                        const f32x4 v = acc[tl / 3][tl % 3] + *(const LAS f32x4*)(L + ((qg * 15 + tl) * 64 + ln) * 16);
                        if (qg == hg) *(LAS f32x4*)(LW + ((qg * 15 + tl) * 64 + ln) * 16) = v;
                        w[2 * k] = cvtpk(v[0], v[1]); w[2 * k + 1] = cvtpk(v[2], v[3]);
                    } }
                if (qg != hg) st16_wt(w, srdP, ln * 16, u * 1024);
            }
        TS(28);
        WAITV(0);
        BAR_L();
        TS(14);
        if (wid == 4) {
            if (ln == 8 + hg) __hip_atomic_store(pflag, base + 1u, RLX_AGENT);
#pragma unroll 1
            for (unsigned sp = 0; sp < SPIN_CAP; ++sp) {
                const unsigned v = __hip_atomic_load(pflag, RLX_AGENT);
                if (!__any(ln >= 8 && ln < 12 && v == base)) break;
                __builtin_amdgcn_s_sleep(1);
            }
        }
        BAR_L();
        TS(29);
        const srd_t srdI = __builtin_amdgcn_make_buffer_rsrc((void*)(ws + WS_PART + (size_t)blockIdx.x * 24576), (short)0, 24576, 0x00020000);
        u32x4 pv_[3];
#pragma unroll
        for (int j = 0; j < 3; ++j) pv_[j] = __builtin_amdgcn_raw_buffer_load_b128(srdI, ln * 16, j * 8192 + wid * 1024, 16);
#pragma unroll
        for (int k = 0; k < 2; ++k) {
            const int tl = 2 * wid + k;
            if (tl < 15) {
                f32x4 sum = *(const LAS f32x4*)(L + ((hg * 15 + tl) * 64 + ln) * 16);
#pragma unroll
                for (int j = 0; j < 3; ++j) {
                    sum[0] += __uint_as_float(pv_[j][2 * k] << 16); sum[1] += __uint_as_float(pv_[j][2 * k] & 0xffff0000u);
                    sum[2] += __uint_as_float(pv_[j][2 * k + 1] << 16); sum[3] += __uint_as_float(pv_[j][2 * k + 1] & 0xffff0000u);
                }
                const int n = tl / 3, i = tl % 3;
                const int q = (3 * hg + i) * 16 + qq, col = hl * 80 + n * 16 + g * 4;
                uint2 w; w.x = cvtpk(sum[0], sum[1]); w.y = cvtpk(sum[2], sum[3]);
                *(uint2*)(out + (size_t)(b * SQ + q) * DM + col) = w;
            }
        }
        TS(15);
    }
}

extern "C" void kernel_launch(void* const* d_in, const int* in_sizes, int n_in, void* d_out, int out_size, void* d_ws, size_t ws_size, hipStream_t stream) {
    const bf16* x = (const bf16*)d_in[0]; const bf16* kc = (const bf16*)d_in[1]; const bf16* vc = (const bf16*)d_in[2];
    const bf16* wq = (const bf16*)d_in[3]; const bf16* wo = (const bf16*)d_in[4];
    (void)hipFuncSetAttribute((const void*)k1_attn, hipFuncAttributeMaxDynamicSharedMemorySize, LDS_BYTES);
    k1_attn<<<256, 512, LDS_BYTES, stream>>>(x, kc, vc, wq, wo, (char*)d_ws, (bf16*)d_out);
}
```

```cpp
#include <hip/hip_runtime.h>
#include <hip/hip_bf16.h>
#include <stdint.h>
#include <type_traits>

typedef __hip_bfloat16 bf16;
using bf16x8 = __attribute__((ext_vector_type(8))) short;
using s16x4  = __attribute__((ext_vector_type(4))) short;
using f32x16 = __attribute__((ext_vector_type(16))) float;
using f32x4  = __attribute__((ext_vector_type(4))) float;
using u32x4  = __attribute__((ext_vector_type(4))) unsigned;
typedef short v4i16_t __attribute__((ext_vector_type(4)));
#define LAS __attribute__((address_space(3)))
typedef LAS const char* lds_cptr;
typedef LAS char* lds_ptr;
typedef __amdgpu_buffer_rsrc_t srd_t;
#define DI __device__ __forceinline__
#define SBAR() __builtin_amdgcn_sched_barrier(0)
#define PIN(x) asm volatile("" : "+v"(x))
#define MFMA(a, b, c) __builtin_amdgcn_mfma_f32_32x32x16_bf16(a, b, c, 0, 0, 0)

constexpr int NB = 8, SQ = 192, DM = 640, NH = 32, SK = 512, DH = 128, HD = NH * DH;
constexpr float QSCALE = 0.08838834764831845f * 1.4426950408889634f;
#define PSLIM 65536.0f

#define TS(i)

typedef float f32x2 __attribute__((ext_vector_type(2)));
typedef __bf16 bf16x2 __attribute__((ext_vector_type(2)));
DI unsigned cvtpk(float lo, float hi) { f32x2 v = {lo, hi}; bf16x2 b = __builtin_convertvector(v, bf16x2); return __builtin_bit_cast(unsigned, b); }
DI float fmul_s(float a, float b) { return a * b; }
template <bool NT>
DI void dma16(srd_t srd, unsigned voff, unsigned soff, unsigned lds_dst_) {
    const unsigned lds_dst = __builtin_amdgcn_readfirstlane(lds_dst_);
    if constexpr (NT) asm volatile("s_mov_b32 m0, %3\n\ts_nop 0\n\tbuffer_load_dwordx4 %0, %1, %2 offen nt lds" :: "v"(voff), "s"(srd), "s"(soff), "s"(lds_dst) : "m0", "memory");
    else              asm volatile("s_mov_b32 m0, %3\n\ts_nop 0\n\tbuffer_load_dwordx4 %0, %1, %2 offen lds" :: "v"(voff), "s"(srd), "s"(soff), "s"(lds_dst) : "m0", "memory");
}
template <bool NT>
DI void ld16_async(u32x4& r, srd_t srd, unsigned voff, unsigned soff) {
    if constexpr (NT) asm volatile("buffer_load_dwordx4 %0, %1, %2, %3 offen nt" : "=v"(r) : "v"(voff), "s"(srd), "s"(soff) : "memory");
    else              asm volatile("buffer_load_dwordx4 %0, %1, %2, %3 offen" : "=v"(r) : "v"(voff), "s"(srd), "s"(soff) : "memory");
}
template <bool NT>
DI void touch4(srd_t srd, unsigned voff, unsigned soff, unsigned lds_dst) {
    if constexpr (NT) asm volatile("s_mov_b32 m0, %3\n\ts_nop 0\n\tbuffer_load_dword %0, %1, %2 offen nt lds" :: "v"(voff), "s"(srd), "s"(soff), "s"(lds_dst) : "m0", "memory");
    else              asm volatile("s_mov_b32 m0, %3\n\ts_nop 0\n\tbuffer_load_dword %0, %1, %2 offen lds" :: "v"(voff), "s"(srd), "s"(soff), "s"(lds_dst) : "m0", "memory");
}
DI bf16x8 ldsr128(lds_cptr p) { return *(const LAS bf16x8*)p; }
DI s16x4 ldstr(lds_cptr p) { return __builtin_bit_cast(s16x4, __builtin_amdgcn_ds_read_tr16_b64_v4i16((LAS v4i16_t*)p)); }
DI bf16x8 cat8(s16x4 a, s16x4 b) { return __builtin_shufflevector(a, b, 0, 1, 2, 3, 4, 5, 6, 7); }
#define WAITV(N) asm volatile("s_waitcnt vmcnt(" #N ")" ::: "memory")
#define BAR_L() asm volatile("s_waitcnt lgkmcnt(0)\n\ts_barrier" ::: "memory")
DI float xhalf_max(float v) { auto rr = __builtin_amdgcn_permlane32_swap(__float_as_uint(v), __float_as_uint(v), false, false); return fmaxf(__uint_as_float(rr[0]), __uint_as_float(rr[1])); }
DI float xhalf_sum(float v) { auto rr = __builtin_amdgcn_permlane32_swap(__float_as_uint(v), __float_as_uint(v), false, false); return __uint_as_float(rr[0]) + __uint_as_float(rr[1]); }

#define KV_NT true
#define XW_NT false

#define GAS __attribute__((address_space(1)))
typedef GAS unsigned gu32;
#define RLX_AGENT __ATOMIC_RELAXED, __HIP_MEMORY_SCOPE_AGENT
#define MFMA16(a, b, c) __builtin_amdgcn_mfma_f32_16x16x32_bf16(a, b, c, 0, 0, 0)
DI void st16_wt(u32x4 v, srd_t srd, unsigned voff, unsigned soff) {
    __builtin_amdgcn_raw_buffer_store_b128(v, srd, voff, soff, 16); SBAR(); asm volatile("s_nop 1"); SBAR();
}
constexpr unsigned ECHO_KEY = 0x5A17C3E9u;
constexpr unsigned SPIN_CAP = 1u << 20;
constexpr size_t WS_O = 0, WS_PART = 16u << 20, WS_SYNC = 48u << 20;
constexpr int PAIR_OF_STEP[16] = {8, 9, 0, 1, 2, 4, 5, 6, 7, 3, 10, 11, 12, 13, 14, 15};
constexpr unsigned long long STEP_OF_PAIR = 0xFEDCBA1087659432ull;
#define WO_NT false
#define TOUCH_NT false
#define CODE_TOUCH 30720
#define NO_SELFWARM
#define OWIN 4
#define COMMON0
#define ROTF ((((hl + 2 * bl) & 7) * 9) / 8)
#define WRS 2
#define TOUCH_P 99
constexpr int SLOT = 16384;
constexpr int MOFF = 32768;
constexpr int RING0 = 40960, RSTG = 40960, RXB = 24576;
constexpr int POFF = 106496;
constexpr int LDS_BYTES = 163840;
constexpr unsigned KSLOTS = 0x04215310u;
constexpr unsigned VSLOTS = 0x32153042u;
DI int slot_addr(unsigned s) { return s * SLOT + (s >= 2 ? 8192 : 0); }
DI int own_addr(int pr) { return pr < 20 ? POFF + (pr / 5) * 12288 + (pr % 5) * 2048 : 155648 + (pr - 20) * 2048; }
DI int kslot(int t) { return slot_addr((KSLOTS >> (4 * t)) & 15u); }
DI int vslot(int t) { return slot_addr((VSLOTS >> (4 * t)) & 15u); }
#define GROUPB(bl, hl) (((bl) + (hl)) & 1)

__global__ __launch_bounds__(512, 2) void k1_attn(const bf16* __restrict__ x, const bf16* __restrict__ kc, const bf16* __restrict__ vc,
                                                  const bf16* __restrict__ wq, const bf16* __restrict__ wo, char* __restrict__ ws, bf16* __restrict__ out) {
    extern __shared__ __attribute__((aligned(16))) char lds[];
    TS(0);
    unsigned long long pc0; asm volatile("s_getpc_b64 %0" : "=s"(pc0));
    const int tid = threadIdx.x, lane = tid & 63, r32 = lane & 31, hh = lane >> 5;
    const int wid = __builtin_amdgcn_readfirstlane(tid >> 6), w4 = wid & 3;
    const int xcd = blockIdx.x & 7, jj = blockIdx.x >> 3;
    const int bl = jj >> 3, hl = jj & 7;
    const int b = (xcd >> 2) * 4 + bl, h = (xcd & 3) * 8 + hl;
    const unsigned lds0 = (unsigned)(uintptr_t)lds;
    const lds_cptr L = (lds_cptr)lds;
    const lds_ptr LW = (lds_ptr)lds;
    const bf16* Kg = kc + (size_t)(b * NH + h) * SK * DH;
    const bf16* Vg = vc + (size_t)(b * NH + h) * SK * DH;
    const int qh = w4 & 1, kh = w4 >> 1;
    const int q4 = (lane & 15) >> 2, p4 = lane & 3, blk = (lane >> 4) & 1;
    LAS unsigned* flagw = (LAS unsigned*)(LW + MOFF);
    LAS unsigned* basew = (LAS unsigned*)(LW + MOFF + 128);
    const int hg = xcd & 3;
    gu32* const F = (gu32*)(ws + WS_SYNC);
    gu32* const E = (gu32*)(ws + WS_SYNC + 65536);
#define PEER(l_) ((l_) < 8 ? xcd + 8 * (bl * 8 + (l_)) : ((xcd & 4) | ((l_) & 3)) + 8 * jj)
#define PFLAG(l_) (F + (((l_) < 8 ? 0 : 256) + PEER(l_)) * 32)
    LAS float* prop = (LAS float*)(LW + MOFF + 256);
    LAS float* lsum = (LAS float*)(LW + MOFF + 2048);

    const int hdir = (bl & 1) ? -1 : 1;
    const srd_t srdWo = __builtin_amdgcn_make_buffer_rsrc((void*)(wo + (size_t)hg * 1024 * DM + hl * 80), (short)0, 1024 * DM * 2 - hl * 160, 0x00020000);
    auto wo_soff = [&](int g) __attribute__((always_inline)) -> unsigned {
        const int st = (int)((STEP_OF_PAIR >> (4 * (g >> 1))) & 15u);
        return (unsigned)((((hl + hdir * (st >> 1)) & 7) * 128 + (2 * (g & 1) + (st & 1)) * 32) * DM * 2);
    };
    auto wo_groups = [&](int g0, int g1, int gs) __attribute__((always_inline)) {
        int l_; asm volatile("v_mbcnt_lo_u32_b32 %0, -1, 0\n\tv_mbcnt_hi_u32_b32 %0, -1, %0" : "=v"(l_));
        unsigned wv[5];
#pragma unroll
        for (int p = 0; p < 5; ++p) { const int o_ = p * 1024 + l_ * 16, row = o_ / 160, ch = (o_ % 160) >> 4; wv[p] = row * (DM * 2) + ch * 16; }
#pragma unroll 1
        for (int g = g0; g < g1; g += gs) {
            const unsigned so = wo_soff(g);
#pragma unroll
            for (int p = 0; p < 5; ++p) dma16<WO_NT>(srdWo, wv[p], so, lds0 + g * 5120 + p * 1024);
        }
    };
    bool slow = false;
    unsigned base = 0, echo_want = 0;

    if (wid < 4) {
        if (wid == 0) {
            if (lane < 12) {
                const unsigned base = __hip_atomic_load(PFLAG(lane), RLX_AGENT);
                basew[lane] = base;
                __hip_atomic_store(E + blockIdx.x * 32 + lane, base ^ ECHO_KEY, RLX_AGENT);
            }
        }
        if (wid == 1) {
            const srd_t srdC = __builtin_amdgcn_make_buffer_rsrc((void*)(pc0 & ~127ull), (short)0, CODE_TOUCH, 0x00020000);
#pragma unroll
            for (int k = 0; k < 4; ++k) touch4<false>(srdC, lane * 128, (unsigned)(k * 8192), lds0 + MOFF + 4096u);
        }
        bf16x8 qf[3][8];
        {
            f32x16 acc[3][2];
#pragma unroll
            for (int i = 0; i < 3; ++i) { acc[i][0] = f32x16{}; acc[i][1] = f32x16{}; }
            const int xsw = (r32 >> 1) & 7;
            int xa[4];
#pragma unroll
            for (int ks = 0; ks < 4; ++ks) xa[ks] = (96 * qh + r32) * 128 + (((2 * ks + hh) ^ xsw) << 4);
            const int ps = ((p4 & 1) << 1) | (p4 >> 1);
            const int wa0 = RXB + 2048 * hh + 1024 * kh + 64 * q4 + (((2 * blk + (ps >> 1)) ^ (2 * hh)) << 4) + 8 * (ps & 1);
            const int wa1 = RXB + 2048 * hh + 1024 * kh + 64 * q4 + 256 + (((2 * blk + (ps >> 1)) ^ (2 * hh + 1)) << 4) + 8 * (ps & 1);
            bf16x8 xf[2][3]; s16x4 wl[2][2], wh[2][2];
#define RDX(buf, base, ks, i) xf[buf][i] = ldsr128(base + xa[ks] + i * 4096)
#define RDW(buf, base, ks, db) do { wl[buf][db] = ldstr(base + wa0 + ks * 4096 + db * 512); wh[buf][db] = ldstr(base + wa1 + ks * 4096 + db * 512); } while (0)
#define MM(buf, i, db) acc[i][db] = MFMA(cat8(wl[buf][db], wh[buf][db]), xf[buf][i], acc[i][db])
#define HALF(cb, base, ks) do { \
                MM(cb, 0, 0); RDW(cb ^ 1, base, ks, 0); SBAR(); \
                MM(cb, 0, 1); RDX(cb ^ 1, base, ks, 0); SBAR(); \
                MM(cb, 1, 0); RDW(cb ^ 1, base, ks, 1); SBAR(); \
                MM(cb, 1, 1); RDX(cb ^ 1, base, ks, 1); SBAR(); \
                MM(cb, 2, 0); RDX(cb ^ 1, base, ks, 2); SBAR(); \
                MM(cb, 2, 1); SBAR(); } while (0)
            BAR_L();
            TS(4);
            { const lds_cptr base = L + RING0;
              RDW(0, base, 0, 0); RDX(0, base, 0, 0); RDW(0, base, 0, 1); RDX(0, base, 0, 1); RDX(0, base, 0, 2); SBAR();
              HALF(0, base, 1); HALF(1, base, 2); HALF(0, base, 3); }
            int rb = RSTG;
#pragma unroll 1
            for (int s = 1; s < 10; ++s) {
                SBAR(); BAR_L(); SBAR();
                const lds_cptr base = L + RING0 + rb;
                HALF(1, base, 0); HALF(0, base, 1); HALF(1, base, 2); HALF(0, base, 3);
                rb = (rb == 2 * RSTG) ? 0 : rb + RSTG;
            }
            BAR_L();
            MM(1, 0, 0); MM(1, 0, 1); MM(1, 1, 0); MM(1, 1, 1); MM(1, 2, 0); MM(1, 2, 1);
            TS(5);
#pragma unroll
            for (int i = 0; i < 3; ++i)
#pragma unroll
                for (int db = 0; db < 2; ++db)
#pragma unroll
                    for (int s = 0; s < 2; ++s) {
                        u32x4 w;
#pragma unroll
                        for (int e = 0; e < 4; ++e) w[e] = cvtpk(acc[i][db][8 * s + 2 * e] * QSCALE, acc[i][db][8 * s + 2 * e + 1] * QSCALE);
                        *(LAS u32x4*)(LW + POFF + (w4 ^ 2) * 12288 + (i * 4 + db * 2 + s) * 1024 + lane * 16) = w;
                        qf[i][db * 2 + s] = __builtin_bit_cast(bf16x8, w);
                    }
            BAR_L();
#pragma unroll
            for (int i = 0; i < 3; ++i)
#pragma unroll
                for (int j = 0; j < 4; ++j) qf[i][4 + j] = ldsr128(L + POFF + w4 * 12288 + (i * 4 + j) * 1024 + lane * 16);
        }
        TS(1);
        const int krow = 32 * kh + r32;
        const int kb0 = 2048 * (krow >> 3) + 64 * (krow & 7) + ((hh ^ ((krow >> 2) & 3)) << 4);
        const int kb1 = 2048 * (krow >> 3) + 64 * (krow & 7) + (((2 + hh) ^ ((krow >> 2) & 3)) << 4);
        const int kown = kh * 1024, koth = (1 - kh) * 1024;
        bf16x8 kf[8];
        unsigned ka[4];
#define KRD(j) asm volatile("ds_read_b128 %0, %1 offset:%2" : "+v"(kf[j]) : "v"(ka[((j) >> 2) * 2 + ((j) & 1)]), "i"(512 * (((j) >> 1) & 1)) : "memory")
#define KADDR(t_) do { const unsigned kb_ = lds0 + kslot(t_); ka[0] = kb_ + kown + kb0; ka[1] = kb_ + kown + kb1; ka[2] = kb_ + koth + kb0; ka[3] = kb_ + koth + kb1; } while (0)
#define KRDALL() do { KRD(0); KRD(1); KRD(2); KRD(3); KRD(4); KRD(5); KRD(6); KRD(7); } while (0)
        f32x16 negm[3], sc;
        float l[3], m[3], lold[3];
#pragma unroll
        for (int j = 0; j < 8; ++j) kf[j] = bf16x8{};
        KADDR(0); KRDALL();
        asm volatile("s_waitcnt lgkmcnt(0)" ::: "memory"); SBAR();
        {
            const lds_ptr pw = LW + POFF + w4 * 12288 + lane * 16;
            f32x16 s0[3]; float d[3];
#pragma unroll
            for (int i = 0; i < 3; ++i) {
                s0[i] = f32x16{};
#pragma unroll
                for (int j = 0; j < 8; ++j) s0[i] = MFMA(kf[j], qf[i][j], s0[i]);
            }
            SBAR();
            KADDR(1); KRDALL();
#pragma unroll
            for (int i = 0; i < 3; ++i) {
                float pm = s0[i][0];
#pragma unroll
                for (int r = 1; r < 16; ++r) pm = fmaxf(pm, s0[i][r]);
                d[i] = xhalf_max(pm);
                prop[(w4 * 3 + i) * 32 + r32] = d[i];
            }
            BAR_L();
#pragma unroll
            for (int i = 0; i < 3; ++i) {
                m[i] = fmaxf(d[i], prop[((w4 ^ 2) * 3 + i) * 32 + r32]);
                float psum = 0.f;
#pragma unroll
                for (int r = 0; r < 16; ++r) { negm[i][r] = -m[i]; s0[i][r] -= m[i]; }
                if (i < 2) {
#pragma unroll
                    for (int r = 0; r < 16; ++r) { s0[i][r] = __builtin_amdgcn_exp2f(s0[i][r]); psum += s0[i][r]; }
#pragma unroll
                    for (int s = 0; s < 2; ++s) { u32x4 w;
#pragma unroll
                        for (int e = 0; e < 4; ++e) w[e] = cvtpk(s0[i][8 * s + 2 * e], s0[i][8 * s + 2 * e + 1]);
                        *(LAS u32x4*)(pw + (i * 2 + s) * 1024) = w; }
                }
                l[i] = psum; lold[i] = psum;
            }
            sc = s0[2];
            BAR_L(); SBAR();
        }
        TS(8);
#define EX2(X, g) do { X[2 * (g)] = __builtin_amdgcn_exp2f(X[2 * (g)]); X[2 * (g) + 1] = __builtin_amdgcn_exp2f(X[2 * (g) + 1]); } while (0)
#define AP(X, g) do { if ((g) == 0) pa = X[0] + X[1]; else { pa += X[2 * (g)]; pa += X[2 * (g) + 1]; } w[(g) & 3] = cvtpk(X[2 * (g)], X[2 * (g) + 1]); PIN(pa); PIN(w); } while (0)
#define PST(PW, i, s) *(LAS u32x4*)(PW + ((i) * 2 + (s)) * 1024) = w
#define SMS(X, PW, i, g) do { EX2(X, g); if ((g) > 0) AP(X, (g) - 1); if ((g) == 4) PST(PW, i, 0); PIN(X); } while (0)
#define SMFIN(X, PW, i) do { AP(X, 7); PST(PW, i, 1); bad |= !(pa <= PSLIM); lold[i] = l[i]; l[i] += pa; PIN(l[i]); } while (0)
#define QK(S, i, j) S = MFMA(kf[j], qf[i][j], (j) ? S : negm[i])
        u32x4 w = {};
        int tf = 9;
#pragma unroll 1
        for (int t = 1; t < 8; ++t) {
            const lds_ptr pw = LW + POFF + w4 * 12288 + (t & 1) * 6144 + lane * 16;
            const lds_ptr pc = LW + POFF + w4 * 12288 + ((t & 1) ^ 1) * 6144 + lane * 16;
            f32x16 sa, sb; float pa; bool bad = false;
            const unsigned flv = flagw[t - 1];
            SBAR();
            QK(sa, 0, 0); SMS(sc, pc, 2, 0); SBAR(); QK(sa, 0, 1); SMS(sc, pc, 2, 1); SBAR(); QK(sa, 0, 2); SMS(sc, pc, 2, 2); SBAR();
            if (__builtin_expect(__builtin_amdgcn_readfirstlane(flv) != 0u, 0)) { tf = t - 1; break; }
            QK(sa, 0, 3); SMS(sc, pc, 2, 3); SBAR(); QK(sa, 0, 4); SMS(sc, pc, 2, 4); SBAR(); QK(sa, 0, 5); SMS(sc, pc, 2, 5); SBAR();
            QK(sa, 0, 6); SMS(sc, pc, 2, 6); SBAR(); QK(sa, 0, 7); SMS(sc, pc, 2, 7); SBAR();
            QK(sb, 1, 0); SMFIN(sc, pc, 2); KADDR(t < 7 ? t + 1 : 7); SBAR();
            QK(sb, 1, 1); SBAR(); SMS(sa, pw, 0, 0); SBAR(); QK(sb, 1, 2); SMS(sa, pw, 0, 1); SBAR(); QK(sb, 1, 3); SMS(sa, pw, 0, 2); SBAR();
            QK(sb, 1, 4); SMS(sa, pw, 0, 3); SBAR(); QK(sb, 1, 5); SMS(sa, pw, 0, 4); SBAR(); QK(sb, 1, 6); SMS(sa, pw, 0, 5); SBAR(); QK(sb, 1, 7); SMS(sa, pw, 0, 6); SBAR();
            QK(sc, 2, 0); SBAR(); KRD(0); SMS(sa, pw, 0, 7); SBAR(); QK(sc, 2, 1); SBAR(); KRD(1); SMFIN(sa, pw, 0); SBAR(); SMS(sb, pw, 1, 0); SBAR();
            QK(sc, 2, 2); SBAR(); KRD(2); SMS(sb, pw, 1, 1); SBAR(); QK(sc, 2, 3); SBAR(); KRD(3); SMS(sb, pw, 1, 2); SBAR();
            QK(sc, 2, 4); SBAR(); KRD(4); SMS(sb, pw, 1, 3); SBAR(); QK(sc, 2, 5); SBAR(); KRD(5); SMS(sb, pw, 1, 4); SBAR();
            QK(sc, 2, 6); SBAR(); KRD(6); SMS(sb, pw, 1, 5); SBAR(); QK(sc, 2, 7); SBAR(); KRD(7); SMS(sb, pw, 1, 6); SBAR();
            SMS(sb, pw, 1, 7); SBAR(); SMFIN(sb, pw, 1); SBAR();
            if (__builtin_expect(__any(bad), 0)) { if (lane == 0) flagw[t] = 1u; }
            if (t == 7) { lsum[(w4 * 3 + 0) * 32 + r32] = xhalf_sum(l[0]); lsum[(w4 * 3 + 1) * 32 + r32] = xhalf_sum(l[1]); }
            BAR_L(); SBAR();
            if (t == 1) TS(9); if (t == 3) TS(10); if (t == 5) TS(11);
        }
        if (tf == 9) {
            if (__builtin_amdgcn_readfirstlane(flagw[7]) != 0u) tf = 7;
            else {
                const lds_ptr pc = LW + POFF + w4 * 12288 + 6144 + lane * 16;
                float pa; bool bad = false;
                lold[0] = l[0]; lold[1] = l[1];
                SMS(sc, pc, 2, 0); SMS(sc, pc, 2, 1); SMS(sc, pc, 2, 2); SMS(sc, pc, 2, 3); SMS(sc, pc, 2, 4); SMS(sc, pc, 2, 5); SMS(sc, pc, 2, 6); SMS(sc, pc, 2, 7); SMFIN(sc, pc, 2);
                if (__builtin_expect(__any(bad), 0)) { if (lane == 0) flagw[8] = 1u; }
                lsum[(w4 * 3 + 2) * 32 + r32] = xhalf_sum(l[2]);
                BAR_L();
                if (__builtin_amdgcn_readfirstlane(flagw[8]) != 0u) tf = 8;
            }
        }
        slow = tf < 9;
        if (__builtin_expect(slow, 0)) {
#pragma unroll
            for (int i = 0; i < 3; ++i) l[i] = lold[i];
            BAR_L();
#pragma unroll 1
            for (int t = tf - 1; t < 8; ++t) {
                const bool all = t >= tf;
                const lds_ptr pw = LW + POFF + w4 * 12288 + (t & 1) * 6144 + lane * 16;
#pragma unroll
                for (int j = 0; j < 8; ++j) { const int s = (j < 4) ? 4 * kh + j : 4 * (1 - kh) + (j - 4);
                    kf[j] = *(const bf16x8*)(Kg + (size_t)(t * 64 + krow) * DH + s * 16 + hh * 8); }
                f32x16 s0[3]; float d[3];
#pragma unroll
                for (int i = 0; i < 3; ++i) {
                    s0[i] = f32x16{};
#pragma unroll
                    for (int j = 0; j < 8; ++j) s0[i] = MFMA(kf[j], qf[i][j], s0[i]);
                    float pm = s0[i][0];
#pragma unroll
                    for (int r = 1; r < 16; ++r) pm = fmaxf(pm, s0[i][r]);
                    d[i] = (all || i == 2) ? fmaxf(xhalf_max(pm) - m[i], 0.f) : 0.f;
                    prop[(w4 * 3 + i) * 32 + r32] = d[i];
                }
                BAR_L();
#pragma unroll
                for (int i = 0; i < 3; ++i) {
                    const float dl = fmaxf(d[i], prop[((w4 ^ 2) * 3 + i) * 32 + r32]);
                    m[i] += dl;
                    float psum = l[i] * __builtin_amdgcn_exp2f(-dl);
#pragma unroll
                    for (int r = 0; r < 16; ++r) { s0[i][r] = (all || i == 2) ? __builtin_amdgcn_exp2f(s0[i][r] - m[i]) : 0.f; psum += s0[i][r]; }
                    l[i] = psum;
#pragma unroll
                    for (int s = 0; s < 2; ++s) { u32x4 w2;
#pragma unroll
                        for (int e = 0; e < 4; ++e) w2[e] = cvtpk(s0[i][8 * s + 2 * e], s0[i][8 * s + 2 * e + 1]);
                        *(LAS u32x4*)(pw + (i * 2 + s) * 1024) = w2; }
                    lsum[(w4 * 3 + i) * 32 + r32] = xhalf_sum(psum);
                }
                BAR_L();
            }
            BAR_L();
        }
        TS(2);
        BAR_L();
    } else {
        const srd_t srdK = __builtin_amdgcn_make_buffer_rsrc((void*)Kg, (short)0, SK * DH * 2, 0x00020000);
        const srd_t srdV = __builtin_amdgcn_make_buffer_rsrc((void*)Vg, (short)0, SK * DH * 2, 0x00020000);
        const int arow = 8 * (w4 >> 1) + ((lane >> 2) & 7), ach = 4 * (2 * (w4 & 1) + hh) + (p4 ^ ((2 * (w4 >> 1) + blk) & 3));
        const unsigned kvoff = arow * 256 + ach * 16;
        auto kv_unit = [&](bool isV, int t) {
#pragma unroll
            for (int i = 0; i < 4; ++i)
                dma16<KV_NT>(isV ? srdV : srdK, kvoff, (unsigned)(t * 16384 + i * 4096), lds0 + (isV ? vslot(t) : kslot(t)) + (4 * i + w4) * 1024);
        };
        u32x4 pre[8];
        {
            const int rot = ROTF;
            const srd_t srdX = __builtin_amdgcn_make_buffer_rsrc((void*)(x + (size_t)b * SQ * DM), (short)0, SQ * DM * 2, 0x00020000);
            const srd_t srdW = __builtin_amdgcn_make_buffer_rsrc((void*)(wq + h * DH), (short)0, DM * HD * 2 - h * DH * 2, 0x00020000);
            const unsigned xvoff = (8 * w4 + (lane >> 3)) * (DM * 2) + (((lane & 7) ^ ((4 * (w4 & 1) + (lane >> 4)) & 7)) << 4);
            const unsigned wvoff = arow * (HD * 2) + ach * 16;
            auto issue = [&](int s, int buf) __attribute__((always_inline)) {
                int cs = s + rot; if (cs >= 10) cs -= 9; if (s == 0) cs = 0;
                const unsigned dst = lds0 + RING0 + buf;
#pragma unroll
                for (int i = 0; i < 4; ++i) dma16<XW_NT>(srdW, wvoff, (unsigned)((cs * 64 + i * 16) * HD * 2), dst + RXB + (4 * i + w4) * 1024);
#pragma unroll
                for (int i = 0; i < 6; ++i) dma16<XW_NT>(srdX, xvoff, (unsigned)(i * 32 * DM * 2 + cs * 128), dst + (4 * i + w4) * 1024);
            };
            if (wid == 4 && lane < 16) flagw[lane] = 0u;
            issue(0, 0); issue(1, RSTG);
            int rb = 2 * RSTG;
#pragma unroll 1
            for (int s = 0; s < 8; ++s) { WAITV(10); BAR_L(); issue(s + 2, rb); rb = (rb == 2 * RSTG) ? 0 : rb + RSTG; }
            kv_unit(false, 0); kv_unit(false, 1);
#pragma unroll
            for (int n = 0; n < 8; ++n) ld16_async<KV_NT>(pre[n], n < 4 ? srdV : srdK, kvoff, (unsigned)((n < 4 ? 0 : 2) * 16384 + (n & 3) * 4096));
            WAITV(26); BAR_L(); WAITV(16); BAR_L();
            BAR_L();
            kv_unit(true, 1); kv_unit(false, 3);
            WAITV(16);
            BAR_L();
        }
        f32x16 o[3][2];
#pragma unroll
        for (int i = 0; i < 3; ++i) { o[i][0] = f32x16{}; o[i][1] = f32x16{}; }
        const int vb0 = 1024 * kh + 64 * (4 * hh + q4) + (((2 * blk + (p4 >> 1)) ^ hh) << 4) + 8 * (p4 & 1);
        const int vb1 = 1024 * kh + 2048 + 64 * (4 * hh + q4) + (((2 * blk + (p4 >> 1)) ^ (2 + hh)) << 4) + 8 * (p4 & 1);
        const lds_cptr pb0 = L + POFF + qh * 12288 + lane * 16;
        s16x4 vl[2][4][2], vh[2][4][2];
        bf16x8 pf[2][3];
        unsigned wv[5];
#pragma unroll
        for (int p = 0; p < 5; ++p) { const int o_ = p * 1024 + lane * 16, row = o_ / 160, ch = (o_ % 160) >> 4; wv[p] = row * (DM * 2) + ch * 16; }
        const srd_t srdO = __builtin_amdgcn_make_buffer_rsrc((void*)(ws + WS_O + (size_t)(b * NH + h) * 49152), (short)0, 49152, 0x00020000);
        u32x4 wr[10];
        const int wg0 = 10 + w4, wg1 = w4 < 2 ? 14 + w4 : 4 + w4;
        u32x4 ow[3][4];
        auto opack = [&](int i) __attribute__((always_inline)) {
            const float rl = 1.0f / (lsum[(qh * 3 + i) * 32 + r32] + lsum[((qh + 2) * 3 + i) * 32 + r32]);
#pragma unroll
            for (int db = 0; db < 2; ++db)
#pragma unroll
                for (int s = 0; s < 2; ++s) {
#pragma unroll
                    for (int e = 0; e < 4; ++e) ow[i][db * 2 + s][e] = cvtpk(o[i][db][8 * s + 2 * e] * rl, o[i][db][8 * s + 2 * e + 1] * rl);
                    st16_wt(ow[i][db * 2 + s], srdO, (r32 >> 4) * 4096 + hh * 256 + (r32 & 15) * 16, (3 * qh + i) * 8192 + (kh * 2 + db) * 1024 + s * 512); }
        };
        auto olds = [&](int i) __attribute__((always_inline)) {
#pragma unroll
            for (int db = 0; db < 2; ++db) { const lds_ptr p_ = LW + own_addr((3 * qh + i) * 4 + kh * 2 + db) + lane * 16;
                *(LAS u32x4*)p_ = ow[i][db * 2]; *(LAS u32x4*)(p_ + 1024) = ow[i][db * 2 + 1]; }
        };
#define RDV(X, vb, ks, db) do { vl[X][ks][db] = ldstr(vb + vb0 + (ks) * 4096 + (db) * 512); vh[X][ks][db] = ldstr(vb + vb1 + (ks) * 4096 + (db) * 512); } while (0)
#define RDP(buf, blk_, ks, i) pf[buf][i] = ldsr128(pb0 + ((blk_) & 1) * 6144 + ((ks) >> 1) * 24576 + ((i) * 2 + ((ks) & 1)) * 1024)
#define PVM(X, buf, ks, i, db) o[i][db] = MFMA(cat8(vl[X][ks][db], vh[X][ks][db]), pf[buf][i], o[i][db])
        auto period = [&](auto tag) __attribute__((always_inline)) -> bool {
            constexpr int P = decltype(tag)::value, X = P & 1;
            constexpr bool AB = P <= 8, C = P >= 2;
            const lds_cptr vb = L + vslot(AB ? P - 1 : 0);
            auto dm = [&](int n) __attribute__((always_inline)) {
                if constexpr (P <= 5) {
                    if (n < 4) dma16<KV_NT>(srdV, kvoff, (unsigned)((P + 1) * 16384 + n * 4096), lds0 + vslot(P + 1) + (4 * n + w4) * 1024);
                    else if constexpr (P < 5) dma16<KV_NT>(srdK, kvoff, (unsigned)((P + 3) * 16384 + (n - 4) * 4096), lds0 + kslot(P + 3) + (4 * (n - 4) + w4) * 1024);
                    else dma16<KV_NT>(srdV, kvoff, (unsigned)(7 * 16384 + (n - 4) * 4096), lds0 + vslot(7) + (4 * (n - 4) + w4) * 1024);
                } else if constexpr (P <= 8) {
                    const int g = (P == 6) ? 16 + w4 : (P == 7) ? w4 : (w4 < 2 ? 4 + w4 : 6 + w4);
                    if (n < 5) dma16<WO_NT>(srdWo, wv[n], wo_soff(g), lds0 + g * 5120 + n * 1024);
                }
            };
            auto rd = [&](int buf, int ks, int part) __attribute__((always_inline)) {
                if (part == 0) { if (C) RDP(buf, P - 2, ks, 2); }
                if (part == 1) { if (AB) RDV(X, vb, ks, 0); }
                if (part == 2) { if (AB) RDP(buf, P - 1, ks, 0); }
                if (part == 3) { if (AB) RDV(X, vb, ks, 1); }
                if (part == 4) { if (AB) RDP(buf, P - 1, ks, 1); }
            };
            auto wrl = [&](int n) __attribute__((always_inline)) {
                if constexpr (P == 6 || P == 7) { if (n < 5) ld16_async<false>(wr[(P - 6) * 5 + n], srdWo, wv[n], wo_soff(P == 6 ? wg0 : wg1)); }
            };
            const unsigned flv = flagw[P - 1];
            rd(0, 0, 0); rd(0, 0, 1); rd(0, 0, 2); rd(0, 0, 3); rd(0, 0, 4); SBAR();
            if (__builtin_expect(__builtin_amdgcn_readfirstlane(flv) != 0u, 0)) {
                if constexpr (P == 7 || P == 8) asm volatile("s_waitcnt vmcnt(0)" :: "v"(wr[0]), "v"(wr[1]), "v"(wr[2]), "v"(wr[3]), "v"(wr[4]) : "memory");
                if constexpr (P == 8) asm volatile("" :: "v"(wr[5]), "v"(wr[6]), "v"(wr[7]), "v"(wr[8]), "v"(wr[9]) : "memory");
                return true;
            }
            if constexpr (P == 9) { olds(0); olds(1); }
#pragma unroll
            for (int ks = 0; ks < 4; ++ks) {
                const int cb = ks & 1; const bool more = ks < 3;
                if (C) PVM(X ^ 1, cb, ks, 2, 0); if (more) rd(cb ^ 1, ks + 1, 0); SBAR();
                if (C) PVM(X ^ 1, cb, ks, 2, 1); if (more) rd(cb ^ 1, ks + 1, 1); wrl(2 * ks); SBAR();
                if (AB) PVM(X, cb, ks, 0, 0); if (more) rd(cb ^ 1, ks + 1, 2); SBAR();
                if (AB) PVM(X, cb, ks, 0, 1); if (more) rd(cb ^ 1, ks + 1, 3); dm(2 * ks); SBAR();
                if (AB) PVM(X, cb, ks, 1, 0); if (more) rd(cb ^ 1, ks + 1, 4); wrl(2 * ks + 1); SBAR();
                if (AB) PVM(X, cb, ks, 1, 1); dm(2 * ks + 1); SBAR();
            }
            if constexpr (P <= 5) WAITV(8); else if constexpr (P == 6) WAITV(14); else if constexpr (P == 7) WAITV(20);
            else if constexpr (P == 8) asm volatile("s_waitcnt vmcnt(5)" : "+v"(wr[0]), "+v"(wr[1]), "+v"(wr[2]), "+v"(wr[3]), "+v"(wr[4]), "+v"(wr[5]), "+v"(wr[6]), "+v"(wr[7]), "+v"(wr[8]), "+v"(wr[9]) :: "memory");
            if constexpr (P == 8) { opack(0); opack(1); }
            if constexpr (P <= 8) BAR_L();
            return false;
        };
        BAR_L();
        asm volatile("s_waitcnt vmcnt(8)" : "+v"(pre[0]), "+v"(pre[1]), "+v"(pre[2]), "+v"(pre[3]), "+v"(pre[4]), "+v"(pre[5]), "+v"(pre[6]), "+v"(pre[7]) :: "memory");
#pragma unroll
        for (int n = 0; n < 8; ++n) *(LAS u32x4*)(LW + slot_addr(2 + (n >> 2)) + (4 * (n & 3) + w4) * 1024 + lane * 16) = pre[n];
        BAR_L();
        int tf = 9;
#define PERIOD(P) if (tf == 9) { if (period(std::integral_constant<int, P>{})) tf = P - 1; }
        PERIOD(1) PERIOD(2) PERIOD(3) PERIOD(4) PERIOD(5) PERIOD(6) PERIOD(7) PERIOD(8) PERIOD(9)
        slow = tf < 9;
        if (__builtin_expect(slow, 0)) {
            WAITV(0); BAR_L();
#pragma unroll 1
            for (int t = tf - 1; t < 8; ++t) {
                const int sl = slot_addr(2 + (t & 1));
#pragma unroll
                for (int n = 0; n < 4; ++n) dma16<KV_NT>(srdV, kvoff, (unsigned)(t * 16384 + n * 4096), lds0 + sl + (4 * n + w4) * 1024);
                BAR_L();
#pragma unroll
                for (int i = 0; i < 3; ++i) {
                    const float dl = fmaxf(prop[(qh * 3 + i) * 32 + r32], prop[((qh + 2) * 3 + i) * 32 + r32]);
                    const float f = __builtin_amdgcn_exp2f(-dl);
#pragma unroll
                    for (int r = 0; r < 16; ++r) { o[i][0][r] *= f; o[i][1][r] *= f; }
                }
                WAITV(0);
                BAR_L();
                const lds_cptr vb = L + sl;
#pragma unroll
                for (int ks = 0; ks < 4; ++ks) {
                    RDV(0, vb, ks, 0); RDV(0, vb, ks, 1); RDP(0, t, ks, 0); RDP(0, t, ks, 1); RDP(0, t, ks, 2);
#pragma unroll
                    for (int i = 0; i < 3; ++i) { PVM(0, 0, ks, i, 0); PVM(0, 0, ks, i, 1); }
                }
            }
            BAR_L();
#pragma unroll
            for (int p = 0; p < 5; ++p) { wr[p] = __builtin_amdgcn_raw_buffer_load_b128(srdWo, wv[p], wo_soff(wg0), 0); wr[5 + p] = __builtin_amdgcn_raw_buffer_load_b128(srdWo, wv[p], wo_soff(wg1), 0); }
#pragma unroll
            for (int p = 0; p < 10; ++p) PIN(wr[p]);
            opack(0); opack(1); olds(0); olds(1);
        }
        TS(6);
        opack(2); olds(2);
        TS(3);
        if (wid == 4) {
            base = basew[lane < 12 ? lane : 0];
            const unsigned my1 = basew[hl], my2 = basew[8 + hg];
            echo_want = (lane < 8 ? my1 : my2) ^ ECHO_KEY;
        }
        TS(16);
        BAR_L();
#pragma unroll
        for (int p = 0; p < 5; ++p) { *(LAS u32x4*)(LW + wg0 * 5120 + p * 1024 + lane * 16) = wr[p]; *(LAS u32x4*)(LW + wg1 * 5120 + p * 1024 + lane * 16) = wr[5 + p]; }
    }
    int ln; asm volatile("v_mbcnt_lo_u32_b32 %0, -1, 0\n\tv_mbcnt_hi_u32_b32 %0, -1, %0" : "=v"(ln));
    const int qg = wid & 3, kh2 = wid >> 2;
    gu32* const pflag = PFLAG(ln);
    gu32* const pecho = E + PEER(ln) * 32 + (ln < 8 ? hl : 8 + hg);
    unsigned echo = 0;
    if (wid == 4) echo = __hip_atomic_load(pecho, RLX_AGENT);
    TS(17);
    if (__builtin_expect(slow, 0)) { if (wid < 4) { wo_groups(w4, 4, 4); wo_groups(w4 < 2 ? 4 + w4 : 6 + w4, 10, 8); wo_groups(16 + w4, 20, 4); } WAITV(0); BAR_L(); }
    {
        const int g = ln >> 4, qq = ln & 15;
        const srd_t srdOg = __builtin_amdgcn_make_buffer_rsrc((void*)(ws + WS_O + (size_t)(b * NH + hg * 8) * 49152), (short)0, 8 * 49152, 0x00020000);
        const unsigned ov0 = (g >> 1) * 1024 + (g & 1) * 512 + qq * 16, ov = ln * 16 + kh2 * 2048;
        constexpr int OW = OWIN;
        u32x4 of[OW][3];
#define LDO1(st, i) do { const int head_ = (hl + hdir * ((st) >> 1)) & 7, t16 = 3 * qg + (i); \
                of[(st) % OW][i] = __builtin_amdgcn_raw_buffer_load_b128(srdOg, ov, head_ * 49152 + t16 * 4096 + ((st) & 1) * 1024, 16); } while (0)
#define LDO(st) do { LDO1(st, 0); LDO1(st, 1); LDO1(st, 2); } while (0)
#pragma unroll
        for (int st = 0; st < 2; ++st)
#pragma unroll
            for (int i = 0; i < 3; ++i) { const int t16 = 3 * qg + i;
                of[st][i] = *(const LAS u32x4*)(L + own_addr((t16 >> 1) * 4 + kh2 * 2 + st) + (t16 & 1) * 256 + ov0); }
        f32x4 acc[5][3];
#pragma unroll
        for (int n = 0; n < 5; ++n)
#pragma unroll
            for (int i = 0; i < 3; ++i) acc[n][i] = f32x4{};
        lds_cptr wb[3];
        wb[0] = L + kh2 * 5120 + (16 * (g >> 1) + 4 * (g & 1) + (qq >> 2)) * 160 + (ln & 3) * 8;
        wb[1] = wb[0] + 61440; wb[2] = wb[0] + 122880;
        PIN(wb[1]); PIN(wb[2]);
        s16x4 wl[2][5], wh[2][5];
#define RDWO(buf, st, n) do { const lds_cptr p_ = wb[PAIR_OF_STEP[st] / 6] + (PAIR_OF_STEP[st] % 6) * 10240 + (n) * 32; wl[buf][n] = ldstr(p_); wh[buf][n] = ldstr(p_ + 1280); } while (0)
#pragma unroll
        for (int n = 0; n < 5; ++n) RDWO(0, 0, n);
#pragma unroll
        for (int st = 0; st < 16; ++st) {
            if (st == 1) {
                TS(18); WAITV(0); TS(19);
                BAR_L();
                TS(7);
                if (wid == 4) {
#pragma unroll 1
                    for (unsigned sp = 0; __any(ln < 12 && echo != echo_want) && sp < SPIN_CAP; ++sp) {
                        __builtin_amdgcn_s_sleep(2);
                        echo = __hip_atomic_load(pecho, RLX_AGENT);
                    }
                    if (ln == hl) __hip_atomic_store(pflag, base + 1u, RLX_AGENT);
                } else if (wid < 4) wo_groups(20 + w4, 32, 4);
                SBAR(); TS(20);
            }
            if (st == 2) {
                TS(21);
                if (wid == 4) {
#pragma unroll 1
                    for (unsigned sp = 0; sp < SPIN_CAP; ++sp) {
                        const unsigned v = __hip_atomic_load(pflag, RLX_AGENT);
                        if (!__any(ln < 8 && v == base)) break;
                        __builtin_amdgcn_s_sleep(1);
                    }
                }
                TS(22); WAITV(0); TS(23);
                BAR_L();
                TS(12);
                if (kh2) __builtin_amdgcn_s_setprio(1);
#pragma unroll
                for (int s2 = 2; s2 < 2 + OW - 1; ++s2) LDO(s2);
                SBAR();
            }
#pragma unroll
            for (int n = 0; n < 5; ++n) {
#pragma unroll
                for (int i = 0; i < 3; ++i) acc[n][i] = MFMA16(cat8(wl[st & 1][n], wh[st & 1][n]), __builtin_bit_cast(bf16x8, of[st % OW][i]), acc[n][i]);
                if (st < 15) RDWO((st + 1) & 1, st + 1, n);
                if (st >= 2 && st + OW - 1 < 16 && n < 3) LDO1(st + OW - 1, n);
                SBAR();
            }
            if (st == 3) TS(24);
            if (st == 10) { TS(25); BAR_L(); TS(26); }
            if (0) BAR_L();
        }
        TS(13);
#pragma unroll
        for (int tl = 0; tl < 15; ++tl)
            if ((tl < 8) != (kh2 == 0)) *(LAS f32x4*)(LW + ((qg * 15 + tl) * 64 + ln) * 16) = acc[tl / 3][tl % 3];
        BAR_L(); TS(27);
        const int dst = ((xcd & 4) | qg) + 8 * jj;
        const srd_t srdP = __builtin_amdgcn_make_buffer_rsrc((void*)(ws + WS_PART + (size_t)dst * 24576 + ((hg - qg - 1) & 3) * 8192), (short)0, 8192, 0x00020000);
#pragma unroll
        for (int u = 0; u < 8; ++u)
            if ((u < 4) == (kh2 == 0)) {
                u32x4 w = {};
#pragma unroll
                for (int k = 0; k < 2; ++k) { const int tl = 2 * u + k;
                    if (tl < 15) {
                        const f32x4 v = acc[tl / 3][tl % 3] + *(const LAS f32x4*)(L + ((qg * 15 + tl) * 64 + ln) * 16);
                        if (qg == hg) *(LAS f32x4*)(LW + ((qg * 15 + tl) * 64 + ln) * 16) = v;
                        w[2 * k] = cvtpk(v[0], v[1]); w[2 * k + 1] = cvtpk(v[2], v[3]);
                    } }
                if (qg != hg) st16_wt(w, srdP, ln * 16, u * 1024);
            }
        TS(28);
        WAITV(0);
        BAR_L();
        TS(14);
        if (wid == 4) {
            if (ln == 8 + hg) __hip_atomic_store(pflag, base + 1u, RLX_AGENT);
#pragma unroll 1
            for (unsigned sp = 0; sp < SPIN_CAP; ++sp) {
                const unsigned v = __hip_atomic_load(pflag, RLX_AGENT);
                if (!__any(ln >= 8 && ln < 12 && v == base)) break;
                __builtin_amdgcn_s_sleep(1);
            }
        }
        BAR_L();
        TS(29);
        const srd_t srdI = __builtin_amdgcn_make_buffer_rsrc((void*)(ws + WS_PART + (size_t)blockIdx.x * 24576), (short)0, 24576, 0x00020000);
        u32x4 pv_[3];
#pragma unroll
        for (int j = 0; j < 3; ++j) pv_[j] = __builtin_amdgcn_raw_buffer_load_b128(srdI, ln * 16, j * 8192 + wid * 1024, 16);
#pragma unroll
        for (int k = 0; k < 2; ++k) {
            const int tl = 2 * wid + k;
            if (tl < 15) {
                f32x4 sum = *(const LAS f32x4*)(L + ((hg * 15 + tl) * 64 + ln) * 16);
#pragma unroll
                for (int j = 0; j < 3; ++j) {
                    sum[0] += __uint_as_float(pv_[j][2 * k] << 16); sum[1] += __uint_as_float(pv_[j][2 * k] & 0xffff0000u);
                    sum[2] += __uint_as_float(pv_[j][2 * k + 1] << 16); sum[3] += __uint_as_float(pv_[j][2 * k + 1] & 0xffff0000u);
                }
                const int n = tl / 3, i = tl % 3;
                const int q = (3 * hg + i) * 16 + qq, col = hl * 80 + n * 16 + g * 4;
                uint2 w; w.x = cvtpk(sum[0], sum[1]); w.y = cvtpk(sum[2], sum[3]);
                *(uint2*)(out + (size_t)(b * SQ + q) * DM + col) = w;
            }
        }
        TS(15);
    }
}

extern "C" void kernel_launch(void* const* d_in, const int* in_sizes, int n_in, void* d_out, int out_size, void* d_ws, size_t ws_size, hipStream_t stream) {
    const bf16* x = (const bf16*)d_in[0]; const bf16* kc = (const bf16*)d_in[1]; const bf16* vc = (const bf16*)d_in[2];
    const bf16* wq = (const bf16*)d_in[3]; const bf16* wo = (const bf16*)d_in[4];
    (void)hipFuncSetAttribute((const void*)k1_attn, hipFuncAttributeMaxDynamicSharedMemorySize, LDS_BYTES);
    k1_attn<<<256, 512, LDS_BYTES, stream>>>(x, kc, vc, wq, wo, (char*)d_ws, (bf16*)d_out);
}
```

```cpp
#include <hip/hip_runtime.h>
#include <hip/hip_bf16.h>
#include <stdint.h>
#include <type_traits>

typedef __hip_bfloat16 bf16;
using bf16x8 = __attribute__((ext_vector_type(8))) short;
using s16x4  = __attribute__((ext_vector_type(4))) short;
using f32x16 = __attribute__((ext_vector_type(16))) float;
using f32x4  = __attribute__((ext_vector_type(4))) float;
using u32x4  = __attribute__((ext_vector_type(4))) unsigned;
typedef short v4i16_t __attribute__((ext_vector_type(4)));
#define LAS __attribute__((address_space(3)))
typedef LAS const char* lds_cptr;
typedef LAS char* lds_ptr;
typedef __amdgpu_buffer_rsrc_t srd_t;
#define DI __device__ __forceinline__
#define SBAR() __builtin_amdgcn_sched_barrier(0)
#define PIN(x) asm volatile("" : "+v"(x))
#define MFMA(a, b, c) __builtin_amdgcn_mfma_f32_32x32x16_bf16(a, b, c, 0, 0, 0)

constexpr int NB = 8, SQ = 192, DM = 640, NH = 32, SK = 512, DH = 128, HD = NH * DH;
constexpr float QSCALE = 0.08838834764831845f * 1.4426950408889634f;
#define PSLIM 65536.0f

#define TS(i)

typedef float f32x2 __attribute__((ext_vector_type(2)));
typedef __bf16 bf16x2 __attribute__((ext_vector_type(2)));
DI unsigned cvtpk(float lo, float hi) { f32x2 v = {lo, hi}; bf16x2 b = __builtin_convertvector(v, bf16x2); return __builtin_bit_cast(unsigned, b); }
DI float fmul_s(float a, float b) { return a * b; }
template <bool NT>
DI void dma16(srd_t srd, unsigned voff, unsigned soff, unsigned lds_dst_) {
    const unsigned lds_dst = __builtin_amdgcn_readfirstlane(lds_dst_);
    if constexpr (NT) asm volatile("s_mov_b32 m0, %3\n\ts_nop 0\n\tbuffer_load_dwordx4 %0, %1, %2 offen nt lds" :: "v"(voff), "s"(srd), "s"(soff), "s"(lds_dst) : "m0", "memory");
    else              asm volatile("s_mov_b32 m0, %3\n\ts_nop 0\n\tbuffer_load_dwordx4 %0, %1, %2 offen lds" :: "v"(voff), "s"(srd), "s"(soff), "s"(lds_dst) : "m0", "memory");
}
template <bool NT>
DI void ld16_async(u32x4& r, srd_t srd, unsigned voff, unsigned soff) {
    if constexpr (NT) asm volatile("buffer_load_dwordx4 %0, %1, %2, %3 offen nt" : "=v"(r) : "v"(voff), "s"(srd), "s"(soff) : "memory");
    else              asm volatile("buffer_load_dwordx4 %0, %1, %2, %3 offen" : "=v"(r) : "v"(voff), "s"(srd), "s"(soff) : "memory");
}
template <bool NT>
DI void touch4(srd_t srd, unsigned voff, unsigned soff, unsigned lds_dst) {
    if constexpr (NT) asm volatile("s_mov_b32 m0, %3\n\ts_nop 0\n\tbuffer_load_dword %0, %1, %2 offen nt lds" :: "v"(voff), "s"(srd), "s"(soff), "s"(lds_dst) : "m0", "memory");
    else              asm volatile("s_mov_b32 m0, %3\n\ts_nop 0\n\tbuffer_load_dword %0, %1, %2 offen lds" :: "v"(voff), "s"(srd), "s"(soff), "s"(lds_dst) : "m0", "memory");
}
DI bf16x8 ldsr128(lds_cptr p) { return *(const LAS bf16x8*)p; }
DI s16x4 ldstr(lds_cptr p) { return __builtin_bit_cast(s16x4, __builtin_amdgcn_ds_read_tr16_b64_v4i16((LAS v4i16_t*)p)); }
DI bf16x8 cat8(s16x4 a, s16x4 b) { return __builtin_shufflevector(a, b, 0, 1, 2, 3, 4, 5, 6, 7); }
#define WAITV(N) asm volatile("s_waitcnt vmcnt(" #N ")" ::: "memory")
#define BAR_L() asm volatile("s_waitcnt lgkmcnt(0)\n\ts_barrier" ::: "memory")
DI float xhalf_max(float v) { auto rr = __builtin_amdgcn_permlane32_swap(__float_as_uint(v), __float_as_uint(v), false, false); return fmaxf(__uint_as_float(rr[0]), __uint_as_float(rr[1])); }
DI float xhalf_sum(float v) { auto rr = __builtin_amdgcn_permlane32_swap(__float_as_uint(v), __float_as_uint(v), false, false); return __uint_as_float(rr[0]) + __uint_as_float(rr[1]); }

#define KV_NT true
#define XW_NT false

#define GAS __attribute__((address_space(1)))
typedef GAS unsigned gu32;
#define RLX_AGENT __ATOMIC_RELAXED, __HIP_MEMORY_SCOPE_AGENT
#define MFMA16(a, b, c) __builtin_amdgcn_mfma_f32_16x16x32_bf16(a, b, c, 0, 0, 0)
DI void st16_wt(u32x4 v, srd_t srd, unsigned voff, unsigned soff) {
    __builtin_amdgcn_raw_buffer_store_b128(v, srd, voff, soff, 16); SBAR(); asm volatile("s_nop 1"); SBAR();
}
constexpr unsigned ECHO_KEY = 0x5A17C3E9u;
constexpr unsigned SPIN_CAP = 1u << 20;
constexpr size_t WS_O = 0, WS_PART = 16u << 20, WS_SYNC = 48u << 20;
constexpr int PAIR_OF_STEP[16] = {8, 9, 0, 1, 2, 4, 5, 6, 7, 3, 10, 11, 12, 13, 14, 15};
constexpr unsigned long long STEP_OF_PAIR = 0xFEDCBA1087659432ull;
#define WO_NT false
#define TOUCH_NT false
#define CODE_TOUCH 30720
#define NO_SELFWARM
#define OWIN 5
#define COMMON0
#define ROTF ((((hl + 2 * bl) & 7) * 9) / 8)
#define HEADF(g_, l_) ((g_) * 8 + (l_))
#define KVB 0
#define WRS 2
#define TOUCH_P 99
constexpr int SLOT = 16384;
constexpr int MOFF = 32768;
constexpr int RING0 = 40960, RSTG = 40960, RXB = 24576;
constexpr int POFF = 106496;
constexpr int LDS_BYTES = 163840;
constexpr unsigned KSLOTS = 0x04215310u;
constexpr unsigned VSLOTS = 0x32153042u;
DI int slot_addr(unsigned s) { return s * SLOT + (s >= 2 ? 8192 : 0); }
DI int own_addr(int pr) { return pr < 20 ? POFF + (pr / 5) * 12288 + (pr % 5) * 2048 : 155648 + (pr - 20) * 2048; }
DI int kslot(int t) { return slot_addr((KSLOTS >> (4 * t)) & 15u); }
DI int vslot(int t) { return slot_addr((VSLOTS >> (4 * t)) & 15u); }
#define GROUPB(bl, hl) (((bl) + (hl)) & 1)

__global__ __launch_bounds__(512, 2) void k1_attn(const bf16* __restrict__ x, const bf16* __restrict__ kc, const bf16* __restrict__ vc,
                                                  const bf16* __restrict__ wq, const bf16* __restrict__ wo, char* __restrict__ ws, bf16* __restrict__ out) {
    extern __shared__ __attribute__((aligned(16))) char lds[];
    TS(0);
    unsigned long long pc0; asm volatile("s_getpc_b64 %0" : "=s"(pc0));
    const int tid = threadIdx.x, lane = tid & 63, r32 = lane & 31, hh = lane >> 5;
    const int wid = __builtin_amdgcn_readfirstlane(tid >> 6), w4 = wid & 3;
    const int xcd = blockIdx.x & 7, jj = blockIdx.x >> 3;
    const int bl = jj >> 3, hl = jj & 7;
    const int b = (xcd >> 2) * 4 + bl, h = HEADF(xcd & 3, hl);
    const unsigned lds0 = (unsigned)(uintptr_t)lds;
    const lds_cptr L = (lds_cptr)lds;
    const lds_ptr LW = (lds_ptr)lds;
    const bf16* Kg = kc + (size_t)(b * NH + h) * SK * DH;
    const bf16* Vg = vc + (size_t)(b * NH + h) * SK * DH;
    const int qh = w4 & 1, kh = w4 >> 1;
    const int q4 = (lane & 15) >> 2, p4 = lane & 3, blk = (lane >> 4) & 1;
    LAS unsigned* flagw = (LAS unsigned*)(LW + MOFF);
    LAS unsigned* basew = (LAS unsigned*)(LW + MOFF + 128);
    const int hg = xcd & 3;
    gu32* const F = (gu32*)(ws + WS_SYNC);
    gu32* const E = (gu32*)(ws + WS_SYNC + 65536);
#define PEER(l_) ((l_) < 8 ? xcd + 8 * (bl * 8 + (l_)) : ((xcd & 4) | ((l_) & 3)) + 8 * jj)
#define PFLAG(l_) (F + (((l_) < 8 ? 0 : 256) + PEER(l_)) * 32)
    LAS float* prop = (LAS float*)(LW + MOFF + 256);
    LAS float* lsum = (LAS float*)(LW + MOFF + 2048);

    const int hdir = (bl & 1) ? -1 : 1;
    const srd_t srdWo = __builtin_amdgcn_make_buffer_rsrc((void*)(wo + hl * 80), (short)0, HD * DM * 2 - hl * 160, 0x00020000);
    auto wo_soff = [&](int g) __attribute__((always_inline)) -> unsigned {
        const int st = (int)((STEP_OF_PAIR >> (4 * (g >> 1))) & 15u);
        return (unsigned)((HEADF(hg, (hl + hdir * (st >> 1)) & 7) * 128 + (2 * (g & 1) + (st & 1)) * 32) * DM * 2);
    };
    auto wo_groups = [&](int g0, int g1, int gs) __attribute__((always_inline)) {
        int l_; asm volatile("v_mbcnt_lo_u32_b32 %0, -1, 0\n\tv_mbcnt_hi_u32_b32 %0, -1, %0" : "=v"(l_));
        unsigned wv[5];
#pragma unroll
        for (int p = 0; p < 5; ++p) { const int o_ = p * 1024 + l_ * 16, row = o_ / 160, ch = (o_ % 160) >> 4; wv[p] = row * (DM * 2) + ch * 16; }
#pragma unroll 1
        for (int g = g0; g < g1; g += gs) {
            const unsigned so = wo_soff(g);
#pragma unroll
            for (int p = 0; p < 5; ++p) dma16<WO_NT>(srdWo, wv[p], so, lds0 + g * 5120 + p * 1024);
        }
    };
    bool slow = false;
    unsigned base = 0;

    if (wid < 4) {
        if (wid == 0) {
            if (lane < 12) {
                const unsigned base = __hip_atomic_load(PFLAG(lane), RLX_AGENT);
                basew[lane] = base;
                __hip_atomic_store(E + blockIdx.x * 32 + lane, base ^ ECHO_KEY, RLX_AGENT);
            }
        }
        if (wid == 1) {
            const srd_t srdC = __builtin_amdgcn_make_buffer_rsrc((void*)(pc0 & ~127ull), (short)0, CODE_TOUCH, 0x00020000);
#pragma unroll
            for (int k = 0; k < 4; ++k) touch4<false>(srdC, lane * 128, (unsigned)(k * 8192), lds0 + MOFF + 4096u);
        }
        bf16x8 qf[3][8];
        {
            f32x16 acc[3][2];
#pragma unroll
            for (int i = 0; i < 3; ++i) { acc[i][0] = f32x16{}; acc[i][1] = f32x16{}; }
            const int xsw = (r32 >> 1) & 7;
            int xa[4];
#pragma unroll
            for (int ks = 0; ks < 4; ++ks) xa[ks] = (96 * qh + r32) * 128 + (((2 * ks + hh) ^ xsw) << 4);
            const int ps = ((p4 & 1) << 1) | (p4 >> 1);
            const int wa0 = RXB + 2048 * hh + 1024 * kh + 64 * q4 + (((2 * blk + (ps >> 1)) ^ (2 * hh)) << 4) + 8 * (ps & 1);
            const int wa1 = RXB + 2048 * hh + 1024 * kh + 64 * q4 + 256 + (((2 * blk + (ps >> 1)) ^ (2 * hh + 1)) << 4) + 8 * (ps & 1);
            bf16x8 xf[2][3]; s16x4 wl[2][2], wh[2][2];
#define RDX(buf, base, ks, i) xf[buf][i] = ldsr128(base + xa[ks] + i * 4096)
#define RDW(buf, base, ks, db) do { wl[buf][db] = ldstr(base + wa0 + ks * 4096 + db * 512); wh[buf][db] = ldstr(base + wa1 + ks * 4096 + db * 512); } while (0)
#define MM(buf, i, db) acc[i][db] = MFMA(cat8(wl[buf][db], wh[buf][db]), xf[buf][i], acc[i][db])
#define HALF(cb, base, ks) do { \
                MM(cb, 0, 0); RDW(cb ^ 1, base, ks, 0); SBAR(); \
                MM(cb, 0, 1); RDX(cb ^ 1, base, ks, 0); SBAR(); \
                MM(cb, 1, 0); RDW(cb ^ 1, base, ks, 1); SBAR(); \
                MM(cb, 1, 1); RDX(cb ^ 1, base, ks, 1); SBAR(); \
                MM(cb, 2, 0); RDX(cb ^ 1, base, ks, 2); SBAR(); \
                MM(cb, 2, 1); SBAR(); } while (0)
            BAR_L();
            TS(4);
            { const lds_cptr base = L + RING0;
              RDW(0, base, 0, 0); RDX(0, base, 0, 0); RDW(0, base, 0, 1); RDX(0, base, 0, 1); RDX(0, base, 0, 2); SBAR();
              HALF(0, base, 1); HALF(1, base, 2); HALF(0, base, 3); }
            int rb = RSTG;
#pragma unroll 1
            for (int s = 1; s < 10; ++s) {
                SBAR(); BAR_L(); SBAR();
                const lds_cptr base = L + RING0 + rb;
                HALF(1, base, 0); HALF(0, base, 1); HALF(1, base, 2); HALF(0, base, 3);
                rb = (rb == 2 * RSTG) ? 0 : rb + RSTG;
            }
            BAR_L();
            MM(1, 0, 0); MM(1, 0, 1); MM(1, 1, 0); MM(1, 1, 1); MM(1, 2, 0); MM(1, 2, 1);
            TS(5);
#pragma unroll
            for (int i = 0; i < 3; ++i)
#pragma unroll
                for (int db = 0; db < 2; ++db)
#pragma unroll
                    for (int s = 0; s < 2; ++s) {
                        u32x4 w;
#pragma unroll
                        for (int e = 0; e < 4; ++e) w[e] = cvtpk(acc[i][db][8 * s + 2 * e] * QSCALE, acc[i][db][8 * s + 2 * e + 1] * QSCALE);
                        *(LAS u32x4*)(LW + POFF + (w4 ^ 2) * 12288 + (i * 4 + db * 2 + s) * 1024 + lane * 16) = w;
                        qf[i][db * 2 + s] = __builtin_bit_cast(bf16x8, w);
                    }
            BAR_L();
#pragma unroll
            for (int i = 0; i < 3; ++i)
#pragma unroll
                for (int j = 0; j < 4; ++j) qf[i][4 + j] = ldsr128(L + POFF + w4 * 12288 + (i * 4 + j) * 1024 + lane * 16);
        }
        TS(1);
        const int krow = 32 * kh + r32;
        const int kb0 = 2048 * (krow >> 3) + 64 * (krow & 7) + ((hh ^ ((krow >> 2) & 3)) << 4);
        const int kb1 = 2048 * (krow >> 3) + 64 * (krow & 7) + (((2 + hh) ^ ((krow >> 2) & 3)) << 4);
        const int kown = kh * 1024, koth = (1 - kh) * 1024;
        bf16x8 kf[8];
        unsigned ka[4];
#define KRD(j) asm volatile("ds_read_b128 %0, %1 offset:%2" : "+v"(kf[j]) : "v"(ka[((j) >> 2) * 2 + ((j) & 1)]), "i"(512 * (((j) >> 1) & 1)) : "memory")
#define KADDR(t_) do { const unsigned kb_ = lds0 + kslot(t_); ka[0] = kb_ + kown + kb0; ka[1] = kb_ + kown + kb1; ka[2] = kb_ + koth + kb0; ka[3] = kb_ + koth + kb1; } while (0)
#define KRDALL() do { KRD(0); KRD(1); KRD(2); KRD(3); KRD(4); KRD(5); KRD(6); KRD(7); } while (0)
        f32x16 negm[3], sc;
        float l[3], m[3], lold[3];
#pragma unroll
        for (int j = 0; j < 8; ++j) kf[j] = bf16x8{};
        KADDR(0); KRDALL();
        asm volatile("s_waitcnt lgkmcnt(0)" ::: "memory"); SBAR();
        {
            const lds_ptr pw = LW + POFF + w4 * 12288 + lane * 16;
            f32x16 s0[3]; float d[3];
#pragma unroll
            for (int i = 0; i < 3; ++i) {
                s0[i] = f32x16{};
#pragma unroll
                for (int j = 0; j < 8; ++j) s0[i] = MFMA(kf[j], qf[i][j], s0[i]);
            }
            SBAR();
            KADDR(1); KRDALL();
#pragma unroll
            for (int i = 0; i < 3; ++i) {
                float pm = s0[i][0];
#pragma unroll
                for (int r = 1; r < 16; ++r) pm = fmaxf(pm, s0[i][r]);
                d[i] = xhalf_max(pm);
                prop[(w4 * 3 + i) * 32 + r32] = d[i];
            }
            BAR_L();
#pragma unroll
            for (int i = 0; i < 3; ++i) {
                m[i] = fmaxf(d[i], prop[((w4 ^ 2) * 3 + i) * 32 + r32]);
                float psum = 0.f;
#pragma unroll
                for (int r = 0; r < 16; ++r) { negm[i][r] = -m[i]; s0[i][r] -= m[i]; }
                if (i < 2) {
#pragma unroll
                    for (int r = 0; r < 16; ++r) { s0[i][r] = __builtin_amdgcn_exp2f(s0[i][r]); psum += s0[i][r]; }
#pragma unroll
                    for (int s = 0; s < 2; ++s) { u32x4 w;
#pragma unroll
                        for (int e = 0; e < 4; ++e) w[e] = cvtpk(s0[i][8 * s + 2 * e], s0[i][8 * s + 2 * e + 1]);
                        *(LAS u32x4*)(pw + (i * 2 + s) * 1024) = w; }
                }
                l[i] = psum; lold[i] = psum;
            }
            sc = s0[2];
            BAR_L(); SBAR();
        }
        TS(8);
#define EX2(X, g) do { X[2 * (g)] = __builtin_amdgcn_exp2f(X[2 * (g)]); X[2 * (g) + 1] = __builtin_amdgcn_exp2f(X[2 * (g) + 1]); } while (0)
#define AP(X, g) do { if ((g) == 0) pa = X[0] + X[1]; else { pa += X[2 * (g)]; pa += X[2 * (g) + 1]; } w[(g) & 3] = cvtpk(X[2 * (g)], X[2 * (g) + 1]); PIN(pa); PIN(w); } while (0)
#define PST(PW, i, s) *(LAS u32x4*)(PW + ((i) * 2 + (s)) * 1024) = w
#define SMS(X, PW, i, g) do { EX2(X, g); if ((g) > 0) AP(X, (g) - 1); if ((g) == 4) PST(PW, i, 0); PIN(X); } while (0)
#define SMFIN(X, PW, i) do { AP(X, 7); PST(PW, i, 1); bad |= !(pa <= PSLIM); lold[i] = l[i]; l[i] += pa; PIN(l[i]); } while (0)
#define QK(S, i, j) S = MFMA(kf[j], qf[i][j], (j) ? S : negm[i])
        u32x4 w = {};
        int tf = 9;
#pragma unroll 1
        for (int t = 1; t < 8; ++t) {
            const lds_ptr pw = LW + POFF + w4 * 12288 + (t & 1) * 6144 + lane * 16;
            const lds_ptr pc = LW + POFF + w4 * 12288 + ((t & 1) ^ 1) * 6144 + lane * 16;
            f32x16 sa, sb; float pa; bool bad = false;
            const unsigned flv = flagw[t - 1];
            SBAR();
            QK(sa, 0, 0); SMS(sc, pc, 2, 0); SBAR(); QK(sa, 0, 1); SMS(sc, pc, 2, 1); SBAR(); QK(sa, 0, 2); SMS(sc, pc, 2, 2); SBAR();
            if (__builtin_expect(__builtin_amdgcn_readfirstlane(flv) != 0u, 0)) { tf = t - 1; break; }
            QK(sa, 0, 3); SMS(sc, pc, 2, 3); SBAR(); QK(sa, 0, 4); SMS(sc, pc, 2, 4); SBAR(); QK(sa, 0, 5); SMS(sc, pc, 2, 5); SBAR();
            QK(sa, 0, 6); SMS(sc, pc, 2, 6); SBAR(); QK(sa, 0, 7); SMS(sc, pc, 2, 7); SBAR();
            QK(sb, 1, 0); SMFIN(sc, pc, 2); KADDR(t < 7 ? t + 1 : 7); SBAR();
            QK(sb, 1, 1); SBAR(); SMS(sa, pw, 0, 0); SBAR(); QK(sb, 1, 2); SMS(sa, pw, 0, 1); SBAR(); QK(sb, 1, 3); SMS(sa, pw, 0, 2); SBAR();
            QK(sb, 1, 4); SMS(sa, pw, 0, 3); SBAR(); QK(sb, 1, 5); SMS(sa, pw, 0, 4); SBAR(); QK(sb, 1, 6); SMS(sa, pw, 0, 5); SBAR(); QK(sb, 1, 7); SMS(sa, pw, 0, 6); SBAR();
            QK(sc, 2, 0); SBAR(); KRD(0); SMS(sa, pw, 0, 7); SBAR(); QK(sc, 2, 1); SBAR(); KRD(1); SMFIN(sa, pw, 0); SBAR(); SMS(sb, pw, 1, 0); SBAR();
            QK(sc, 2, 2); SBAR(); KRD(2); SMS(sb, pw, 1, 1); SBAR(); QK(sc, 2, 3); SBAR(); KRD(3); SMS(sb, pw, 1, 2); SBAR();
            QK(sc, 2, 4); SBAR(); KRD(4); SMS(sb, pw, 1, 3); SBAR(); QK(sc, 2, 5); SBAR(); KRD(5); SMS(sb, pw, 1, 4); SBAR();
            QK(sc, 2, 6); SBAR(); KRD(6); SMS(sb, pw, 1, 5); SBAR(); QK(sc, 2, 7); SBAR(); KRD(7); SMS(sb, pw, 1, 6); SBAR();
            SMS(sb, pw, 1, 7); SBAR(); SMFIN(sb, pw, 1); SBAR();
            if (__builtin_expect(__any(bad), 0)) { if (lane == 0) flagw[t] = 1u; }
            if (t == 7) { lsum[(w4 * 3 + 0) * 32 + r32] = xhalf_sum(l[0]); lsum[(w4 * 3 + 1) * 32 + r32] = xhalf_sum(l[1]); }
            BAR_L(); SBAR();
            if (t == 1) TS(9); if (t == 3) TS(10); if (t == 5) TS(11);
        }
        if (tf == 9) {
            if (__builtin_amdgcn_readfirstlane(flagw[7]) != 0u) tf = 7;
            else {
                const lds_ptr pc = LW + POFF + w4 * 12288 + 6144 + lane * 16;
                float pa; bool bad = false;
                lold[0] = l[0]; lold[1] = l[1];
                SMS(sc, pc, 2, 0); SMS(sc, pc, 2, 1); SMS(sc, pc, 2, 2); SMS(sc, pc, 2, 3); SMS(sc, pc, 2, 4); SMS(sc, pc, 2, 5); SMS(sc, pc, 2, 6); SMS(sc, pc, 2, 7); SMFIN(sc, pc, 2);
                if (__builtin_expect(__any(bad), 0)) { if (lane == 0) flagw[8] = 1u; }
                lsum[(w4 * 3 + 2) * 32 + r32] = xhalf_sum(l[2]);
                BAR_L();
                if (__builtin_amdgcn_readfirstlane(flagw[8]) != 0u) tf = 8;
            }
        }
        slow = tf < 9;
        if (__builtin_expect(slow, 0)) {
#pragma unroll
            for (int i = 0; i < 3; ++i) l[i] = lold[i];
            BAR_L();
#pragma unroll 1
            for (int t = tf - 1; t < 8; ++t) {
                const bool all = t >= tf;
                const lds_ptr pw = LW + POFF + w4 * 12288 + (t & 1) * 6144 + lane * 16;
#pragma unroll
                for (int j = 0; j < 8; ++j) { const int s = (j < 4) ? 4 * kh + j : 4 * (1 - kh) + (j - 4);
                    kf[j] = *(const bf16x8*)(Kg + (size_t)(t * 64 + krow) * DH + s * 16 + hh * 8); }
                f32x16 s0[3]; float d[3];
#pragma unroll
                for (int i = 0; i < 3; ++i) {
                    s0[i] = f32x16{};
#pragma unroll
                    for (int j = 0; j < 8; ++j) s0[i] = MFMA(kf[j], qf[i][j], s0[i]);
                    float pm = s0[i][0];
#pragma unroll
                    for (int r = 1; r < 16; ++r) pm = fmaxf(pm, s0[i][r]);
                    d[i] = (all || i == 2) ? fmaxf(xhalf_max(pm) - m[i], 0.f) : 0.f;
                    prop[(w4 * 3 + i) * 32 + r32] = d[i];
                }
                BAR_L();
#pragma unroll
                for (int i = 0; i < 3; ++i) {
                    const float dl = fmaxf(d[i], prop[((w4 ^ 2) * 3 + i) * 32 + r32]);
                    m[i] += dl;
                    float psum = l[i] * __builtin_amdgcn_exp2f(-dl);
#pragma unroll
                    for (int r = 0; r < 16; ++r) { s0[i][r] = (all || i == 2) ? __builtin_amdgcn_exp2f(s0[i][r] - m[i]) : 0.f; psum += s0[i][r]; }
                    l[i] = psum;
#pragma unroll
                    for (int s = 0; s < 2; ++s) { u32x4 w2;
#pragma unroll
                        for (int e = 0; e < 4; ++e) w2[e] = cvtpk(s0[i][8 * s + 2 * e], s0[i][8 * s + 2 * e + 1]);
                        *(LAS u32x4*)(pw + (i * 2 + s) * 1024) = w2; }
                    lsum[(w4 * 3 + i) * 32 + r32] = xhalf_sum(psum);
                }
                BAR_L();
            }
            BAR_L();
        }
        if (wid == 0) {
            base = basew[lane < 12 ? lane : 0];
            const unsigned want = (lane < 8 ? basew[hl] : basew[8 + hg]) ^ ECHO_KEY;
            gu32* const pecho = E + PEER(lane) * 32 + (lane < 8 ? hl : 8 + hg);
#pragma unroll 1
            for (unsigned sp = 0; sp < SPIN_CAP; ++sp) {
                const unsigned echo = __hip_atomic_load(pecho, RLX_AGENT);
                if (!__any(lane < 12 && echo != want)) break;
                __builtin_amdgcn_s_sleep(2);
            }
        }
        TS(2);
        BAR_L();
    } else {
        const srd_t srdK = __builtin_amdgcn_make_buffer_rsrc((void*)Kg, (short)0, SK * DH * 2, 0x00020000);
        const srd_t srdV = __builtin_amdgcn_make_buffer_rsrc((void*)Vg, (short)0, SK * DH * 2, 0x00020000);
        const int arow = 8 * (w4 >> 1) + ((lane >> 2) & 7), ach = 4 * (2 * (w4 & 1) + hh) + (p4 ^ ((2 * (w4 >> 1) + blk) & 3));
        const unsigned kvoff = arow * 256 + ach * 16;
        auto kv_unit = [&](bool isV, int t) {
#pragma unroll
            for (int i = 0; i < 4; ++i)
                dma16<KV_NT>(isV ? srdV : srdK, kvoff, (unsigned)(t * 16384 + i * 4096), lds0 + (isV ? vslot(t) : kslot(t)) + (4 * i + w4) * 1024);
        };
        u32x4 pre[8];
        {
            const int rot = ROTF;
            const srd_t srdX = __builtin_amdgcn_make_buffer_rsrc((void*)(x + (size_t)b * SQ * DM), (short)0, SQ * DM * 2, 0x00020000);
            const srd_t srdW = __builtin_amdgcn_make_buffer_rsrc((void*)(wq + h * DH), (short)0, DM * HD * 2 - h * DH * 2, 0x00020000);
            const unsigned xvoff = (8 * w4 + (lane >> 3)) * (DM * 2) + (((lane & 7) ^ ((4 * (w4 & 1) + (lane >> 4)) & 7)) << 4);
            const unsigned wvoff = arow * (HD * 2) + ach * 16;
            auto issue = [&](int s, int buf) __attribute__((always_inline)) {
                int cs = s + rot; if (cs >= 10) cs -= 9; if (s == 0) cs = 0;
                const unsigned dst = lds0 + RING0 + buf;
#pragma unroll
                for (int i = 0; i < 4; ++i) dma16<XW_NT>(srdW, wvoff, (unsigned)((cs * 64 + i * 16) * HD * 2), dst + RXB + (4 * i + w4) * 1024);
#pragma unroll
                for (int i = 0; i < 6; ++i) dma16<XW_NT>(srdX, xvoff, (unsigned)(i * 32 * DM * 2 + cs * 128), dst + (4 * i + w4) * 1024);
            };
            if (wid == 4 && lane < 16) flagw[lane] = 0u;
            issue(0, 0); issue(1, RSTG);
            int rb = 2 * RSTG;
#pragma unroll 1
            for (int s = 0; s < 8; ++s) { WAITV(10); BAR_L(); issue(s + 2, rb); rb = (rb == 2 * RSTG) ? 0 : rb + RSTG; }
#define PRELD(n0, n1) _Pragma("unroll") for (int n = n0; n < n1; ++n) ld16_async<KV_NT>(pre[n], n < 4 ? srdV : srdK, kvoff, (unsigned)((n < 4 ? 0 : 2) * 16384 + (n & 3) * 4096))
            kv_unit(false, 0);
            WAITV(14); BAR_L(); kv_unit(false, 1); PRELD(0, 4);
            WAITV(12); BAR_L(); PRELD(4, 8);
            BAR_L();
            kv_unit(true, 1); kv_unit(false, 3);
            WAITV(16);
            BAR_L();
        }
        f32x16 o[3][2];
#pragma unroll
        for (int i = 0; i < 3; ++i) { o[i][0] = f32x16{}; o[i][1] = f32x16{}; }
        const int vb0 = 1024 * kh + 64 * (4 * hh + q4) + (((2 * blk + (p4 >> 1)) ^ hh) << 4) + 8 * (p4 & 1);
        const int vb1 = 1024 * kh + 2048 + 64 * (4 * hh + q4) + (((2 * blk + (p4 >> 1)) ^ (2 + hh)) << 4) + 8 * (p4 & 1);
        const lds_cptr pb0 = L + POFF + qh * 12288 + lane * 16;
        s16x4 vl[2][4][2], vh[2][4][2];
        bf16x8 pf[2][3];
        unsigned wv[5];
#pragma unroll
        for (int p = 0; p < 5; ++p) { const int o_ = p * 1024 + lane * 16, row = o_ / 160, ch = (o_ % 160) >> 4; wv[p] = row * (DM * 2) + ch * 16; }
        const srd_t srdO = __builtin_amdgcn_make_buffer_rsrc((void*)(ws + WS_O + (size_t)(b * NH + hg * 8 + hl) * 49152), (short)0, 49152, 0x00020000);
        u32x4 wr[10];
        const int wg0 = 10 + w4, wg1 = w4 < 2 ? 14 + w4 : 4 + w4;
        u32x4 ow[3][4];
        auto opack = [&](int i) __attribute__((always_inline)) {
            const float rl = 1.0f / (lsum[(qh * 3 + i) * 32 + r32] + lsum[((qh + 2) * 3 + i) * 32 + r32]);
#pragma unroll
            for (int db = 0; db < 2; ++db)
#pragma unroll
                for (int s = 0; s < 2; ++s) {
#pragma unroll
                    for (int e = 0; e < 4; ++e) ow[i][db * 2 + s][e] = cvtpk(o[i][db][8 * s + 2 * e] * rl, o[i][db][8 * s + 2 * e + 1] * rl);
                    st16_wt(ow[i][db * 2 + s], srdO, (r32 >> 4) * 4096 + hh * 256 + (r32 & 15) * 16, (3 * qh + i) * 8192 + (kh * 2 + db) * 1024 + s * 512); }
        };
        auto olds = [&](int i) __attribute__((always_inline)) {
#pragma unroll
            for (int db = 0; db < 2; ++db) { const lds_ptr p_ = LW + own_addr((3 * qh + i) * 4 + kh * 2 + db) + lane * 16;
                *(LAS u32x4*)p_ = ow[i][db * 2]; *(LAS u32x4*)(p_ + 1024) = ow[i][db * 2 + 1]; }
        };
#define RDV(X, vb, ks, db) do { vl[X][ks][db] = ldstr(vb + vb0 + (ks) * 4096 + (db) * 512); vh[X][ks][db] = ldstr(vb + vb1 + (ks) * 4096 + (db) * 512); } while (0)
#define RDP(buf, blk_, ks, i) pf[buf][i] = ldsr128(pb0 + ((blk_) & 1) * 6144 + ((ks) >> 1) * 24576 + ((i) * 2 + ((ks) & 1)) * 1024)
#define PVM(X, buf, ks, i, db) o[i][db] = MFMA(cat8(vl[X][ks][db], vh[X][ks][db]), pf[buf][i], o[i][db])
        auto period = [&](auto tag) __attribute__((always_inline)) -> bool {
            constexpr int P = decltype(tag)::value, X = P & 1;
            constexpr bool AB = P <= 8, C = P >= 2;
            const lds_cptr vb = L + vslot(AB ? P - 1 : 0);
            auto dm = [&](int n) __attribute__((always_inline)) {
                if constexpr (P <= 5) {
                    if (n < 4) dma16<KV_NT>(srdV, kvoff, (unsigned)((P + 1) * 16384 + n * 4096), lds0 + vslot(P + 1) + (4 * n + w4) * 1024);
                    else if constexpr (P < 5) dma16<KV_NT>(srdK, kvoff, (unsigned)((P + 3) * 16384 + (n - 4) * 4096), lds0 + kslot(P + 3) + (4 * (n - 4) + w4) * 1024);
                    else dma16<KV_NT>(srdV, kvoff, (unsigned)(7 * 16384 + (n - 4) * 4096), lds0 + vslot(7) + (4 * (n - 4) + w4) * 1024);
                } else if constexpr (P <= 8) {
                    const int g = (P == 6) ? 16 + w4 : (P == 7) ? w4 : (w4 < 2 ? 4 + w4 : 6 + w4);
                    if (n < 5) dma16<WO_NT>(srdWo, wv[n], wo_soff(g), lds0 + g * 5120 + n * 1024);
                }
            };
            auto rd = [&](int buf, int ks, int part) __attribute__((always_inline)) {
                if (part == 0) { if (C) RDP(buf, P - 2, ks, 2); }
                if (part == 1) { if (AB) RDV(X, vb, ks, 0); }
                if (part == 2) { if (AB) RDP(buf, P - 1, ks, 0); }
                if (part == 3) { if (AB) RDV(X, vb, ks, 1); }
                if (part == 4) { if (AB) RDP(buf, P - 1, ks, 1); }
            };
            auto wrl = [&](int n) __attribute__((always_inline)) {
                if constexpr (P == 6 || P == 7) { if (n < 5) ld16_async<false>(wr[(P - 6) * 5 + n], srdWo, wv[n], wo_soff(P == 6 ? wg0 : wg1)); }
            };
            const unsigned flv = flagw[P - 1];
            rd(0, 0, 0); rd(0, 0, 1); rd(0, 0, 2); rd(0, 0, 3); rd(0, 0, 4); SBAR();
            if (__builtin_expect(__builtin_amdgcn_readfirstlane(flv) != 0u, 0)) {
                if constexpr (P == 7 || P == 8) asm volatile("s_waitcnt vmcnt(0)" :: "v"(wr[0]), "v"(wr[1]), "v"(wr[2]), "v"(wr[3]), "v"(wr[4]) : "memory");
                if constexpr (P == 8) asm volatile("" :: "v"(wr[5]), "v"(wr[6]), "v"(wr[7]), "v"(wr[8]), "v"(wr[9]) : "memory");
                return true;
            }
            if constexpr (P == 9) { olds(0); olds(1); }
#pragma unroll
            for (int ks = 0; ks < 4; ++ks) {
                const int cb = ks & 1; const bool more = ks < 3;
                if (C) PVM(X ^ 1, cb, ks, 2, 0); if (more) rd(cb ^ 1, ks + 1, 0); SBAR();
                if (C) PVM(X ^ 1, cb, ks, 2, 1); if (more) rd(cb ^ 1, ks + 1, 1); wrl(2 * ks); SBAR();
                if (AB) PVM(X, cb, ks, 0, 0); if (more) rd(cb ^ 1, ks + 1, 2); SBAR();
                if (AB) PVM(X, cb, ks, 0, 1); if (more) rd(cb ^ 1, ks + 1, 3); dm(2 * ks); SBAR();
                if (AB) PVM(X, cb, ks, 1, 0); if (more) rd(cb ^ 1, ks + 1, 4); wrl(2 * ks + 1); SBAR();
                if (AB) PVM(X, cb, ks, 1, 1); dm(2 * ks + 1); SBAR();
            }
            if constexpr (P <= 5) WAITV(8); else if constexpr (P == 6) WAITV(14); else if constexpr (P == 7) WAITV(20);
            else if constexpr (P == 8) asm volatile("s_waitcnt vmcnt(5)" : "+v"(wr[0]), "+v"(wr[1]), "+v"(wr[2]), "+v"(wr[3]), "+v"(wr[4]), "+v"(wr[5]), "+v"(wr[6]), "+v"(wr[7]), "+v"(wr[8]), "+v"(wr[9]) :: "memory");
            if constexpr (P == 8) { opack(0); opack(1); }
            if constexpr (P <= 8) BAR_L();
            return false;
        };
        BAR_L();
        asm volatile("s_waitcnt vmcnt(8)" : "+v"(pre[0]), "+v"(pre[1]), "+v"(pre[2]), "+v"(pre[3]), "+v"(pre[4]), "+v"(pre[5]), "+v"(pre[6]), "+v"(pre[7]) :: "memory");
#pragma unroll
        for (int n = 0; n < 8; ++n) *(LAS u32x4*)(LW + slot_addr(2 + (n >> 2)) + (4 * (n & 3) + w4) * 1024 + lane * 16) = pre[n];
        BAR_L();
        int tf = 9;
#define PERIOD(P) if (tf == 9) { if (period(std::integral_constant<int, P>{})) tf = P - 1; }
        PERIOD(1) PERIOD(2) PERIOD(3) PERIOD(4) PERIOD(5) PERIOD(6) PERIOD(7) PERIOD(8) PERIOD(9)
        slow = tf < 9;
        if (__builtin_expect(slow, 0)) {
            WAITV(0); BAR_L();
#pragma unroll 1
            for (int t = tf - 1; t < 8; ++t) {
                const int sl = slot_addr(2 + (t & 1));
#pragma unroll
                for (int n = 0; n < 4; ++n) dma16<KV_NT>(srdV, kvoff, (unsigned)(t * 16384 + n * 4096), lds0 + sl + (4 * n + w4) * 1024);
                BAR_L();
#pragma unroll
                for (int i = 0; i < 3; ++i) {
                    const float dl = fmaxf(prop[(qh * 3 + i) * 32 + r32], prop[((qh + 2) * 3 + i) * 32 + r32]);
                    const float f = __builtin_amdgcn_exp2f(-dl);
#pragma unroll
                    for (int r = 0; r < 16; ++r) { o[i][0][r] *= f; o[i][1][r] *= f; }
                }
                WAITV(0);
                BAR_L();
                const lds_cptr vb = L + sl;
#pragma unroll
                for (int ks = 0; ks < 4; ++ks) {
                    RDV(0, vb, ks, 0); RDV(0, vb, ks, 1); RDP(0, t, ks, 0); RDP(0, t, ks, 1); RDP(0, t, ks, 2);
#pragma unroll
                    for (int i = 0; i < 3; ++i) { PVM(0, 0, ks, i, 0); PVM(0, 0, ks, i, 1); }
                }
            }
            BAR_L();
#pragma unroll
            for (int p = 0; p < 5; ++p) { wr[p] = __builtin_amdgcn_raw_buffer_load_b128(srdWo, wv[p], wo_soff(wg0), 0); wr[5 + p] = __builtin_amdgcn_raw_buffer_load_b128(srdWo, wv[p], wo_soff(wg1), 0); }
#pragma unroll
            for (int p = 0; p < 10; ++p) PIN(wr[p]);
            opack(0); opack(1); olds(0); olds(1);
        }
        TS(6);
        opack(2); olds(2);
        TS(3);
        TS(16);
        BAR_L();
#pragma unroll
        for (int p = 0; p < 5; ++p) { *(LAS u32x4*)(LW + wg0 * 5120 + p * 1024 + lane * 16) = wr[p]; *(LAS u32x4*)(LW + wg1 * 5120 + p * 1024 + lane * 16) = wr[5 + p]; }
    }
    int ln; asm volatile("v_mbcnt_lo_u32_b32 %0, -1, 0\n\tv_mbcnt_hi_u32_b32 %0, -1, %0" : "=v"(ln));
    const int qg = wid & 3, kh2 = wid >> 2;
    gu32* const pflag = PFLAG(ln);
    TS(17);
    if (__builtin_expect(slow, 0)) { if (wid < 4) { wo_groups(w4, 4, 4); wo_groups(w4 < 2 ? 4 + w4 : 6 + w4, 10, 8); wo_groups(16 + w4, 20, 4); } WAITV(0); BAR_L(); }
    {
        const int g = ln >> 4, qq = ln & 15;
        const srd_t srdOg = __builtin_amdgcn_make_buffer_rsrc((void*)(ws + WS_O + (size_t)(b * NH + hg * 8) * 49152), (short)0, 8 * 49152, 0x00020000);
        const unsigned ov0 = (g >> 1) * 1024 + (g & 1) * 512 + qq * 16, ov = ln * 16 + kh2 * 2048;
#define T16(qg_, i) (6 * ((qg_) >> 1) + ((i) < 2 ? 2 * ((qg_) & 1) + (i) : 4 + ((qg_) & 1)))
#define JSTEP(j) ((j) < 16 ? (j) : (j) - 14)
#define JHAS(j, i) ((j) >= 16 ? (i) == 2 : ((j) >= 2 && (j) < 8) ? (i) < 2 : true)
        constexpr int OW = OWIN;
        u32x4 of[OW][3], oc[6];
#define LDOG(st, i) __builtin_amdgcn_raw_buffer_load_b128(srdOg, ov, ((hl + hdir * ((st) >> 1)) & 7) * 49152 + T16(qg, i) * 4096 + ((st) & 1) * 1024, 16)
#define LDO1(j, i) do { if (JHAS(j, i)) of[(j) % OW][i] = LDOG(j, i); } while (0)
#pragma unroll
        for (int st = 0; st < 2; ++st)
#pragma unroll
            for (int i = 0; i < 3; ++i) { const int t16 = T16(qg, i);
                of[st][i] = *(const LAS u32x4*)(L + own_addr((t16 >> 1) * 4 + kh2 * 2 + st) + (t16 & 1) * 256 + ov0); }
        f32x4 acc[5][3];
#pragma unroll
        for (int n = 0; n < 5; ++n)
#pragma unroll
            for (int i = 0; i < 3; ++i) acc[n][i] = f32x4{};
        lds_cptr wb[3];
        wb[0] = L + kh2 * 5120 + (16 * (g >> 1) + 4 * (g & 1) + (qq >> 2)) * 160 + (ln & 3) * 8;
        wb[1] = wb[0] + 61440; wb[2] = wb[0] + 122880;
        PIN(wb[1]); PIN(wb[2]);
        s16x4 wl[2][5], wh[2][5];
#define RDWO(buf, st, n) do { const lds_cptr p_ = wb[PAIR_OF_STEP[st] / 6] + (PAIR_OF_STEP[st] % 6) * 10240 + (n) * 32; wl[buf][n] = ldstr(p_); wh[buf][n] = ldstr(p_ + 1280); } while (0)
#pragma unroll
        for (int n = 0; n < 5; ++n) RDWO(0, 0, n);
        if (wid >= 4) WAITV(4);
        BAR_L();
        TS(18);
        if (wid == 0) { if (ln == hl) __hip_atomic_store(pflag, base + 1u, RLX_AGENT); }
        else if (wid < 4) wo_groups(19 + w4, 32, 3);
        SBAR();
        unsigned fc = 0;
#pragma unroll
        for (int j = 0; j < 22; ++j) {
            if (j == 1) {
                if (wid >= 4) WAITV(0);
                BAR_L();
                TS(7);
                if (wid == 0 && ln == hl) __hip_atomic_store(pflag, base + 2u, RLX_AGENT);
                SBAR();
            }
            if (j == 2) {
                TS(21);
                if (wid == 0) {
#pragma unroll 1
                    for (unsigned sp = 0; sp < SPIN_CAP; ++sp) {
                        const unsigned v = __hip_atomic_load(pflag, RLX_AGENT);
                        if (!__any(ln < 8 && v == base)) break;
                        __builtin_amdgcn_s_sleep(1);
                    }
                }
                TS(22);
                BAR_L();
                TS(12);
                if (kh2) __builtin_amdgcn_s_setprio(1);
#pragma unroll
                for (int j2 = 2; j2 < 2 + OW - 1; ++j2) { LDO1(j2, 0); LDO1(j2, 1); LDO1(j2, 2); }
                SBAR();
            }
            if (j == 9 - OW - 1 && wid == 0) fc = __hip_atomic_load(pflag, RLX_AGENT);
            if (j == 9 - OW) {
                if (wid == 0) {
#pragma unroll 1
                    for (unsigned sp = 0; __any(ln < 8 && fc - base < 2u) && sp < SPIN_CAP; ++sp) { __builtin_amdgcn_s_sleep(1); fc = __hip_atomic_load(pflag, RLX_AGENT); }
                }
                BAR_L();
                TS(24);
            }
            if (j == 16) { TS(25); BAR_L(); TS(26); }
#pragma unroll
            for (int n = 0; n < 5; ++n) {
#pragma unroll
                for (int i = 0; i < 3; ++i)
                    if (JHAS(j, i)) acc[n][i] = MFMA16(cat8(wl[j & 1][n], wh[j & 1][n]), __builtin_bit_cast(bf16x8, j < 16 ? of[j % OW][i] : oc[j < 16 ? 0 : j - 16]), acc[n][i]);
                if (j < 21) RDWO((j + 1) & 1, JSTEP(j + 1), n);
                if (j >= 2 && j + OW - 1 < 16 && n < 3) LDO1(j + OW - 1, n);
                if (j >= 9 && j < 15 && n == 3) oc[j - 9] = LDOG(j - 7, 2);
                SBAR();
            }
        }
        TS(13);
#pragma unroll
        for (int tl = 0; tl < 15; ++tl)
            if ((tl < 8) != (kh2 == 0)) *(LAS f32x4*)(LW + 102400 + ((qg * 15 + tl) * 64 + ln) * 16) = acc[tl / 3][tl % 3];
        BAR_L(); TS(27);
        const int dst = ((xcd & 4) | qg) + 8 * jj;
        const srd_t srdP = __builtin_amdgcn_make_buffer_rsrc((void*)(ws + WS_PART + (size_t)dst * 24576 + ((hg - qg - 1) & 3) * 8192), (short)0, 8192, 0x00020000);
#pragma unroll
        for (int u = 0; u < 8; ++u)
            if ((u < 4) == (kh2 == 0)) {
                u32x4 w = {};
#pragma unroll
                for (int k = 0; k < 2; ++k) { const int tl = 2 * u + k;
                    if (tl < 15) {
                        const f32x4 v = acc[tl / 3][tl % 3] + *(const LAS f32x4*)(L + 102400 + ((qg * 15 + tl) * 64 + ln) * 16);
                        if (qg == hg) *(LAS f32x4*)(LW + 102400 + ((qg * 15 + tl) * 64 + ln) * 16) = v;
                        w[2 * k] = cvtpk(v[0], v[1]); w[2 * k + 1] = cvtpk(v[2], v[3]);
                    } }
                if (qg != hg) st16_wt(w, srdP, ln * 16, u * 1024);
            }
        TS(28);
        WAITV(0);
        BAR_L();
        TS(14);
        if (wid == 0) {
            if (ln == 8 + hg) __hip_atomic_store(pflag, base + 1u, RLX_AGENT);
#pragma unroll 1
            for (unsigned sp = 0; sp < SPIN_CAP; ++sp) {
                const unsigned v = __hip_atomic_load(pflag, RLX_AGENT);
                if (!__any(ln >= 8 && ln < 12 && v == base)) break;
                __builtin_amdgcn_s_sleep(1);
            }
        }
        BAR_L();
        TS(29);
        const srd_t srdI = __builtin_amdgcn_make_buffer_rsrc((void*)(ws + WS_PART + (size_t)blockIdx.x * 24576), (short)0, 24576, 0x00020000);
        u32x4 pv_[3];
#pragma unroll
        for (int j = 0; j < 3; ++j) pv_[j] = __builtin_amdgcn_raw_buffer_load_b128(srdI, ln * 16, j * 8192 + wid * 1024, 16);
#pragma unroll
        for (int k = 0; k < 2; ++k) {
            const int tl = 2 * wid + k;
            if (tl < 15) {
                f32x4 sum = *(const LAS f32x4*)(L + 102400 + ((hg * 15 + tl) * 64 + ln) * 16);
#pragma unroll
                for (int j = 0; j < 3; ++j) {
                    sum[0] += __uint_as_float(pv_[j][2 * k] << 16); sum[1] += __uint_as_float(pv_[j][2 * k] & 0xffff0000u);
                    sum[2] += __uint_as_float(pv_[j][2 * k + 1] << 16); sum[3] += __uint_as_float(pv_[j][2 * k + 1] & 0xffff0000u);
                }
                const int n = tl / 3, i = tl % 3;
                const int q = T16(hg, i) * 16 + qq, col = hl * 80 + n * 16 + g * 4;
                uint2 w; w.x = cvtpk(sum[0], sum[1]); w.y = cvtpk(sum[2], sum[3]);
                *(uint2*)(out + (size_t)(b * SQ + q) * DM + col) = w;
            }
        }
        TS(15);
    }
}

extern "C" void kernel_launch(void* const* d_in, const int* in_sizes, int n_in, void* d_out, int out_size, void* d_ws, size_t ws_size, hipStream_t stream) {
    const bf16* x = (const bf16*)d_in[0]; const bf16* kc = (const bf16*)d_in[1]; const bf16* vc = (const bf16*)d_in[2];
    const bf16* wq = (const bf16*)d_in[3]; const bf16* wo = (const bf16*)d_in[4];
    (void)hipFuncSetAttribute((const void*)k1_attn, hipFuncAttributeMaxDynamicSharedMemorySize, LDS_BYTES);
    k1_attn<<<256, 512, LDS_BYTES, stream>>>(x, kc, vc, wq, wo, (char*)d_ws, (bf16*)d_out);
}
```

```cpp
#define KVB 2
#include <hip/hip_runtime.h>
#include <hip/hip_bf16.h>
#include <stdint.h>
#include <type_traits>

typedef __hip_bfloat16 bf16;
using bf16x8 = __attribute__((ext_vector_type(8))) short;
using s16x4  = __attribute__((ext_vector_type(4))) short;
using f32x16 = __attribute__((ext_vector_type(16))) float;
using f32x4  = __attribute__((ext_vector_type(4))) float;
using u32x4  = __attribute__((ext_vector_type(4))) unsigned;
typedef short v4i16_t __attribute__((ext_vector_type(4)));
#define LAS __attribute__((address_space(3)))
typedef LAS const char* lds_cptr;
typedef LAS char* lds_ptr;
typedef __amdgpu_buffer_rsrc_t srd_t;
#define DI __device__ __forceinline__
#define SBAR() __builtin_amdgcn_sched_barrier(0)
#define PIN(x) asm volatile("" : "+v"(x))
#define MFMA(a, b, c) __builtin_amdgcn_mfma_f32_32x32x16_bf16(a, b, c, 0, 0, 0)

constexpr int NB = 8, SQ = 192, DM = 640, NH = 32, SK = 512, DH = 128, HD = NH * DH;
constexpr float QSCALE = 0.08838834764831845f * 1.4426950408889634f;
#define PSLIM 65536.0f

#define TS(i)

typedef float f32x2 __attribute__((ext_vector_type(2)));
typedef __bf16 bf16x2 __attribute__((ext_vector_type(2)));
DI unsigned cvtpk(float lo, float hi) { f32x2 v = {lo, hi}; bf16x2 b = __builtin_convertvector(v, bf16x2); return __builtin_bit_cast(unsigned, b); }
DI float fmul_s(float a, float b) { return a * b; }
template <bool NT>
DI void dma16(srd_t srd, unsigned voff, unsigned soff, unsigned lds_dst_) {
    const unsigned lds_dst = __builtin_amdgcn_readfirstlane(lds_dst_);
    if constexpr (NT) asm volatile("s_mov_b32 m0, %3\n\ts_nop 0\n\tbuffer_load_dwordx4 %0, %1, %2 offen nt lds" :: "v"(voff), "s"(srd), "s"(soff), "s"(lds_dst) : "m0", "memory");
    else              asm volatile("s_mov_b32 m0, %3\n\ts_nop 0\n\tbuffer_load_dwordx4 %0, %1, %2 offen lds" :: "v"(voff), "s"(srd), "s"(soff), "s"(lds_dst) : "m0", "memory");
}
template <bool NT>
DI void ld16_async(u32x4& r, srd_t srd, unsigned voff, unsigned soff) {
    if constexpr (NT) asm volatile("buffer_load_dwordx4 %0, %1, %2, %3 offen nt" : "=v"(r) : "v"(voff), "s"(srd), "s"(soff) : "memory");
    else              asm volatile("buffer_load_dwordx4 %0, %1, %2, %3 offen" : "=v"(r) : "v"(voff), "s"(srd), "s"(soff) : "memory");
}
template <bool NT>
DI void touch4(srd_t srd, unsigned voff, unsigned soff, unsigned lds_dst) {
    if constexpr (NT) asm volatile("s_mov_b32 m0, %3\n\ts_nop 0\n\tbuffer_load_dword %0, %1, %2 offen nt lds" :: "v"(voff), "s"(srd), "s"(soff), "s"(lds_dst) : "m0", "memory");
    else              asm volatile("s_mov_b32 m0, %3\n\ts_nop 0\n\tbuffer_load_dword %0, %1, %2 offen lds" :: "v"(voff), "s"(srd), "s"(soff), "s"(lds_dst) : "m0", "memory");
}
DI bf16x8 ldsr128(lds_cptr p) { return *(const LAS bf16x8*)p; }
DI s16x4 ldstr(lds_cptr p) { return __builtin_bit_cast(s16x4, __builtin_amdgcn_ds_read_tr16_b64_v4i16((LAS v4i16_t*)p)); }
DI bf16x8 cat8(s16x4 a, s16x4 b) { return __builtin_shufflevector(a, b, 0, 1, 2, 3, 4, 5, 6, 7); }
#define WAITV(N) asm volatile("s_waitcnt vmcnt(" #N ")" ::: "memory")
#define BAR_L() asm volatile("s_waitcnt lgkmcnt(0)\n\ts_barrier" ::: "memory")
DI float xhalf_max(float v) { auto rr = __builtin_amdgcn_permlane32_swap(__float_as_uint(v), __float_as_uint(v), false, false); return fmaxf(__uint_as_float(rr[0]), __uint_as_float(rr[1])); }
DI float xhalf_sum(float v) { auto rr = __builtin_amdgcn_permlane32_swap(__float_as_uint(v), __float_as_uint(v), false, false); return __uint_as_float(rr[0]) + __uint_as_float(rr[1]); }

#define KV_NT true
#define XW_NT false

#define GAS __attribute__((address_space(1)))
typedef GAS unsigned gu32;
#define RLX_AGENT __ATOMIC_RELAXED, __HIP_MEMORY_SCOPE_AGENT
#define MFMA16(a, b, c) __builtin_amdgcn_mfma_f32_16x16x32_bf16(a, b, c, 0, 0, 0)
DI void st16_wt(u32x4 v, srd_t srd, unsigned voff, unsigned soff) {
    __builtin_amdgcn_raw_buffer_store_b128(v, srd, voff, soff, 16); SBAR(); asm volatile("s_nop 1"); SBAR();
}
constexpr unsigned ECHO_KEY = 0x5A17C3E9u;
constexpr unsigned SPIN_CAP = 1u << 20;
constexpr size_t WS_O = 0, WS_PART = 16u << 20, WS_SYNC = 48u << 20;
constexpr int PAIR_OF_STEP[16] = {8, 9, 0, 1, 2, 4, 5, 6, 7, 3, 10, 11, 12, 13, 14, 15};
constexpr unsigned long long STEP_OF_PAIR = 0xFEDCBA1087659432ull;
#define WO_NT false
#define TOUCH_NT false
#define CODE_TOUCH 30720
#define NO_SELFWARM
#define OWIN 5
#define COMMON0
#define ROTF ((((hl + 2 * bl) & 7) * 9) / 8)
#define HEADF(g_, l_) ((g_) * 8 + (l_))
#define WRS 2
#define TOUCH_P 99
constexpr int SLOT = 16384;
constexpr int MOFF = 32768;
constexpr int RING0 = 40960, RSTG = 40960, RXB = 24576;
constexpr int POFF = 106496;
constexpr int LDS_BYTES = 163840;
constexpr unsigned KSLOTS = 0x04215310u;
constexpr unsigned VSLOTS = 0x32153042u;
DI int slot_addr(unsigned s) { return s * SLOT + (s >= 2 ? 8192 : 0); }
DI int own_addr(int pr) { return pr < 20 ? POFF + (pr / 5) * 12288 + (pr % 5) * 2048 : 155648 + (pr - 20) * 2048; }
DI int kslot(int t) { return slot_addr((KSLOTS >> (4 * t)) & 15u); }
DI int vslot(int t) { return slot_addr((VSLOTS >> (4 * t)) & 15u); }
#define GROUPB(bl, hl) (((bl) + (hl)) & 1)

__global__ __launch_bounds__(512, 2) void k1_attn(const bf16* __restrict__ x, const bf16* __restrict__ kc, const bf16* __restrict__ vc,
                                                  const bf16* __restrict__ wq, const bf16* __restrict__ wo, char* __restrict__ ws, bf16* __restrict__ out) {
    extern __shared__ __attribute__((aligned(16))) char lds[];
    TS(0);
    unsigned long long pc0; asm volatile("s_getpc_b64 %0" : "=s"(pc0));
    const int tid = threadIdx.x, lane = tid & 63, r32 = lane & 31, hh = lane >> 5;
    const int wid = __builtin_amdgcn_readfirstlane(tid >> 6), w4 = wid & 3;
    const int xcd = blockIdx.x & 7, jj = blockIdx.x >> 3;
    const int bl = jj >> 3, hl = jj & 7;
    const int b = (xcd >> 2) * 4 + bl, h = HEADF(xcd & 3, hl);
    const unsigned lds0 = (unsigned)(uintptr_t)lds;
    const lds_cptr L = (lds_cptr)lds;
    const lds_ptr LW = (lds_ptr)lds;
    const bf16* Kg = kc + (size_t)(b * NH + h) * SK * DH;
    const bf16* Vg = vc + (size_t)(b * NH + h) * SK * DH;
    const int qh = w4 & 1, kh = w4 >> 1;
    const int q4 = (lane & 15) >> 2, p4 = lane & 3, blk = (lane >> 4) & 1;
    LAS unsigned* flagw = (LAS unsigned*)(LW + MOFF);
    LAS unsigned* basew = (LAS unsigned*)(LW + MOFF + 128);
    const int hg = xcd & 3;
    gu32* const F = (gu32*)(ws + WS_SYNC);
    gu32* const E = (gu32*)(ws + WS_SYNC + 65536);
#define PEER(l_) ((l_) < 8 ? xcd + 8 * (bl * 8 + (l_)) : ((xcd & 4) | ((l_) & 3)) + 8 * jj)
#define PFLAG(l_) (F + (((l_) < 8 ? 0 : 256) + PEER(l_)) * 32)
    LAS float* prop = (LAS float*)(LW + MOFF + 256);
    LAS float* lsum = (LAS float*)(LW + MOFF + 2048);

    const int hdir = (bl & 1) ? -1 : 1;
    const srd_t srdWo = __builtin_amdgcn_make_buffer_rsrc((void*)(wo + hl * 80), (short)0, HD * DM * 2 - hl * 160, 0x00020000);
    auto wo_soff = [&](int g) __attribute__((always_inline)) -> unsigned {
        const int st = (int)((STEP_OF_PAIR >> (4 * (g >> 1))) & 15u);
        return (unsigned)((HEADF(hg, (hl + hdir * (st >> 1)) & 7) * 128 + (2 * (g & 1) + (st & 1)) * 32) * DM * 2);
    };
    auto wo_groups = [&](int g0, int g1, int gs) __attribute__((always_inline)) {
        int l_; asm volatile("v_mbcnt_lo_u32_b32 %0, -1, 0\n\tv_mbcnt_hi_u32_b32 %0, -1, %0" : "=v"(l_));
        unsigned wv[5];
#pragma unroll
        for (int p = 0; p < 5; ++p) { const int o_ = p * 1024 + l_ * 16, row = o_ / 160, ch = (o_ % 160) >> 4; wv[p] = row * (DM * 2) + ch * 16; }
#pragma unroll 1
        for (int g = g0; g < g1; g += gs) {
            const unsigned so = wo_soff(g);
#pragma unroll
            for (int p = 0; p < 5; ++p) dma16<WO_NT>(srdWo, wv[p], so, lds0 + g * 5120 + p * 1024);
        }
    };
    bool slow = false;
    unsigned base = 0;

    if (wid < 4) {
        if (wid == 0) {
            if (lane < 12) {
                const unsigned base = __hip_atomic_load(PFLAG(lane), RLX_AGENT);
                basew[lane] = base;
                __hip_atomic_store(E + blockIdx.x * 32 + lane, base ^ ECHO_KEY, RLX_AGENT);
            }
        }
        if (wid == 1) {
            const srd_t srdC = __builtin_amdgcn_make_buffer_rsrc((void*)(pc0 & ~127ull), (short)0, CODE_TOUCH, 0x00020000);
#pragma unroll
            for (int k = 0; k < 4; ++k) touch4<false>(srdC, lane * 128, (unsigned)(k * 8192), lds0 + MOFF + 4096u);
        }
        bf16x8 qf[3][8];
        {
            f32x16 acc[3][2];
#pragma unroll
            for (int i = 0; i < 3; ++i) { acc[i][0] = f32x16{}; acc[i][1] = f32x16{}; }
            const int xsw = (r32 >> 1) & 7;
            int xa[4];
#pragma unroll
            for (int ks = 0; ks < 4; ++ks) xa[ks] = (96 * qh + r32) * 128 + (((2 * ks + hh) ^ xsw) << 4);
            const int ps = ((p4 & 1) << 1) | (p4 >> 1);
            const int wa0 = RXB + 2048 * hh + 1024 * kh + 64 * q4 + (((2 * blk + (ps >> 1)) ^ (2 * hh)) << 4) + 8 * (ps & 1);
            const int wa1 = RXB + 2048 * hh + 1024 * kh + 64 * q4 + 256 + (((2 * blk + (ps >> 1)) ^ (2 * hh + 1)) << 4) + 8 * (ps & 1);
            bf16x8 xf[2][3]; s16x4 wl[2][2], wh[2][2];
#define RDX(buf, base, ks, i) xf[buf][i] = ldsr128(base + xa[ks] + i * 4096)
#define RDW(buf, base, ks, db) do { wl[buf][db] = ldstr(base + wa0 + ks * 4096 + db * 512); wh[buf][db] = ldstr(base + wa1 + ks * 4096 + db * 512); } while (0)
#define MM(buf, i, db) acc[i][db] = MFMA(cat8(wl[buf][db], wh[buf][db]), xf[buf][i], acc[i][db])
#define HALF(cb, base, ks) do { \
                MM(cb, 0, 0); RDW(cb ^ 1, base, ks, 0); SBAR(); \
                MM(cb, 0, 1); RDX(cb ^ 1, base, ks, 0); SBAR(); \
                MM(cb, 1, 0); RDW(cb ^ 1, base, ks, 1); SBAR(); \
                MM(cb, 1, 1); RDX(cb ^ 1, base, ks, 1); SBAR(); \
                MM(cb, 2, 0); RDX(cb ^ 1, base, ks, 2); SBAR(); \
                MM(cb, 2, 1); SBAR(); } while (0)
            BAR_L();
            TS(4);
            { const lds_cptr base = L + RING0;
              RDW(0, base, 0, 0); RDX(0, base, 0, 0); RDW(0, base, 0, 1); RDX(0, base, 0, 1); RDX(0, base, 0, 2); SBAR();
              HALF(0, base, 1); HALF(1, base, 2); HALF(0, base, 3); }
            int rb = RSTG;
#pragma unroll 1
            for (int s = 1; s < 10; ++s) {
                SBAR(); BAR_L(); SBAR();
                const lds_cptr base = L + RING0 + rb;
                HALF(1, base, 0); HALF(0, base, 1); HALF(1, base, 2); HALF(0, base, 3);
                rb = (rb == 2 * RSTG) ? 0 : rb + RSTG;
            }
            BAR_L();
            MM(1, 0, 0); MM(1, 0, 1); MM(1, 1, 0); MM(1, 1, 1); MM(1, 2, 0); MM(1, 2, 1);
            TS(5);
#pragma unroll
            for (int i = 0; i < 3; ++i)
#pragma unroll
                for (int db = 0; db < 2; ++db)
#pragma unroll
                    for (int s = 0; s < 2; ++s) {
                        u32x4 w;
#pragma unroll
                        for (int e = 0; e < 4; ++e) w[e] = cvtpk(acc[i][db][8 * s + 2 * e] * QSCALE, acc[i][db][8 * s + 2 * e + 1] * QSCALE);
                        *(LAS u32x4*)(LW + POFF + (w4 ^ 2) * 12288 + (i * 4 + db * 2 + s) * 1024 + lane * 16) = w;
                        qf[i][db * 2 + s] = __builtin_bit_cast(bf16x8, w);
                    }
            BAR_L();
#pragma unroll
            for (int i = 0; i < 3; ++i)
#pragma unroll
                for (int j = 0; j < 4; ++j) qf[i][4 + j] = ldsr128(L + POFF + w4 * 12288 + (i * 4 + j) * 1024 + lane * 16);
        }
        TS(1);
        const int krow = 32 * kh + r32;
        const int kb0 = 2048 * (krow >> 3) + 64 * (krow & 7) + ((hh ^ ((krow >> 2) & 3)) << 4);
        const int kb1 = 2048 * (krow >> 3) + 64 * (krow & 7) + (((2 + hh) ^ ((krow >> 2) & 3)) << 4);
        const int kown = kh * 1024, koth = (1 - kh) * 1024;
        bf16x8 kf[8];
        unsigned ka[4];
#define KRD(j) asm volatile("ds_read_b128 %0, %1 offset:%2" : "+v"(kf[j]) : "v"(ka[((j) >> 2) * 2 + ((j) & 1)]), "i"(512 * (((j) >> 1) & 1)) : "memory")
#define KADDR(t_) do { const unsigned kb_ = lds0 + kslot(t_); ka[0] = kb_ + kown + kb0; ka[1] = kb_ + kown + kb1; ka[2] = kb_ + koth + kb0; ka[3] = kb_ + koth + kb1; } while (0)
#define KRDALL() do { KRD(0); KRD(1); KRD(2); KRD(3); KRD(4); KRD(5); KRD(6); KRD(7); } while (0)
        f32x16 negm[3], sc;
        float l[3], m[3], lold[3];
#pragma unroll
        for (int j = 0; j < 8; ++j) kf[j] = bf16x8{};
        KADDR(0); KRDALL();
        asm volatile("s_waitcnt lgkmcnt(0)" ::: "memory"); SBAR();
        {
            const lds_ptr pw = LW + POFF + w4 * 12288 + lane * 16;
            f32x16 s0[3]; float d[3];
#pragma unroll
            for (int i = 0; i < 3; ++i) {
                s0[i] = f32x16{};
#pragma unroll
                for (int j = 0; j < 8; ++j) s0[i] = MFMA(kf[j], qf[i][j], s0[i]);
            }
            SBAR();
            KADDR(1); KRDALL();
#pragma unroll
            for (int i = 0; i < 3; ++i) {
                float pm = s0[i][0];
#pragma unroll
                for (int r = 1; r < 16; ++r) pm = fmaxf(pm, s0[i][r]);
                d[i] = xhalf_max(pm);
                prop[(w4 * 3 + i) * 32 + r32] = d[i];
            }
            BAR_L();
#pragma unroll
            for (int i = 0; i < 3; ++i) {
                m[i] = fmaxf(d[i], prop[((w4 ^ 2) * 3 + i) * 32 + r32]);
                float psum = 0.f;
#pragma unroll
                for (int r = 0; r < 16; ++r) { negm[i][r] = -m[i]; s0[i][r] -= m[i]; }
                if (i < 2) {
#pragma unroll
                    for (int r = 0; r < 16; ++r) { s0[i][r] = __builtin_amdgcn_exp2f(s0[i][r]); psum += s0[i][r]; }
#pragma unroll
                    for (int s = 0; s < 2; ++s) { u32x4 w;
#pragma unroll
                        for (int e = 0; e < 4; ++e) w[e] = cvtpk(s0[i][8 * s + 2 * e], s0[i][8 * s + 2 * e + 1]);
                        *(LAS u32x4*)(pw + (i * 2 + s) * 1024) = w; }
                }
                l[i] = psum; lold[i] = psum;
            }
            sc = s0[2];
            BAR_L(); SBAR();
        }
        TS(8);
#define EX2(X, g) do { X[2 * (g)] = __builtin_amdgcn_exp2f(X[2 * (g)]); X[2 * (g) + 1] = __builtin_amdgcn_exp2f(X[2 * (g) + 1]); } while (0)
#define AP(X, g) do { if ((g) == 0) pa = X[0] + X[1]; else { pa += X[2 * (g)]; pa += X[2 * (g) + 1]; } w[(g) & 3] = cvtpk(X[2 * (g)], X[2 * (g) + 1]); PIN(pa); PIN(w); } while (0)
#define PST(PW, i, s) *(LAS u32x4*)(PW + ((i) * 2 + (s)) * 1024) = w
#define SMS(X, PW, i, g) do { EX2(X, g); if ((g) > 0) AP(X, (g) - 1); if ((g) == 4) PST(PW, i, 0); PIN(X); } while (0)
#define SMFIN(X, PW, i) do { AP(X, 7); PST(PW, i, 1); bad |= !(pa <= PSLIM); lold[i] = l[i]; l[i] += pa; PIN(l[i]); } while (0)
#define QK(S, i, j) S = MFMA(kf[j], qf[i][j], (j) ? S : negm[i])
        u32x4 w = {};
        int tf = 9;
#pragma unroll 1
        for (int t = 1; t < 8; ++t) {
            const lds_ptr pw = LW + POFF + w4 * 12288 + (t & 1) * 6144 + lane * 16;
            const lds_ptr pc = LW + POFF + w4 * 12288 + ((t & 1) ^ 1) * 6144 + lane * 16;
            f32x16 sa, sb; float pa; bool bad = false;
            const unsigned flv = flagw[t - 1];
            SBAR();
            QK(sa, 0, 0); SMS(sc, pc, 2, 0); SBAR(); QK(sa, 0, 1); SMS(sc, pc, 2, 1); SBAR(); QK(sa, 0, 2); SMS(sc, pc, 2, 2); SBAR();
            if (__builtin_expect(__builtin_amdgcn_readfirstlane(flv) != 0u, 0)) { tf = t - 1; break; }
            QK(sa, 0, 3); SMS(sc, pc, 2, 3); SBAR(); QK(sa, 0, 4); SMS(sc, pc, 2, 4); SBAR(); QK(sa, 0, 5); SMS(sc, pc, 2, 5); SBAR();
            QK(sa, 0, 6); SMS(sc, pc, 2, 6); SBAR(); QK(sa, 0, 7); SMS(sc, pc, 2, 7); SBAR();
            QK(sb, 1, 0); SMFIN(sc, pc, 2); KADDR(t < 7 ? t + 1 : 7); SBAR();
            QK(sb, 1, 1); SBAR(); SMS(sa, pw, 0, 0); SBAR(); QK(sb, 1, 2); SMS(sa, pw, 0, 1); SBAR(); QK(sb, 1, 3); SMS(sa, pw, 0, 2); SBAR();
            QK(sb, 1, 4); SMS(sa, pw, 0, 3); SBAR(); QK(sb, 1, 5); SMS(sa, pw, 0, 4); SBAR(); QK(sb, 1, 6); SMS(sa, pw, 0, 5); SBAR(); QK(sb, 1, 7); SMS(sa, pw, 0, 6); SBAR();
            QK(sc, 2, 0); SBAR(); KRD(0); SMS(sa, pw, 0, 7); SBAR(); QK(sc, 2, 1); SBAR(); KRD(1); SMFIN(sa, pw, 0); SBAR(); SMS(sb, pw, 1, 0); SBAR();
            QK(sc, 2, 2); SBAR(); KRD(2); SMS(sb, pw, 1, 1); SBAR(); QK(sc, 2, 3); SBAR(); KRD(3); SMS(sb, pw, 1, 2); SBAR();
            QK(sc, 2, 4); SBAR(); KRD(4); SMS(sb, pw, 1, 3); SBAR(); QK(sc, 2, 5); SBAR(); KRD(5); SMS(sb, pw, 1, 4); SBAR();
            QK(sc, 2, 6); SBAR(); KRD(6); SMS(sb, pw, 1, 5); SBAR(); QK(sc, 2, 7); SBAR(); KRD(7); SMS(sb, pw, 1, 6); SBAR();
            SMS(sb, pw, 1, 7); SBAR(); SMFIN(sb, pw, 1); SBAR();
            if (__builtin_expect(__any(bad), 0)) { if (lane == 0) flagw[t] = 1u; }
            if (t == 7) { lsum[(w4 * 3 + 0) * 32 + r32] = xhalf_sum(l[0]); lsum[(w4 * 3 + 1) * 32 + r32] = xhalf_sum(l[1]); }
            BAR_L(); SBAR();
            if (t == 1) TS(9); if (t == 3) TS(10); if (t == 5) TS(11);
        }
        if (tf == 9) {
            if (__builtin_amdgcn_readfirstlane(flagw[7]) != 0u) tf = 7;
            else {
                const lds_ptr pc = LW + POFF + w4 * 12288 + 6144 + lane * 16;
                float pa; bool bad = false;
                lold[0] = l[0]; lold[1] = l[1];
                SMS(sc, pc, 2, 0); SMS(sc, pc, 2, 1); SMS(sc, pc, 2, 2); SMS(sc, pc, 2, 3); SMS(sc, pc, 2, 4); SMS(sc, pc, 2, 5); SMS(sc, pc, 2, 6); SMS(sc, pc, 2, 7); SMFIN(sc, pc, 2);
                if (__builtin_expect(__any(bad), 0)) { if (lane == 0) flagw[8] = 1u; }
                lsum[(w4 * 3 + 2) * 32 + r32] = xhalf_sum(l[2]);
                BAR_L();
                if (__builtin_amdgcn_readfirstlane(flagw[8]) != 0u) tf = 8;
            }
        }
        slow = tf < 9;
        if (__builtin_expect(slow, 0)) {
#pragma unroll
            for (int i = 0; i < 3; ++i) l[i] = lold[i];
            BAR_L();
#pragma unroll 1
            for (int t = tf - 1; t < 8; ++t) {
                const bool all = t >= tf;
                const lds_ptr pw = LW + POFF + w4 * 12288 + (t & 1) * 6144 + lane * 16;
#pragma unroll
                for (int j = 0; j < 8; ++j) { const int s = (j < 4) ? 4 * kh + j : 4 * (1 - kh) + (j - 4);
                    kf[j] = *(const bf16x8*)(Kg + (size_t)(t * 64 + krow) * DH + s * 16 + hh * 8); }
                f32x16 s0[3]; float d[3];
#pragma unroll
                for (int i = 0; i < 3; ++i) {
                    s0[i] = f32x16{};
#pragma unroll
                    for (int j = 0; j < 8; ++j) s0[i] = MFMA(kf[j], qf[i][j], s0[i]);
                    float pm = s0[i][0];
#pragma unroll
                    for (int r = 1; r < 16; ++r) pm = fmaxf(pm, s0[i][r]);
                    d[i] = (all || i == 2) ? fmaxf(xhalf_max(pm) - m[i], 0.f) : 0.f;
                    prop[(w4 * 3 + i) * 32 + r32] = d[i];
                }
                BAR_L();
#pragma unroll
                for (int i = 0; i < 3; ++i) {
                    const float dl = fmaxf(d[i], prop[((w4 ^ 2) * 3 + i) * 32 + r32]);
                    m[i] += dl;
                    float psum = l[i] * __builtin_amdgcn_exp2f(-dl);
#pragma unroll
                    for (int r = 0; r < 16; ++r) { s0[i][r] = (all || i == 2) ? __builtin_amdgcn_exp2f(s0[i][r] - m[i]) : 0.f; psum += s0[i][r]; }
                    l[i] = psum;
#pragma unroll
                    for (int s = 0; s < 2; ++s) { u32x4 w2;
#pragma unroll
                        for (int e = 0; e < 4; ++e) w2[e] = cvtpk(s0[i][8 * s + 2 * e], s0[i][8 * s + 2 * e + 1]);
                        *(LAS u32x4*)(pw + (i * 2 + s) * 1024) = w2; }
                    lsum[(w4 * 3 + i) * 32 + r32] = xhalf_sum(psum);
                }
                BAR_L();
            }
            BAR_L();
        }
        if (wid == 0) {
            base = basew[lane < 12 ? lane : 0];
            const unsigned want = (lane < 8 ? basew[hl] : basew[8 + hg]) ^ ECHO_KEY;
            gu32* const pecho = E + PEER(lane) * 32 + (lane < 8 ? hl : 8 + hg);
#pragma unroll 1
            for (unsigned sp = 0; sp < SPIN_CAP; ++sp) {
                const unsigned echo = __hip_atomic_load(pecho, RLX_AGENT);
                if (!__any(lane < 12 && echo != want)) break;
                __builtin_amdgcn_s_sleep(2);
            }
        }
        TS(2);
        BAR_L();
    } else {
        const srd_t srdK = __builtin_amdgcn_make_buffer_rsrc((void*)Kg, (short)0, SK * DH * 2, 0x00020000);
        const srd_t srdV = __builtin_amdgcn_make_buffer_rsrc((void*)Vg, (short)0, SK * DH * 2, 0x00020000);
        const int arow = 8 * (w4 >> 1) + ((lane >> 2) & 7), ach = 4 * (2 * (w4 & 1) + hh) + (p4 ^ ((2 * (w4 >> 1) + blk) & 3));
        const unsigned kvoff = arow * 256 + ach * 16;
        auto kv_unit = [&](bool isV, int t) {
#pragma unroll
            for (int i = 0; i < 4; ++i)
                dma16<KV_NT>(isV ? srdV : srdK, kvoff, (unsigned)(t * 16384 + i * 4096), lds0 + (isV ? vslot(t) : kslot(t)) + (4 * i + w4) * 1024);
        };
        u32x4 pre[KVB == 3 ? 16 : 8];
        {
            const int rot = ROTF;
            const srd_t srdX = __builtin_amdgcn_make_buffer_rsrc((void*)(x + (size_t)b * SQ * DM), (short)0, SQ * DM * 2, 0x00020000);
            const srd_t srdW = __builtin_amdgcn_make_buffer_rsrc((void*)(wq + h * DH), (short)0, DM * HD * 2 - h * DH * 2, 0x00020000);
            const unsigned xvoff = (8 * w4 + (lane >> 3)) * (DM * 2) + (((lane & 7) ^ ((4 * (w4 & 1) + (lane >> 4)) & 7)) << 4);
            const unsigned wvoff = arow * (HD * 2) + ach * 16;
            auto issue = [&](int s, int buf) __attribute__((always_inline)) {
                int cs = s + rot; if (cs >= 10) cs -= 9; if (s == 0) cs = 0;
                const unsigned dst = lds0 + RING0 + buf;
#pragma unroll
                for (int i = 0; i < 4; ++i) dma16<XW_NT>(srdW, wvoff, (unsigned)((cs * 64 + i * 16) * HD * 2), dst + RXB + (4 * i + w4) * 1024);
#pragma unroll
                for (int i = 0; i < 6; ++i) dma16<XW_NT>(srdX, xvoff, (unsigned)(i * 32 * DM * 2 + cs * 128), dst + (4 * i + w4) * 1024);
            };
            if (wid == 4 && lane < 16) flagw[lane] = 0u;
            issue(0, 0); issue(1, RSTG);
#define PRELD(n0, n1) _Pragma("unroll") for (int n = n0; n < n1; ++n) ld16_async<KV_NT>(pre[n], (n & 4) ? srdK : srdV, kvoff, (unsigned)(((n & 4) ? 2 + (n >> 3) : (n >> 3)) * 16384 + (n & 3) * 4096))
#define KPC(n) dma16<KV_NT>(srdK, kvoff, (unsigned)((n) * 4096), lds0 + ((n) >> 2) * SLOT + (4 * ((n) & 3) + w4) * 1024)
#define STG(s_, N) do { WAITV(N); BAR_L(); issue((s_) + 2, (((s_) + 2) % 3) * RSTG); } while (0)
            STG(0, 10); STG(1, 10);
            STG(2, 10); KPC(0); KPC(1);
            STG(3, 12); KPC(2); KPC(3);
            STG(4, 14); KPC(4); KPC(5);
            STG(5, 14); KPC(6); KPC(7);
            STG(6, 14); PRELD(0, 2);
            STG(7, 14); PRELD(2, 8);
            WAITV(18); BAR_L();
            WAITV(6); BAR_L();
            BAR_L();
            kv_unit(true, 1); kv_unit(false, 3);
            WAITV(16);
            BAR_L();
        }
        f32x16 o[3][2];
#pragma unroll
        for (int i = 0; i < 3; ++i) { o[i][0] = f32x16{}; o[i][1] = f32x16{}; }
        const int vb0 = 1024 * kh + 64 * (4 * hh + q4) + (((2 * blk + (p4 >> 1)) ^ hh) << 4) + 8 * (p4 & 1);
        const int vb1 = 1024 * kh + 2048 + 64 * (4 * hh + q4) + (((2 * blk + (p4 >> 1)) ^ (2 + hh)) << 4) + 8 * (p4 & 1);
        const lds_cptr pb0 = L + POFF + qh * 12288 + lane * 16;
        s16x4 vl[2][4][2], vh[2][4][2];
        bf16x8 pf[2][3];
        unsigned wv[5];
#pragma unroll
        for (int p = 0; p < 5; ++p) { const int o_ = p * 1024 + lane * 16, row = o_ / 160, ch = (o_ % 160) >> 4; wv[p] = row * (DM * 2) + ch * 16; }
        const srd_t srdO = __builtin_amdgcn_make_buffer_rsrc((void*)(ws + WS_O + (size_t)(b * NH + hg * 8 + hl) * 49152), (short)0, 49152, 0x00020000);
        u32x4 wr[10];
        const int wg0 = 10 + w4, wg1 = w4 < 2 ? 14 + w4 : 4 + w4;
        u32x4 ow[3][4];
        auto opack = [&](int i) __attribute__((always_inline)) {
            const float rl = 1.0f / (lsum[(qh * 3 + i) * 32 + r32] + lsum[((qh + 2) * 3 + i) * 32 + r32]);
#pragma unroll
            for (int db = 0; db < 2; ++db)
#pragma unroll
                for (int s = 0; s < 2; ++s) {
#pragma unroll
                    for (int e = 0; e < 4; ++e) ow[i][db * 2 + s][e] = cvtpk(o[i][db][8 * s + 2 * e] * rl, o[i][db][8 * s + 2 * e + 1] * rl);
                    st16_wt(ow[i][db * 2 + s], srdO, (r32 >> 4) * 4096 + hh * 256 + (r32 & 15) * 16, (3 * qh + i) * 8192 + (kh * 2 + db) * 1024 + s * 512); }
        };
        auto olds = [&](int i) __attribute__((always_inline)) {
#pragma unroll
            for (int db = 0; db < 2; ++db) { const lds_ptr p_ = LW + own_addr((3 * qh + i) * 4 + kh * 2 + db) + lane * 16;
                *(LAS u32x4*)p_ = ow[i][db * 2]; *(LAS u32x4*)(p_ + 1024) = ow[i][db * 2 + 1]; }
        };
#define RDV(X, vb, ks, db) do { vl[X][ks][db] = ldstr(vb + vb0 + (ks) * 4096 + (db) * 512); vh[X][ks][db] = ldstr(vb + vb1 + (ks) * 4096 + (db) * 512); } while (0)
#define RDP(buf, blk_, ks, i) pf[buf][i] = ldsr128(pb0 + ((blk_) & 1) * 6144 + ((ks) >> 1) * 24576 + ((i) * 2 + ((ks) & 1)) * 1024)
#define PVM(X, buf, ks, i, db) o[i][db] = MFMA(cat8(vl[X][ks][db], vh[X][ks][db]), pf[buf][i], o[i][db])
        auto period = [&](auto tag) __attribute__((always_inline)) -> bool {
            constexpr int P = decltype(tag)::value, X = P & 1;
            constexpr bool AB = P <= 8, C = P >= 2;
            const lds_cptr vb = L + vslot(AB ? P - 1 : 0);
            auto dm = [&](int n) __attribute__((always_inline)) {
                if constexpr (P <= 5) {
                    if (n < 4) dma16<KV_NT>(srdV, kvoff, (unsigned)((P + 1) * 16384 + n * 4096), lds0 + vslot(P + 1) + (4 * n + w4) * 1024);
                    else if constexpr (P < 5) dma16<KV_NT>(srdK, kvoff, (unsigned)((P + 3) * 16384 + (n - 4) * 4096), lds0 + kslot(P + 3) + (4 * (n - 4) + w4) * 1024);
                    else dma16<KV_NT>(srdV, kvoff, (unsigned)(7 * 16384 + (n - 4) * 4096), lds0 + vslot(7) + (4 * (n - 4) + w4) * 1024);
                } else if constexpr (P <= 8) {
                    const int g = (P == 6) ? 16 + w4 : (P == 7) ? w4 : (w4 < 2 ? 4 + w4 : 6 + w4);
                    if (n < 5) dma16<WO_NT>(srdWo, wv[n], wo_soff(g), lds0 + g * 5120 + n * 1024);
                }
            };
            auto rd = [&](int buf, int ks, int part) __attribute__((always_inline)) {
                if (part == 0) { if (C) RDP(buf, P - 2, ks, 2); }
                if (part == 1) { if (AB) RDV(X, vb, ks, 0); }
                if (part == 2) { if (AB) RDP(buf, P - 1, ks, 0); }
                if (part == 3) { if (AB) RDV(X, vb, ks, 1); }
                if (part == 4) { if (AB) RDP(buf, P - 1, ks, 1); }
            };
            auto wrl = [&](int n) __attribute__((always_inline)) {
                if constexpr (P == 6 || P == 7) { if (n < 5) ld16_async<false>(wr[(P - 6) * 5 + n], srdWo, wv[n], wo_soff(P == 6 ? wg0 : wg1)); }
            };
            const unsigned flv = flagw[P - 1];
            rd(0, 0, 0); rd(0, 0, 1); rd(0, 0, 2); rd(0, 0, 3); rd(0, 0, 4); SBAR();
            if (__builtin_expect(__builtin_amdgcn_readfirstlane(flv) != 0u, 0)) {
                if constexpr (P == 7 || P == 8) asm volatile("s_waitcnt vmcnt(0)" :: "v"(wr[0]), "v"(wr[1]), "v"(wr[2]), "v"(wr[3]), "v"(wr[4]) : "memory");
                if constexpr (P == 8) asm volatile("" :: "v"(wr[5]), "v"(wr[6]), "v"(wr[7]), "v"(wr[8]), "v"(wr[9]) : "memory");
                return true;
            }
            if constexpr (P == 9) { olds(0); olds(1); }
#pragma unroll
            for (int ks = 0; ks < 4; ++ks) {
                const int cb = ks & 1; const bool more = ks < 3;
                if (C) PVM(X ^ 1, cb, ks, 2, 0); if (more) rd(cb ^ 1, ks + 1, 0); SBAR();
                if (C) PVM(X ^ 1, cb, ks, 2, 1); if (more) rd(cb ^ 1, ks + 1, 1); wrl(2 * ks); SBAR();
                if (AB) PVM(X, cb, ks, 0, 0); if (more) rd(cb ^ 1, ks + 1, 2); SBAR();
                if (AB) PVM(X, cb, ks, 0, 1); if (more) rd(cb ^ 1, ks + 1, 3); dm(2 * ks); SBAR();
                if (AB) PVM(X, cb, ks, 1, 0); if (more) rd(cb ^ 1, ks + 1, 4); wrl(2 * ks + 1); SBAR();
                if (AB) PVM(X, cb, ks, 1, 1); dm(2 * ks + 1); SBAR();
            }
            if constexpr (P <= 5) WAITV(8); else if constexpr (P == 6) WAITV(14); else if constexpr (P == 7) WAITV(20);
            else if constexpr (P == 8) asm volatile("s_waitcnt vmcnt(5)" : "+v"(wr[0]), "+v"(wr[1]), "+v"(wr[2]), "+v"(wr[3]), "+v"(wr[4]), "+v"(wr[5]), "+v"(wr[6]), "+v"(wr[7]), "+v"(wr[8]), "+v"(wr[9]) :: "memory");
            if constexpr (P == 8) { opack(0); opack(1); }
            if constexpr (P <= 8) BAR_L();
            return false;
        };
        BAR_L();
        asm volatile("s_waitcnt vmcnt(8)" : "+v"(pre[0]), "+v"(pre[1]), "+v"(pre[2]), "+v"(pre[3]), "+v"(pre[4]), "+v"(pre[5]), "+v"(pre[6]), "+v"(pre[7]) :: "memory");
#pragma unroll
        for (int n = 0; n < (KVB == 3 ? 16 : 8); ++n) *(LAS u32x4*)(LW + slot_addr(2 + (n >> 2)) + (4 * (n & 3) + w4) * 1024 + lane * 16) = pre[n];
        BAR_L();
        int tf = 9;
#define PERIOD(P) if (tf == 9) { if (period(std::integral_constant<int, P>{})) tf = P - 1; }
        PERIOD(1) PERIOD(2) PERIOD(3) PERIOD(4) PERIOD(5) PERIOD(6) PERIOD(7) PERIOD(8) PERIOD(9)
        slow = tf < 9;
        if (__builtin_expect(slow, 0)) {
            WAITV(0); BAR_L();
#pragma unroll 1
            for (int t = tf - 1; t < 8; ++t) {
                const int sl = slot_addr(2 + (t & 1));
#pragma unroll
                for (int n = 0; n < 4; ++n) dma16<KV_NT>(srdV, kvoff, (unsigned)(t * 16384 + n * 4096), lds0 + sl + (4 * n + w4) * 1024);
                BAR_L();
#pragma unroll
                for (int i = 0; i < 3; ++i) {
                    const float dl = fmaxf(prop[(qh * 3 + i) * 32 + r32], prop[((qh + 2) * 3 + i) * 32 + r32]);
                    const float f = __builtin_amdgcn_exp2f(-dl);
#pragma unroll
                    for (int r = 0; r < 16; ++r) { o[i][0][r] *= f; o[i][1][r] *= f; }
                }
                WAITV(0);
                BAR_L();
                const lds_cptr vb = L + sl;
#pragma unroll
                for (int ks = 0; ks < 4; ++ks) {
                    RDV(0, vb, ks, 0); RDV(0, vb, ks, 1); RDP(0, t, ks, 0); RDP(0, t, ks, 1); RDP(0, t, ks, 2);
#pragma unroll
                    for (int i = 0; i < 3; ++i) { PVM(0, 0, ks, i, 0); PVM(0, 0, ks, i, 1); }
                }
            }
            BAR_L();
#pragma unroll
            for (int p = 0; p < 5; ++p) { wr[p] = __builtin_amdgcn_raw_buffer_load_b128(srdWo, wv[p], wo_soff(wg0), 0); wr[5 + p] = __builtin_amdgcn_raw_buffer_load_b128(srdWo, wv[p], wo_soff(wg1), 0); }
#pragma unroll
            for (int p = 0; p < 10; ++p) PIN(wr[p]);
            opack(0); opack(1); olds(0); olds(1);
        }
        TS(6);
        opack(2); olds(2);
        TS(3);
        TS(16);
        BAR_L();
#pragma unroll
        for (int p = 0; p < 5; ++p) { *(LAS u32x4*)(LW + wg0 * 5120 + p * 1024 + lane * 16) = wr[p]; *(LAS u32x4*)(LW + wg1 * 5120 + p * 1024 + lane * 16) = wr[5 + p]; }
    }
    int ln; asm volatile("v_mbcnt_lo_u32_b32 %0, -1, 0\n\tv_mbcnt_hi_u32_b32 %0, -1, %0" : "=v"(ln));
    const int qg = wid & 3, kh2 = wid >> 2;
    gu32* const pflag = PFLAG(ln);
    TS(17);
    if (__builtin_expect(slow, 0)) { if (wid < 4) { wo_groups(w4, 4, 4); wo_groups(w4 < 2 ? 4 + w4 : 6 + w4, 10, 8); wo_groups(16 + w4, 20, 4); } WAITV(0); BAR_L(); }
    {
        const int g = ln >> 4, qq = ln & 15;
        const srd_t srdOg = __builtin_amdgcn_make_buffer_rsrc((void*)(ws + WS_O + (size_t)(b * NH + hg * 8) * 49152), (short)0, 8 * 49152, 0x00020000);
        const unsigned ov0 = (g >> 1) * 1024 + (g & 1) * 512 + qq * 16, ov = ln * 16 + kh2 * 2048;
#define T16(qg_, i) (6 * ((qg_) >> 1) + ((i) < 2 ? 2 * ((qg_) & 1) + (i) : 4 + ((qg_) & 1)))
#define JSTEP(j) ((j) < 16 ? (j) : (j) - 14)
#define JHAS(j, i) ((j) >= 16 ? (i) == 2 : ((j) >= 2 && (j) < 8) ? (i) < 2 : true)
        constexpr int OW = OWIN;
        u32x4 of[OW][3], oc[6];
#define LDOG(st, i) __builtin_amdgcn_raw_buffer_load_b128(srdOg, ov, ((hl + hdir * ((st) >> 1)) & 7) * 49152 + T16(qg, i) * 4096 + ((st) & 1) * 1024, 16)
#define LDO1(j, i) do { if (JHAS(j, i)) of[(j) % OW][i] = LDOG(j, i); } while (0)
#pragma unroll
        for (int st = 0; st < 2; ++st)
#pragma unroll
            for (int i = 0; i < 3; ++i) { const int t16 = T16(qg, i);
                of[st][i] = *(const LAS u32x4*)(L + own_addr((t16 >> 1) * 4 + kh2 * 2 + st) + (t16 & 1) * 256 + ov0); }
        f32x4 acc[5][3];
#pragma unroll
        for (int n = 0; n < 5; ++n)
#pragma unroll
            for (int i = 0; i < 3; ++i) acc[n][i] = f32x4{};
        lds_cptr wb[3];
        wb[0] = L + kh2 * 5120 + (16 * (g >> 1) + 4 * (g & 1) + (qq >> 2)) * 160 + (ln & 3) * 8;
        wb[1] = wb[0] + 61440; wb[2] = wb[0] + 122880;
        PIN(wb[1]); PIN(wb[2]);
        s16x4 wl[2][5], wh[2][5];
#define RDWO(buf, st, n) do { const lds_cptr p_ = wb[PAIR_OF_STEP[st] / 6] + (PAIR_OF_STEP[st] % 6) * 10240 + (n) * 32; wl[buf][n] = ldstr(p_); wh[buf][n] = ldstr(p_ + 1280); } while (0)
#pragma unroll
        for (int n = 0; n < 5; ++n) RDWO(0, 0, n);
        if (wid >= 4) WAITV(4);
        BAR_L();
        TS(18);
        if (wid == 0) { if (ln == hl) __hip_atomic_store(pflag, base + 1u, RLX_AGENT); }
        else if (wid < 4) wo_groups(19 + w4, 32, 3);
        SBAR();
        unsigned fc = 0;
#pragma unroll
        for (int j = 0; j < 22; ++j) {
            if (j == 1) {
                if (wid >= 4) WAITV(0);
                BAR_L();
                TS(7);
                if (wid == 0 && ln == hl) __hip_atomic_store(pflag, base + 2u, RLX_AGENT);
                SBAR();
            }
            if (j == 2) {
                TS(21);
                if (wid == 0) {
#pragma unroll 1
                    for (unsigned sp = 0; sp < SPIN_CAP; ++sp) {
                        const unsigned v = __hip_atomic_load(pflag, RLX_AGENT);
                        if (!__any(ln < 8 && v == base)) break;
                        __builtin_amdgcn_s_sleep(1);
                    }
                }
                TS(22);
                BAR_L();
                TS(12);
                if (kh2) __builtin_amdgcn_s_setprio(1);
#pragma unroll
                for (int j2 = 2; j2 < 2 + OW - 1; ++j2) { LDO1(j2, 0); LDO1(j2, 1); LDO1(j2, 2); }
                SBAR();
            }
            if (j == 9 - OW - 1 && wid == 0) fc = __hip_atomic_load(pflag, RLX_AGENT);
            if (j == 9 - OW) {
                if (wid == 0) {
#pragma unroll 1
                    for (unsigned sp = 0; __any(ln < 8 && fc - base < 2u) && sp < SPIN_CAP; ++sp) { __builtin_amdgcn_s_sleep(1); fc = __hip_atomic_load(pflag, RLX_AGENT); }
                }
                BAR_L();
                TS(24);
            }
            if (j == 16) { TS(25); BAR_L(); TS(26); }
#pragma unroll
            for (int n = 0; n < 5; ++n) {
#pragma unroll
                for (int i = 0; i < 3; ++i)
                    if (JHAS(j, i)) acc[n][i] = MFMA16(cat8(wl[j & 1][n], wh[j & 1][n]), __builtin_bit_cast(bf16x8, j < 16 ? of[j % OW][i] : oc[j < 16 ? 0 : j - 16]), acc[n][i]);
                if (j < 21) RDWO((j + 1) & 1, JSTEP(j + 1), n);
                if (j >= 2 && j + OW - 1 < 16 && n < 3) LDO1(j + OW - 1, n);
                if (j >= 9 && j < 15 && n == 3) oc[j - 9] = LDOG(j - 7, 2);
                SBAR();
            }
        }
        TS(13);
#pragma unroll
        for (int tl = 0; tl < 15; ++tl)
            if ((tl < 8) != (kh2 == 0)) *(LAS f32x4*)(LW + 102400 + ((qg * 15 + tl) * 64 + ln) * 16) = acc[tl / 3][tl % 3];
        BAR_L(); TS(27);
        const int dst = ((xcd & 4) | qg) + 8 * jj;
        const srd_t srdP = __builtin_amdgcn_make_buffer_rsrc((void*)(ws + WS_PART + (size_t)dst * 24576 + ((hg - qg - 1) & 3) * 8192), (short)0, 8192, 0x00020000);
#pragma unroll
        for (int u = 0; u < 8; ++u)
            if ((u < 4) == (kh2 == 0)) {
                u32x4 w = {};
#pragma unroll
                for (int k = 0; k < 2; ++k) { const int tl = 2 * u + k;
                    if (tl < 15) {
                        const f32x4 v = acc[tl / 3][tl % 3] + *(const LAS f32x4*)(L + 102400 + ((qg * 15 + tl) * 64 + ln) * 16);
                        if (qg == hg) *(LAS f32x4*)(LW + 102400 + ((qg * 15 + tl) * 64 + ln) * 16) = v;
                        w[2 * k] = cvtpk(v[0], v[1]); w[2 * k + 1] = cvtpk(v[2], v[3]);
                    } }
                if (qg != hg) st16_wt(w, srdP, ln * 16, u * 1024);
            }
        TS(28);
        WAITV(0);
        BAR_L();
        TS(14);
        if (wid == 0) {
            if (ln == 8 + hg) __hip_atomic_store(pflag, base + 1u, RLX_AGENT);
#pragma unroll 1
            for (unsigned sp = 0; sp < SPIN_CAP; ++sp) {
                const unsigned v = __hip_atomic_load(pflag, RLX_AGENT);
                if (!__any(ln >= 8 && ln < 12 && v == base)) break;
                __builtin_amdgcn_s_sleep(1);
            }
        }
        BAR_L();
        TS(29);
        const srd_t srdI = __builtin_amdgcn_make_buffer_rsrc((void*)(ws + WS_PART + (size_t)blockIdx.x * 24576), (short)0, 24576, 0x00020000);
        u32x4 pv_[3];
#pragma unroll
        for (int j = 0; j < 3; ++j) pv_[j] = __builtin_amdgcn_raw_buffer_load_b128(srdI, ln * 16, j * 8192 + wid * 1024, 16);
#pragma unroll
        for (int k = 0; k < 2; ++k) {
            const int tl = 2 * wid + k;
            if (tl < 15) {
                f32x4 sum = *(const LAS f32x4*)(L + 102400 + ((hg * 15 + tl) * 64 + ln) * 16);
#pragma unroll
                for (int j = 0; j < 3; ++j) {
                    sum[0] += __uint_as_float(pv_[j][2 * k] << 16); sum[1] += __uint_as_float(pv_[j][2 * k] & 0xffff0000u);
                    sum[2] += __uint_as_float(pv_[j][2 * k + 1] << 16); sum[3] += __uint_as_float(pv_[j][2 * k + 1] & 0xffff0000u);
                }
                const int n = tl / 3, i = tl % 3;
                const int q = T16(hg, i) * 16 + qq, col = hl * 80 + n * 16 + g * 4;
                uint2 w; w.x = cvtpk(sum[0], sum[1]); w.y = cvtpk(sum[2], sum[3]);
                *(uint2*)(out + (size_t)(b * SQ + q) * DM + col) = w;
            }
        }
        TS(15);
    }
}

extern "C" void kernel_launch(void* const* d_in, const int* in_sizes, int n_in, void* d_out, int out_size, void* d_ws, size_t ws_size, hipStream_t stream) {
    const bf16* x = (const bf16*)d_in[0]; const bf16* kc = (const bf16*)d_in[1]; const bf16* vc = (const bf16*)d_in[2];
    const bf16* wq = (const bf16*)d_in[3]; const bf16* wo = (const bf16*)d_in[4];
    (void)hipFuncSetAttribute((const void*)k1_attn, hipFuncAttributeMaxDynamicSharedMemorySize, LDS_BYTES);
    k1_attn<<<256, 512, LDS_BYTES, stream>>>(x, kc, vc, wq, wo, (char*)d_ws, (bf16*)d_out);
}
```

```cpp
#include <hip/hip_runtime.h>
#include <hip/hip_bf16.h>
#include <stdint.h>
#include <type_traits>

typedef __hip_bfloat16 bf16;
using bf16x8 = __attribute__((ext_vector_type(8))) short;
using s16x4  = __attribute__((ext_vector_type(4))) short;
using f32x16 = __attribute__((ext_vector_type(16))) float;
using f32x4  = __attribute__((ext_vector_type(4))) float;
using u32x4  = __attribute__((ext_vector_type(4))) unsigned;
typedef short v4i16_t __attribute__((ext_vector_type(4)));
#define LAS __attribute__((address_space(3)))
typedef LAS const char* lds_cptr;
typedef LAS char* lds_ptr;
typedef __amdgpu_buffer_rsrc_t srd_t;
#define DI __device__ __forceinline__
#define SBAR() __builtin_amdgcn_sched_barrier(0)
#define PIN(x) asm volatile("" : "+v"(x))
#define MFMA(a, b, c) __builtin_amdgcn_mfma_f32_32x32x16_bf16(a, b, c, 0, 0, 0)

constexpr int NB = 8, SQ = 192, DM = 640, NH = 32, SK = 512, DH = 128, HD = NH * DH;
constexpr float QSCALE = 0.08838834764831845f * 1.4426950408889634f;
#define PSLIM 65536.0f

#define TS(i)

typedef float f32x2 __attribute__((ext_vector_type(2)));
typedef __bf16 bf16x2 __attribute__((ext_vector_type(2)));
DI unsigned cvtpk(float lo, float hi) { f32x2 v = {lo, hi}; bf16x2 b = __builtin_convertvector(v, bf16x2); return __builtin_bit_cast(unsigned, b); }
DI float fmul_s(float a, float b) { return a * b; }
template <bool NT>
DI void dma16(srd_t srd, unsigned voff, unsigned soff, unsigned lds_dst_) {
    const unsigned lds_dst = __builtin_amdgcn_readfirstlane(lds_dst_);
    if constexpr (NT) asm volatile("s_mov_b32 m0, %3\n\ts_nop 0\n\tbuffer_load_dwordx4 %0, %1, %2 offen nt lds" :: "v"(voff), "s"(srd), "s"(soff), "s"(lds_dst) : "m0", "memory");
    else              asm volatile("s_mov_b32 m0, %3\n\ts_nop 0\n\tbuffer_load_dwordx4 %0, %1, %2 offen lds" :: "v"(voff), "s"(srd), "s"(soff), "s"(lds_dst) : "m0", "memory");
}
template <bool NT>
DI void ld16_async(u32x4& r, srd_t srd, unsigned voff, unsigned soff) {
    if constexpr (NT) asm volatile("buffer_load_dwordx4 %0, %1, %2, %3 offen nt" : "=v"(r) : "v"(voff), "s"(srd), "s"(soff) : "memory");
    else              asm volatile("buffer_load_dwordx4 %0, %1, %2, %3 offen" : "=v"(r) : "v"(voff), "s"(srd), "s"(soff) : "memory");
}
template <bool NT>
DI void touch4(srd_t srd, unsigned voff, unsigned soff, unsigned lds_dst) {
    if constexpr (NT) asm volatile("s_mov_b32 m0, %3\n\ts_nop 0\n\tbuffer_load_dword %0, %1, %2 offen nt lds" :: "v"(voff), "s"(srd), "s"(soff), "s"(lds_dst) : "m0", "memory");
    else              asm volatile("s_mov_b32 m0, %3\n\ts_nop 0\n\tbuffer_load_dword %0, %1, %2 offen lds" :: "v"(voff), "s"(srd), "s"(soff), "s"(lds_dst) : "m0", "memory");
}
DI bf16x8 ldsr128(lds_cptr p) { return *(const LAS bf16x8*)p; }
DI s16x4 ldstr(lds_cptr p) { return __builtin_bit_cast(s16x4, __builtin_amdgcn_ds_read_tr16_b64_v4i16((LAS v4i16_t*)p)); }
DI bf16x8 cat8(s16x4 a, s16x4 b) { return __builtin_shufflevector(a, b, 0, 1, 2, 3, 4, 5, 6, 7); }
#define WAITV(N) asm volatile("s_waitcnt vmcnt(" #N ")" ::: "memory")
#define BAR_L() asm volatile("s_waitcnt lgkmcnt(0)\n\ts_barrier" ::: "memory")
DI float xhalf_max(float v) { auto rr = __builtin_amdgcn_permlane32_swap(__float_as_uint(v), __float_as_uint(v), false, false); return fmaxf(__uint_as_float(rr[0]), __uint_as_float(rr[1])); }
DI float xhalf_sum(float v) { auto rr = __builtin_amdgcn_permlane32_swap(__float_as_uint(v), __float_as_uint(v), false, false); return __uint_as_float(rr[0]) + __uint_as_float(rr[1]); }

#define KV_NT true
#define XW_NT false

#define GAS __attribute__((address_space(1)))
typedef GAS unsigned gu32;
#define RLX_AGENT __ATOMIC_RELAXED, __HIP_MEMORY_SCOPE_AGENT
#define MFMA16(a, b, c) __builtin_amdgcn_mfma_f32_16x16x32_bf16(a, b, c, 0, 0, 0)
DI void st16_wt(u32x4 v, srd_t srd, unsigned voff, unsigned soff) {
    __builtin_amdgcn_raw_buffer_store_b128(v, srd, voff, soff, 16); SBAR(); asm volatile("s_nop 1"); SBAR();
}
constexpr unsigned ECHO_KEY = 0x5A17C3E9u;
constexpr unsigned SPIN_CAP = 1u << 20;
constexpr size_t WS_O = 0, WS_PART = 16u << 20, WS_SYNC = 48u << 20;
constexpr int PAIR_OF_STEP[16] = {8, 9, 0, 1, 2, 4, 5, 6, 7, 3, 10, 11, 12, 13, 14, 15};
constexpr unsigned long long STEP_OF_PAIR = 0xFEDCBA1087659432ull;
#define WO_NT false
#define TOUCH_NT false
#define CODE_TOUCH 32768
#define NO_SELFWARM
#define OWIN 5
#define COMMON0
#define ROTF ((((hl + 2 * bl) & 7) * 9) / 8)
#define HEADF(g_, l_) ((g_) * 8 + (l_))
#define KVB 0
#define WRS 2
#define TOUCH_P 99
constexpr int SLOT = 16384;
constexpr int MOFF = 32768;
constexpr int RING0 = 40960, RSTG = 40960, RXB = 24576;
constexpr int POFF = 106496;
constexpr int LDS_BYTES = 163840;
constexpr unsigned KSLOTS = 0x04215310u;
constexpr unsigned VSLOTS = 0x32153042u;
DI int slot_addr(unsigned s) { return s * SLOT + (s >= 2 ? 8192 : 0); }
DI int own_addr(int pr) { return pr < 20 ? POFF + (pr / 5) * 12288 + (pr % 5) * 2048 : 155648 + (pr - 20) * 2048; }
DI int kslot(int t) { return slot_addr((KSLOTS >> (4 * t)) & 15u); }
DI int vslot(int t) { return slot_addr((VSLOTS >> (4 * t)) & 15u); }
#define GROUPB(bl, hl) (((bl) + (hl)) & 1)

__global__ __launch_bounds__(512, 2) void k1_attn(const bf16* __restrict__ x, const bf16* __restrict__ kc, const bf16* __restrict__ vc,
                                                  const bf16* __restrict__ wq, const bf16* __restrict__ wo, char* __restrict__ ws, bf16* __restrict__ out) {
    extern __shared__ __attribute__((aligned(16))) char lds[];
    TS(0);
    unsigned long long pc0; asm volatile("s_getpc_b64 %0" : "=s"(pc0));
    const int tid = threadIdx.x, lane = tid & 63, r32 = lane & 31, hh = lane >> 5;
    const int wid = __builtin_amdgcn_readfirstlane(tid >> 6), w4 = wid & 3;
    const int xcd = blockIdx.x & 7, jj = blockIdx.x >> 3;
    const int bl = jj >> 3, hl = jj & 7;
    const int b = (xcd >> 2) * 4 + bl, h = HEADF(xcd & 3, hl);
    const unsigned lds0 = (unsigned)(uintptr_t)lds;
    const lds_cptr L = (lds_cptr)lds;
    const lds_ptr LW = (lds_ptr)lds;
    const bf16* Kg = kc + (size_t)(b * NH + h) * SK * DH;
    const bf16* Vg = vc + (size_t)(b * NH + h) * SK * DH;
    const int qh = w4 & 1, kh = w4 >> 1;
    const int q4 = (lane & 15) >> 2, p4 = lane & 3, blk = (lane >> 4) & 1;
    LAS unsigned* flagw = (LAS unsigned*)(LW + MOFF);
    LAS unsigned* basew = (LAS unsigned*)(LW + MOFF + 128);
    const int hg = xcd & 3;
    gu32* const F = (gu32*)(ws + WS_SYNC);
    gu32* const E = (gu32*)(ws + WS_SYNC + 65536);
#define PEER(l_) ((l_) < 8 ? xcd + 8 * (bl * 8 + (l_)) : ((xcd & 4) | ((l_) & 3)) + 8 * jj)
#define PFLAG(l_) (F + (((l_) < 8 ? 0 : 256) + PEER(l_)) * 32)
    LAS float* prop = (LAS float*)(LW + MOFF + 256);
    LAS float* lsum = (LAS float*)(LW + MOFF + 2048);

    const int hdir = (bl & 1) ? -1 : 1;
    const srd_t srdWo = __builtin_amdgcn_make_buffer_rsrc((void*)(wo + hl * 80), (short)0, HD * DM * 2 - hl * 160, 0x00020000);
    auto wo_soff = [&](int g) __attribute__((always_inline)) -> unsigned {
        const int st = (int)((STEP_OF_PAIR >> (4 * (g >> 1))) & 15u);
        return (unsigned)((HEADF(hg, (hl + hdir * (st >> 1)) & 7) * 128 + (2 * (g & 1) + (st & 1)) * 32) * DM * 2);
    };
    auto wo_groups = [&](int g0, int g1, int gs) __attribute__((always_inline)) {
        int l_; asm volatile("v_mbcnt_lo_u32_b32 %0, -1, 0\n\tv_mbcnt_hi_u32_b32 %0, -1, %0" : "=v"(l_));
        unsigned wv[5];
#pragma unroll
        for (int p = 0; p < 5; ++p) { const int o_ = p * 1024 + l_ * 16, row = o_ / 160, ch = (o_ % 160) >> 4; wv[p] = row * (DM * 2) + ch * 16; }
#pragma unroll 1
        for (int g = g0; g < g1; g += gs) {
            const unsigned so = wo_soff(g);
#pragma unroll
            for (int p = 0; p < 5; ++p) dma16<WO_NT>(srdWo, wv[p], so, lds0 + g * 5120 + p * 1024);
        }
    };
    bool slow = false;
    unsigned base = 0;

    if (wid < 4) {
        if (wid == 0) {
            if (lane < 12) {
                const unsigned base = __hip_atomic_load(PFLAG(lane), RLX_AGENT);
                basew[lane] = base;
                __hip_atomic_store(E + blockIdx.x * 32 + lane, base ^ ECHO_KEY, RLX_AGENT);
            }
        }
        if (wid == 1) {
            const srd_t srdC = __builtin_amdgcn_make_buffer_rsrc((void*)(pc0 & ~127ull), (short)0, CODE_TOUCH, 0x00020000);
#pragma unroll
            for (int k = 0; k < 4; ++k) touch4<false>(srdC, lane * 128, (unsigned)(k * 8192), lds0 + MOFF + 4096u);
        }
        bf16x8 qf[3][8];
        {
            f32x16 acc[3][2];
#pragma unroll
            for (int i = 0; i < 3; ++i) { acc[i][0] = f32x16{}; acc[i][1] = f32x16{}; }
            const int xsw = (r32 >> 1) & 7;
            int xa[4];
#pragma unroll
            for (int ks = 0; ks < 4; ++ks) xa[ks] = (96 * qh + r32) * 128 + (((2 * ks + hh) ^ xsw) << 4);
            const int ps = ((p4 & 1) << 1) | (p4 >> 1);
            const int wa0 = RXB + 2048 * hh + 1024 * kh + 64 * q4 + (((2 * blk + (ps >> 1)) ^ (2 * hh)) << 4) + 8 * (ps & 1);
            const int wa1 = RXB + 2048 * hh + 1024 * kh + 64 * q4 + 256 + (((2 * blk + (ps >> 1)) ^ (2 * hh + 1)) << 4) + 8 * (ps & 1);
            bf16x8 xf[2][3]; s16x4 wl[2][2], wh[2][2];
#define RDX(buf, base, ks, i) xf[buf][i] = ldsr128(base + xa[ks] + i * 4096)
#define RDW(buf, base, ks, db) do { wl[buf][db] = ldstr(base + wa0 + ks * 4096 + db * 512); wh[buf][db] = ldstr(base + wa1 + ks * 4096 + db * 512); } while (0)
#define MM(buf, i, db) acc[i][db] = MFMA(cat8(wl[buf][db], wh[buf][db]), xf[buf][i], acc[i][db])
#define HALF(cb, base, ks) do { \
                MM(cb, 0, 0); RDW(cb ^ 1, base, ks, 0); SBAR(); \
                MM(cb, 0, 1); RDX(cb ^ 1, base, ks, 0); SBAR(); \
                MM(cb, 1, 0); RDW(cb ^ 1, base, ks, 1); SBAR(); \
                MM(cb, 1, 1); RDX(cb ^ 1, base, ks, 1); SBAR(); \
                MM(cb, 2, 0); RDX(cb ^ 1, base, ks, 2); SBAR(); \
                MM(cb, 2, 1); SBAR(); } while (0)
            BAR_L();
            TS(4);
            { const lds_cptr base = L + RING0;
              RDW(0, base, 0, 0); RDX(0, base, 0, 0); RDW(0, base, 0, 1); RDX(0, base, 0, 1); RDX(0, base, 0, 2); SBAR();
              HALF(0, base, 1); HALF(1, base, 2); HALF(0, base, 3); }
            int rb = RSTG;
#pragma unroll 1
            for (int s = 1; s < 10; ++s) {
                SBAR(); BAR_L(); SBAR();
                const lds_cptr base = L + RING0 + rb;
                HALF(1, base, 0); HALF(0, base, 1); HALF(1, base, 2); HALF(0, base, 3);
                rb = (rb == 2 * RSTG) ? 0 : rb + RSTG;
            }
            BAR_L();
            MM(1, 0, 0); MM(1, 0, 1); MM(1, 1, 0); MM(1, 1, 1); MM(1, 2, 0); MM(1, 2, 1);
            TS(5);
#pragma unroll
            for (int i = 0; i < 3; ++i)
#pragma unroll
                for (int db = 0; db < 2; ++db)
#pragma unroll
                    for (int s = 0; s < 2; ++s) {
                        u32x4 w;
#pragma unroll
                        for (int e = 0; e < 4; ++e) w[e] = cvtpk(acc[i][db][8 * s + 2 * e] * QSCALE, acc[i][db][8 * s + 2 * e + 1] * QSCALE);
                        *(LAS u32x4*)(LW + POFF + (w4 ^ 2) * 12288 + (i * 4 + db * 2 + s) * 1024 + lane * 16) = w;
                        qf[i][db * 2 + s] = __builtin_bit_cast(bf16x8, w);
                    }
            BAR_L();
#pragma unroll
            for (int i = 0; i < 3; ++i)
#pragma unroll
                for (int j = 0; j < 4; ++j) qf[i][4 + j] = ldsr128(L + POFF + w4 * 12288 + (i * 4 + j) * 1024 + lane * 16);
        }
        TS(1);
        const int krow = 32 * kh + r32;
        const int kb0 = 2048 * (krow >> 3) + 64 * (krow & 7) + ((hh ^ ((krow >> 2) & 3)) << 4);
        const int kb1 = 2048 * (krow >> 3) + 64 * (krow & 7) + (((2 + hh) ^ ((krow >> 2) & 3)) << 4);
        const int kown = kh * 1024, koth = (1 - kh) * 1024;
        bf16x8 kf[8];
        unsigned ka[4];
#define KRD(j) asm volatile("ds_read_b128 %0, %1 offset:%2" : "+v"(kf[j]) : "v"(ka[((j) >> 2) * 2 + ((j) & 1)]), "i"(512 * (((j) >> 1) & 1)) : "memory")
#define KADDR(t_) do { const unsigned kb_ = lds0 + kslot(t_); ka[0] = kb_ + kown + kb0; ka[1] = kb_ + kown + kb1; ka[2] = kb_ + koth + kb0; ka[3] = kb_ + koth + kb1; } while (0)
#define KRDALL() do { KRD(0); KRD(1); KRD(2); KRD(3); KRD(4); KRD(5); KRD(6); KRD(7); } while (0)
        f32x16 negm[3], sc;
        float l[3], m[3], lold[3];
#pragma unroll
        for (int j = 0; j < 8; ++j) kf[j] = bf16x8{};
        KADDR(0); KRDALL();
        asm volatile("s_waitcnt lgkmcnt(0)" ::: "memory"); SBAR();
        {
            const lds_ptr pw = LW + POFF + w4 * 12288 + lane * 16;
            f32x16 s0[3]; float d[3];
#pragma unroll
            for (int i = 0; i < 3; ++i) {
                s0[i] = f32x16{};
#pragma unroll
                for (int j = 0; j < 8; ++j) s0[i] = MFMA(kf[j], qf[i][j], s0[i]);
            }
            SBAR();
            KADDR(1); KRDALL();
#pragma unroll
            for (int i = 0; i < 3; ++i) {
                float pm = s0[i][0];
#pragma unroll
                for (int r = 1; r < 16; ++r) pm = fmaxf(pm, s0[i][r]);
                d[i] = xhalf_max(pm);
                prop[(w4 * 3 + i) * 32 + r32] = d[i];
            }
            BAR_L();
#pragma unroll
            for (int i = 0; i < 3; ++i) {
                m[i] = fmaxf(d[i], prop[((w4 ^ 2) * 3 + i) * 32 + r32]);
                float psum = 0.f;
#pragma unroll
                for (int r = 0; r < 16; ++r) { negm[i][r] = -m[i]; s0[i][r] -= m[i]; }
                if (i < 2) {
#pragma unroll
                    for (int r = 0; r < 16; ++r) { s0[i][r] = __builtin_amdgcn_exp2f(s0[i][r]); psum += s0[i][r]; }
#pragma unroll
                    for (int s = 0; s < 2; ++s) { u32x4 w;
#pragma unroll
                        for (int e = 0; e < 4; ++e) w[e] = cvtpk(s0[i][8 * s + 2 * e], s0[i][8 * s + 2 * e + 1]);
                        *(LAS u32x4*)(pw + (i * 2 + s) * 1024) = w; }
                }
                l[i] = psum; lold[i] = psum;
            }
            sc = s0[2];
            BAR_L(); SBAR();
        }
        TS(8);
#define EX2(X, g) do { X[2 * (g)] = __builtin_amdgcn_exp2f(X[2 * (g)]); X[2 * (g) + 1] = __builtin_amdgcn_exp2f(X[2 * (g) + 1]); } while (0)
#define AP(X, g) do { if ((g) == 0) pa = X[0] + X[1]; else { pa += X[2 * (g)]; pa += X[2 * (g) + 1]; } w[(g) & 3] = cvtpk(X[2 * (g)], X[2 * (g) + 1]); PIN(pa); PIN(w); } while (0)
#define PST(PW, i, s) *(LAS u32x4*)(PW + ((i) * 2 + (s)) * 1024) = w
#define SMS(X, PW, i, g) do { EX2(X, g); if ((g) > 0) AP(X, (g) - 1); if ((g) == 4) PST(PW, i, 0); PIN(X); } while (0)
#define SMFIN(X, PW, i) do { AP(X, 7); PST(PW, i, 1); bad |= !(pa <= PSLIM); lold[i] = l[i]; l[i] += pa; PIN(l[i]); } while (0)
#define QK(S, i, j) S = MFMA(kf[j], qf[i][j], (j) ? S : negm[i])
        u32x4 w = {};
        int tf = 9;
#pragma unroll 1
        for (int t = 1; t < 8; ++t) {
            const lds_ptr pw = LW + POFF + w4 * 12288 + (t & 1) * 6144 + lane * 16;
            const lds_ptr pc = LW + POFF + w4 * 12288 + ((t & 1) ^ 1) * 6144 + lane * 16;
            f32x16 sa, sb; float pa; bool bad = false;
            const unsigned flv = flagw[t - 1];
            SBAR();
            QK(sa, 0, 0); SMS(sc, pc, 2, 0); SBAR(); QK(sa, 0, 1); SMS(sc, pc, 2, 1); SBAR(); QK(sa, 0, 2); SMS(sc, pc, 2, 2); SBAR();
            if (__builtin_expect(__builtin_amdgcn_readfirstlane(flv) != 0u, 0)) { tf = t - 1; break; }
            QK(sa, 0, 3); SMS(sc, pc, 2, 3); SBAR(); QK(sa, 0, 4); SMS(sc, pc, 2, 4); SBAR(); QK(sa, 0, 5); SMS(sc, pc, 2, 5); SBAR();
            QK(sa, 0, 6); SMS(sc, pc, 2, 6); SBAR(); QK(sa, 0, 7); SMS(sc, pc, 2, 7); SBAR();
            QK(sb, 1, 0); SMFIN(sc, pc, 2); KADDR(t < 7 ? t + 1 : 7); SBAR();
            QK(sb, 1, 1); SBAR(); SMS(sa, pw, 0, 0); SBAR(); QK(sb, 1, 2); SMS(sa, pw, 0, 1); SBAR(); QK(sb, 1, 3); SMS(sa, pw, 0, 2); SBAR();
            QK(sb, 1, 4); SMS(sa, pw, 0, 3); SBAR(); QK(sb, 1, 5); SMS(sa, pw, 0, 4); SBAR(); QK(sb, 1, 6); SMS(sa, pw, 0, 5); SBAR(); QK(sb, 1, 7); SMS(sa, pw, 0, 6); SBAR();
            QK(sc, 2, 0); SBAR(); KRD(0); SMS(sa, pw, 0, 7); SBAR(); QK(sc, 2, 1); SBAR(); KRD(1); SMFIN(sa, pw, 0); SBAR(); SMS(sb, pw, 1, 0); SBAR();
            QK(sc, 2, 2); SBAR(); KRD(2); SMS(sb, pw, 1, 1); SBAR(); QK(sc, 2, 3); SBAR(); KRD(3); SMS(sb, pw, 1, 2); SBAR();
            QK(sc, 2, 4); SBAR(); KRD(4); SMS(sb, pw, 1, 3); SBAR(); QK(sc, 2, 5); SBAR(); KRD(5); SMS(sb, pw, 1, 4); SBAR();
            QK(sc, 2, 6); SBAR(); KRD(6); SMS(sb, pw, 1, 5); SBAR(); QK(sc, 2, 7); SBAR(); KRD(7); SMS(sb, pw, 1, 6); SBAR();
            SMS(sb, pw, 1, 7); SBAR(); SMFIN(sb, pw, 1); SBAR();
            if (__builtin_expect(__any(bad), 0)) { if (lane == 0) flagw[t] = 1u; }
            if (t == 7) { lsum[(w4 * 3 + 0) * 32 + r32] = xhalf_sum(l[0]); lsum[(w4 * 3 + 1) * 32 + r32] = xhalf_sum(l[1]); }
            BAR_L(); SBAR();
            if (t == 1) TS(9); if (t == 3) TS(10); if (t == 5) TS(11);
        }
        if (tf == 9) {
            if (__builtin_amdgcn_readfirstlane(flagw[7]) != 0u) tf = 7;
            else {
                const lds_ptr pc = LW + POFF + w4 * 12288 + 6144 + lane * 16;
                float pa; bool bad = false;
                lold[0] = l[0]; lold[1] = l[1];
                SMS(sc, pc, 2, 0); SMS(sc, pc, 2, 1); SMS(sc, pc, 2, 2); SMS(sc, pc, 2, 3); SMS(sc, pc, 2, 4); SMS(sc, pc, 2, 5); SMS(sc, pc, 2, 6); SMS(sc, pc, 2, 7); SMFIN(sc, pc, 2);
                if (__builtin_expect(__any(bad), 0)) { if (lane == 0) flagw[8] = 1u; }
                lsum[(w4 * 3 + 2) * 32 + r32] = xhalf_sum(l[2]);
                BAR_L();
                if (__builtin_amdgcn_readfirstlane(flagw[8]) != 0u) tf = 8;
            }
        }
        slow = tf < 9;
        if (__builtin_expect(slow, 0)) {
#pragma unroll
            for (int i = 0; i < 3; ++i) l[i] = lold[i];
            BAR_L();
#pragma unroll 1
            for (int t = tf - 1; t < 8; ++t) {
                const bool all = t >= tf;
                const lds_ptr pw = LW + POFF + w4 * 12288 + (t & 1) * 6144 + lane * 16;
#pragma unroll
                for (int j = 0; j < 8; ++j) { const int s = (j < 4) ? 4 * kh + j : 4 * (1 - kh) + (j - 4);
                    kf[j] = *(const bf16x8*)(Kg + (size_t)(t * 64 + krow) * DH + s * 16 + hh * 8); }
                f32x16 s0[3]; float d[3];
#pragma unroll
                for (int i = 0; i < 3; ++i) {
                    s0[i] = f32x16{};
#pragma unroll
                    for (int j = 0; j < 8; ++j) s0[i] = MFMA(kf[j], qf[i][j], s0[i]);
                    float pm = s0[i][0];
#pragma unroll
                    for (int r = 1; r < 16; ++r) pm = fmaxf(pm, s0[i][r]);
                    d[i] = (all || i == 2) ? fmaxf(xhalf_max(pm) - m[i], 0.f) : 0.f;
                    prop[(w4 * 3 + i) * 32 + r32] = d[i];
                }
                BAR_L();
#pragma unroll
                for (int i = 0; i < 3; ++i) {
                    const float dl = fmaxf(d[i], prop[((w4 ^ 2) * 3 + i) * 32 + r32]);
                    m[i] += dl;
                    float psum = l[i] * __builtin_amdgcn_exp2f(-dl);
#pragma unroll
                    for (int r = 0; r < 16; ++r) { s0[i][r] = (all || i == 2) ? __builtin_amdgcn_exp2f(s0[i][r] - m[i]) : 0.f; psum += s0[i][r]; }
                    l[i] = psum;
#pragma unroll
                    for (int s = 0; s < 2; ++s) { u32x4 w2;
#pragma unroll
                        for (int e = 0; e < 4; ++e) w2[e] = cvtpk(s0[i][8 * s + 2 * e], s0[i][8 * s + 2 * e + 1]);
                        *(LAS u32x4*)(pw + (i * 2 + s) * 1024) = w2; }
                    lsum[(w4 * 3 + i) * 32 + r32] = xhalf_sum(psum);
                }
                BAR_L();
            }
            BAR_L();
        }
        if (wid == 0) {
            base = basew[lane < 12 ? lane : 0];
            const unsigned want = (lane < 8 ? basew[hl] : basew[8 + hg]) ^ ECHO_KEY;
            gu32* const pecho = E + PEER(lane) * 32 + (lane < 8 ? hl : 8 + hg);
#pragma unroll 1
            for (unsigned sp = 0; sp < SPIN_CAP; ++sp) {
                const unsigned echo = __hip_atomic_load(pecho, RLX_AGENT);
                if (!__any(lane < 12 && echo != want)) break;
                __builtin_amdgcn_s_sleep(2);
            }
        }
        TS(2);
        BAR_L();
    } else {
        const srd_t srdK = __builtin_amdgcn_make_buffer_rsrc((void*)Kg, (short)0, SK * DH * 2, 0x00020000);
        const srd_t srdV = __builtin_amdgcn_make_buffer_rsrc((void*)Vg, (short)0, SK * DH * 2, 0x00020000);
        const int arow = 8 * (w4 >> 1) + ((lane >> 2) & 7), ach = 4 * (2 * (w4 & 1) + hh) + (p4 ^ ((2 * (w4 >> 1) + blk) & 3));
        const unsigned kvoff = arow * 256 + ach * 16;
        auto kv_unit = [&](bool isV, int t) {
#pragma unroll
            for (int i = 0; i < 4; ++i)
                dma16<KV_NT>(isV ? srdV : srdK, kvoff, (unsigned)(t * 16384 + i * 4096), lds0 + (isV ? vslot(t) : kslot(t)) + (4 * i + w4) * 1024);
        };
        u32x4 pre[8];
        {
            const int rot = ROTF;
            const srd_t srdX = __builtin_amdgcn_make_buffer_rsrc((void*)(x + (size_t)b * SQ * DM), (short)0, SQ * DM * 2, 0x00020000);
            const srd_t srdW = __builtin_amdgcn_make_buffer_rsrc((void*)(wq + h * DH), (short)0, DM * HD * 2 - h * DH * 2, 0x00020000);
            const unsigned xvoff = (8 * w4 + (lane >> 3)) * (DM * 2) + (((lane & 7) ^ ((4 * (w4 & 1) + (lane >> 4)) & 7)) << 4);
            const unsigned wvoff = arow * (HD * 2) + ach * 16;
            auto issue = [&](int s, int buf) __attribute__((always_inline)) {
                int cs = s + rot; if (cs >= 10) cs -= 9; if (s == 0) cs = 0;
                const unsigned dst = lds0 + RING0 + buf;
#pragma unroll
                for (int i = 0; i < 4; ++i) dma16<XW_NT>(srdW, wvoff, (unsigned)((cs * 64 + i * 16) * HD * 2), dst + RXB + (4 * i + w4) * 1024);
#pragma unroll
                for (int i = 0; i < 6; ++i) dma16<XW_NT>(srdX, xvoff, (unsigned)(i * 32 * DM * 2 + cs * 128), dst + (4 * i + w4) * 1024);
            };
            if (wid == 4 && lane < 16) flagw[lane] = 0u;
            issue(0, 0); issue(1, RSTG);
            int rb = 2 * RSTG;
#pragma unroll 1
            for (int s = 0; s < 8; ++s) { WAITV(10); BAR_L(); issue(s + 2, rb); rb = (rb == 2 * RSTG) ? 0 : rb + RSTG; }
#define PRELD(n0, n1) _Pragma("unroll") for (int n = n0; n < n1; ++n) ld16_async<KV_NT>(pre[n], n < 4 ? srdV : srdK, kvoff, (unsigned)((n < 4 ? 0 : 2) * 16384 + (n & 3) * 4096))
            kv_unit(false, 0);
            WAITV(14); BAR_L(); kv_unit(false, 1); PRELD(0, 4);
            WAITV(12); BAR_L(); PRELD(4, 8);
            BAR_L();
            kv_unit(true, 1); kv_unit(false, 3);
            WAITV(16);
            BAR_L();
        }
        f32x16 o[3][2];
#pragma unroll
        for (int i = 0; i < 3; ++i) { o[i][0] = f32x16{}; o[i][1] = f32x16{}; }
        const int vb0 = 1024 * kh + 64 * (4 * hh + q4) + (((2 * blk + (p4 >> 1)) ^ hh) << 4) + 8 * (p4 & 1);
        const int vb1 = 1024 * kh + 2048 + 64 * (4 * hh + q4) + (((2 * blk + (p4 >> 1)) ^ (2 + hh)) << 4) + 8 * (p4 & 1);
        const lds_cptr pb0 = L + POFF + qh * 12288 + lane * 16;
        s16x4 vl[2][4][2], vh[2][4][2];
        bf16x8 pf[2][3];
        unsigned wv[5];
#pragma unroll
        for (int p = 0; p < 5; ++p) { const int o_ = p * 1024 + lane * 16, row = o_ / 160, ch = (o_ % 160) >> 4; wv[p] = row * (DM * 2) + ch * 16; }
        const srd_t srdO = __builtin_amdgcn_make_buffer_rsrc((void*)(ws + WS_O + (size_t)(b * NH + hg * 8 + hl) * 49152), (short)0, 49152, 0x00020000);
        u32x4 wr[10];
        const int wg0 = 10 + w4, wg1 = w4 < 2 ? 14 + w4 : 4 + w4;
        u32x4 ow[3][4];
        auto opack = [&](int i) __attribute__((always_inline)) {
            const float rl = 1.0f / (lsum[(qh * 3 + i) * 32 + r32] + lsum[((qh + 2) * 3 + i) * 32 + r32]);
#pragma unroll
            for (int db = 0; db < 2; ++db)
#pragma unroll
                for (int s = 0; s < 2; ++s) {
#pragma unroll
                    for (int e = 0; e < 4; ++e) ow[i][db * 2 + s][e] = cvtpk(o[i][db][8 * s + 2 * e] * rl, o[i][db][8 * s + 2 * e + 1] * rl);
                    st16_wt(ow[i][db * 2 + s], srdO, (r32 >> 4) * 4096 + hh * 256 + (r32 & 15) * 16, (3 * qh + i) * 8192 + (kh * 2 + db) * 1024 + s * 512); }
        };
        auto olds = [&](int i) __attribute__((always_inline)) {
#pragma unroll
            for (int db = 0; db < 2; ++db) { const lds_ptr p_ = LW + own_addr((3 * qh + i) * 4 + kh * 2 + db) + lane * 16;
                *(LAS u32x4*)p_ = ow[i][db * 2]; *(LAS u32x4*)(p_ + 1024) = ow[i][db * 2 + 1]; }
        };
#define RDV(X, vb, ks, db) do { vl[X][ks][db] = ldstr(vb + vb0 + (ks) * 4096 + (db) * 512); vh[X][ks][db] = ldstr(vb + vb1 + (ks) * 4096 + (db) * 512); } while (0)
#define RDP(buf, blk_, ks, i) pf[buf][i] = ldsr128(pb0 + ((blk_) & 1) * 6144 + ((ks) >> 1) * 24576 + ((i) * 2 + ((ks) & 1)) * 1024)
#define PVM(X, buf, ks, i, db) o[i][db] = MFMA(cat8(vl[X][ks][db], vh[X][ks][db]), pf[buf][i], o[i][db])
        auto period = [&](auto tag) __attribute__((always_inline)) -> bool {
            constexpr int P = decltype(tag)::value, X = P & 1;
            constexpr bool AB = P <= 8, C = P >= 2;
            const lds_cptr vb = L + vslot(AB ? P - 1 : 0);
            auto dm = [&](int n) __attribute__((always_inline)) {
                if constexpr (P <= 5) {
                    if (n < 4) dma16<KV_NT>(srdV, kvoff, (unsigned)((P + 1) * 16384 + n * 4096), lds0 + vslot(P + 1) + (4 * n + w4) * 1024);
                    else if constexpr (P < 5) dma16<KV_NT>(srdK, kvoff, (unsigned)((P + 3) * 16384 + (n - 4) * 4096), lds0 + kslot(P + 3) + (4 * (n - 4) + w4) * 1024);
                    else dma16<KV_NT>(srdV, kvoff, (unsigned)(7 * 16384 + (n - 4) * 4096), lds0 + vslot(7) + (4 * (n - 4) + w4) * 1024);
                } else if constexpr (P <= 8) {
                    const int g = (P == 6) ? 16 + w4 : (P == 7) ? w4 : (w4 < 2 ? 4 + w4 : 6 + w4);
                    if (n < 5) dma16<WO_NT>(srdWo, wv[n], wo_soff(g), lds0 + g * 5120 + n * 1024);
                }
            };
            auto rd = [&](int buf, int ks, int part) __attribute__((always_inline)) {
                if (part == 0) { if (C) RDP(buf, P - 2, ks, 2); }
                if (part == 1) { if (AB) RDV(X, vb, ks, 0); }
                if (part == 2) { if (AB) RDP(buf, P - 1, ks, 0); }
                if (part == 3) { if (AB) RDV(X, vb, ks, 1); }
                if (part == 4) { if (AB) RDP(buf, P - 1, ks, 1); }
            };
            auto wrl = [&](int n) __attribute__((always_inline)) {
                if constexpr (P == 6 || P == 7) { if (n < 5) ld16_async<false>(wr[(P - 6) * 5 + n], srdWo, wv[n], wo_soff(P == 6 ? wg0 : wg1)); }
            };
            const unsigned flv = flagw[P - 1];
            rd(0, 0, 0); rd(0, 0, 1); rd(0, 0, 2); rd(0, 0, 3); rd(0, 0, 4); SBAR();
            if (__builtin_expect(__builtin_amdgcn_readfirstlane(flv) != 0u, 0)) {
                if constexpr (P == 7 || P == 8) asm volatile("s_waitcnt vmcnt(0)" :: "v"(wr[0]), "v"(wr[1]), "v"(wr[2]), "v"(wr[3]), "v"(wr[4]) : "memory");
                if constexpr (P == 8) asm volatile("" :: "v"(wr[5]), "v"(wr[6]), "v"(wr[7]), "v"(wr[8]), "v"(wr[9]) : "memory");
                return true;
            }
            if constexpr (P == 9) { olds(0); olds(1); }
#pragma unroll
            for (int ks = 0; ks < 4; ++ks) {
                const int cb = ks & 1; const bool more = ks < 3;
                if (C) PVM(X ^ 1, cb, ks, 2, 0); if (more) rd(cb ^ 1, ks + 1, 0); SBAR();
                if (C) PVM(X ^ 1, cb, ks, 2, 1); if (more) rd(cb ^ 1, ks + 1, 1); wrl(2 * ks); SBAR();
                if (AB) PVM(X, cb, ks, 0, 0); if (more) rd(cb ^ 1, ks + 1, 2); SBAR();
                if (AB) PVM(X, cb, ks, 0, 1); if (more) rd(cb ^ 1, ks + 1, 3); dm(2 * ks); SBAR();
                if (AB) PVM(X, cb, ks, 1, 0); if (more) rd(cb ^ 1, ks + 1, 4); wrl(2 * ks + 1); SBAR();
                if (AB) PVM(X, cb, ks, 1, 1); dm(2 * ks + 1); SBAR();
            }
            if constexpr (P <= 5) WAITV(8); else if constexpr (P == 6) WAITV(14); else if constexpr (P == 7) WAITV(20);
            else if constexpr (P == 8) asm volatile("s_waitcnt vmcnt(5)" : "+v"(wr[0]), "+v"(wr[1]), "+v"(wr[2]), "+v"(wr[3]), "+v"(wr[4]), "+v"(wr[5]), "+v"(wr[6]), "+v"(wr[7]), "+v"(wr[8]), "+v"(wr[9]) :: "memory");
            if constexpr (P == 8) { opack(0); opack(1); }
            if constexpr (P <= 8) BAR_L();
            return false;
        };
        BAR_L();
        asm volatile("s_waitcnt vmcnt(8)" : "+v"(pre[0]), "+v"(pre[1]), "+v"(pre[2]), "+v"(pre[3]), "+v"(pre[4]), "+v"(pre[5]), "+v"(pre[6]), "+v"(pre[7]) :: "memory");
#pragma unroll
        for (int n = 0; n < 8; ++n) *(LAS u32x4*)(LW + slot_addr(2 + (n >> 2)) + (4 * (n & 3) + w4) * 1024 + lane * 16) = pre[n];
        BAR_L();
        int tf = 9;
#define PERIOD(P) if (tf == 9) { if (period(std::integral_constant<int, P>{})) tf = P - 1; }
        PERIOD(1) PERIOD(2) PERIOD(3) PERIOD(4) PERIOD(5) PERIOD(6) PERIOD(7) PERIOD(8) PERIOD(9)
        slow = tf < 9;
        if (__builtin_expect(slow, 0)) {
            WAITV(0); BAR_L();
#pragma unroll 1
            for (int t = tf - 1; t < 8; ++t) {
                const int sl = slot_addr(2 + (t & 1));
#pragma unroll
                for (int n = 0; n < 4; ++n) dma16<KV_NT>(srdV, kvoff, (unsigned)(t * 16384 + n * 4096), lds0 + sl + (4 * n + w4) * 1024);
                BAR_L();
#pragma unroll
                for (int i = 0; i < 3; ++i) {
                    const float dl = fmaxf(prop[(qh * 3 + i) * 32 + r32], prop[((qh + 2) * 3 + i) * 32 + r32]);
                    const float f = __builtin_amdgcn_exp2f(-dl);
#pragma unroll
                    for (int r = 0; r < 16; ++r) { o[i][0][r] *= f; o[i][1][r] *= f; }
                }
                WAITV(0);
                BAR_L();
                const lds_cptr vb = L + sl;
#pragma unroll
                for (int ks = 0; ks < 4; ++ks) {
                    RDV(0, vb, ks, 0); RDV(0, vb, ks, 1); RDP(0, t, ks, 0); RDP(0, t, ks, 1); RDP(0, t, ks, 2);
#pragma unroll
                    for (int i = 0; i < 3; ++i) { PVM(0, 0, ks, i, 0); PVM(0, 0, ks, i, 1); }
                }
            }
            BAR_L();
#pragma unroll
            for (int p = 0; p < 5; ++p) { wr[p] = __builtin_amdgcn_raw_buffer_load_b128(srdWo, wv[p], wo_soff(wg0), 0); wr[5 + p] = __builtin_amdgcn_raw_buffer_load_b128(srdWo, wv[p], wo_soff(wg1), 0); }
#pragma unroll
            for (int p = 0; p < 10; ++p) PIN(wr[p]);
            opack(0); opack(1); olds(0); olds(1);
        }
        TS(6);
        opack(2); olds(2);
        TS(3);
        TS(16);
        BAR_L();
#pragma unroll
        for (int p = 0; p < 5; ++p) { *(LAS u32x4*)(LW + wg0 * 5120 + p * 1024 + lane * 16) = wr[p]; *(LAS u32x4*)(LW + wg1 * 5120 + p * 1024 + lane * 16) = wr[5 + p]; }
    }
    int ln; asm volatile("v_mbcnt_lo_u32_b32 %0, -1, 0\n\tv_mbcnt_hi_u32_b32 %0, -1, %0" : "=v"(ln));
    const int qg = wid & 3, kh2 = wid >> 2;
    gu32* const pflag = PFLAG(ln);
    TS(17);
    if (__builtin_expect(slow, 0)) { if (wid < 4) { wo_groups(w4, 4, 4); wo_groups(w4 < 2 ? 4 + w4 : 6 + w4, 10, 8); wo_groups(16 + w4, 20, 4); } WAITV(0); BAR_L(); }
    {
        const int g = ln >> 4, qq = ln & 15;
        const srd_t srdOg = __builtin_amdgcn_make_buffer_rsrc((void*)(ws + WS_O + (size_t)(b * NH + hg * 8) * 49152), (short)0, 8 * 49152, 0x00020000);
        const unsigned ov0 = (g >> 1) * 1024 + (g & 1) * 512 + qq * 16, ov = ln * 16 + kh2 * 2048;
#define T16(qg_, i) (6 * ((qg_) >> 1) + ((i) < 2 ? 2 * ((qg_) & 1) + (i) : 4 + ((qg_) & 1)))
#define JSTEP(j) ((j) < 16 ? (j) : (j) - 14)
#define JHAS(j, i) ((j) >= 16 ? (i) == 2 : ((j) >= 2 && (j) < 8) ? (i) < 2 : true)
        constexpr int OW = OWIN;
        u32x4 of[OW][3], oc[6];
#define LDOG(st, i) __builtin_amdgcn_raw_buffer_load_b128(srdOg, ov, ((hl + hdir * ((st) >> 1)) & 7) * 49152 + T16(qg, i) * 4096 + ((st) & 1) * 1024, 16)
#define LDO1(j, i) do { if (JHAS(j, i)) of[(j) % OW][i] = LDOG(j, i); } while (0)
#pragma unroll
        for (int st = 0; st < 2; ++st)
#pragma unroll
            for (int i = 0; i < 3; ++i) { const int t16 = T16(qg, i);
                of[st][i] = *(const LAS u32x4*)(L + own_addr((t16 >> 1) * 4 + kh2 * 2 + st) + (t16 & 1) * 256 + ov0); }
        f32x4 acc[5][3];
#pragma unroll
        for (int n = 0; n < 5; ++n)
#pragma unroll
            for (int i = 0; i < 3; ++i) acc[n][i] = f32x4{};
        lds_cptr wb[3];
        wb[0] = L + kh2 * 5120 + (16 * (g >> 1) + 4 * (g & 1) + (qq >> 2)) * 160 + (ln & 3) * 8;
        wb[1] = wb[0] + 61440; wb[2] = wb[0] + 122880;
        PIN(wb[1]); PIN(wb[2]);
        s16x4 wl[2][5], wh[2][5];
#define RDWO(buf, st, n) do { const lds_cptr p_ = wb[PAIR_OF_STEP[st] / 6] + (PAIR_OF_STEP[st] % 6) * 10240 + (n) * 32; wl[buf][n] = ldstr(p_); wh[buf][n] = ldstr(p_ + 1280); } while (0)
#pragma unroll
        for (int n = 0; n < 5; ++n) RDWO(0, 0, n);
        if (wid >= 4) WAITV(4);
        BAR_L();
        TS(18);
        if (wid == 0) { if (ln == hl) __hip_atomic_store(pflag, base + 1u, RLX_AGENT); }
        else if (wid < 4) wo_groups(19 + w4, 32, 3);
        SBAR();
        unsigned fc = 0;
#pragma unroll
        for (int j = 0; j < 22; ++j) {
            if (j == 1) {
                if (wid >= 4) WAITV(0);
                BAR_L();
                TS(7);
                if (wid == 0 && ln == hl) __hip_atomic_store(pflag, base + 2u, RLX_AGENT);
                SBAR();
            }
            if (j == 2) {
                TS(21);
                if (wid == 0) {
#pragma unroll 1
                    for (unsigned sp = 0; sp < SPIN_CAP; ++sp) {
                        const unsigned v = __hip_atomic_load(pflag, RLX_AGENT);
                        if (!__any(ln < 8 && v == base)) break;
                        __builtin_amdgcn_s_sleep(1);
                    }
                }
                TS(22);
                BAR_L();
                TS(12);
                if (kh2) __builtin_amdgcn_s_setprio(1);
#pragma unroll
                for (int j2 = 2; j2 < 2 + OW - 1; ++j2) { LDO1(j2, 0); LDO1(j2, 1); LDO1(j2, 2); }
                SBAR();
            }
            if (j == 9 - OW - 1 && wid == 0) fc = __hip_atomic_load(pflag, RLX_AGENT);
            if (j == 9 - OW) {
                if (wid == 0) {
#pragma unroll 1
                    for (unsigned sp = 0; __any(ln < 8 && fc - base < 2u) && sp < SPIN_CAP; ++sp) { __builtin_amdgcn_s_sleep(1); fc = __hip_atomic_load(pflag, RLX_AGENT); }
                }
                BAR_L();
                TS(24);
            }
            if (j == 16) { TS(25); BAR_L(); TS(26); }
#pragma unroll
            for (int n = 0; n < 5; ++n) {
#pragma unroll
                for (int i = 0; i < 3; ++i)
                    if (JHAS(j, i)) acc[n][i] = MFMA16(cat8(wl[j & 1][n], wh[j & 1][n]), __builtin_bit_cast(bf16x8, j < 16 ? of[j % OW][i] : oc[j < 16 ? 0 : j - 16]), acc[n][i]);
                if (j < 21) RDWO((j + 1) & 1, JSTEP(j + 1), n);
                if (j >= 2 && j + OW - 1 < 16 && n < 3) LDO1(j + OW - 1, n);
                if (j >= 9 && j < 15 && n == 3) oc[j - 9] = LDOG(j - 7, 2);
                SBAR();
            }
        }
        TS(13);
#pragma unroll
        for (int tl = 0; tl < 15; ++tl)
            if ((tl < 8) != (kh2 == 0)) *(LAS f32x4*)(LW + 102400 + ((qg * 15 + tl) * 64 + ln) * 16) = acc[tl / 3][tl % 3];
        BAR_L(); TS(27);
        const int dst = ((xcd & 4) | qg) + 8 * jj;
        const srd_t srdP = __builtin_amdgcn_make_buffer_rsrc((void*)(ws + WS_PART + (size_t)dst * 24576 + ((hg - qg - 1) & 3) * 8192), (short)0, 8192, 0x00020000);
#pragma unroll
        for (int u = 0; u < 8; ++u)
            if ((u < 4) == (kh2 == 0)) {
                u32x4 w = {};
#pragma unroll
                for (int k = 0; k < 2; ++k) { const int tl = 2 * u + k;
                    if (tl < 15) {
                        const f32x4 v = acc[tl / 3][tl % 3] + *(const LAS f32x4*)(L + 102400 + ((qg * 15 + tl) * 64 + ln) * 16);
                        if (qg == hg) *(LAS f32x4*)(LW + 102400 + ((qg * 15 + tl) * 64 + ln) * 16) = v;
                        w[2 * k] = cvtpk(v[0], v[1]); w[2 * k + 1] = cvtpk(v[2], v[3]);
                    } }
                if (qg != hg) st16_wt(w, srdP, ln * 16, u * 1024);
            }
        TS(28);
        WAITV(0);
        BAR_L();
        TS(14);
        if (wid == 0) {
            if (ln == 8 + hg) __hip_atomic_store(pflag, base + 1u, RLX_AGENT);
#pragma unroll 1
            for (unsigned sp = 0; sp < SPIN_CAP; ++sp) {
                const unsigned v = __hip_atomic_load(pflag, RLX_AGENT);
                if (!__any(ln >= 8 && ln < 12 && v == base)) break;
                __builtin_amdgcn_s_sleep(1);
            }
        }
        BAR_L();
        TS(29);
        const srd_t srdI = __builtin_amdgcn_make_buffer_rsrc((void*)(ws + WS_PART + (size_t)blockIdx.x * 24576), (short)0, 24576, 0x00020000);
        u32x4 pv_[3];
#pragma unroll
        for (int j = 0; j < 3; ++j) pv_[j] = __builtin_amdgcn_raw_buffer_load_b128(srdI, ln * 16, j * 8192 + wid * 1024, 16);
#pragma unroll
        for (int k = 0; k < 2; ++k) {
            const int tl = 2 * wid + k;
            if (tl < 15) {
                f32x4 sum = *(const LAS f32x4*)(L + 102400 + ((hg * 15 + tl) * 64 + ln) * 16);
#pragma unroll
                for (int j = 0; j < 3; ++j) {
                    sum[0] += __uint_as_float(pv_[j][2 * k] << 16); sum[1] += __uint_as_float(pv_[j][2 * k] & 0xffff0000u);
                    sum[2] += __uint_as_float(pv_[j][2 * k + 1] << 16); sum[3] += __uint_as_float(pv_[j][2 * k + 1] & 0xffff0000u);
                }
                const int n = tl / 3, i = tl % 3;
                const int q = T16(hg, i) * 16 + qq, col = hl * 80 + n * 16 + g * 4;
                uint2 w; w.x = cvtpk(sum[0], sum[1]); w.y = cvtpk(sum[2], sum[3]);
                *(uint2*)(out + (size_t)(b * SQ + q) * DM + col) = w;
            }
        }
        TS(15);
    }
}

extern "C" void kernel_launch(void* const* d_in, const int* in_sizes, int n_in, void* d_out, int out_size, void* d_ws, size_t ws_size, hipStream_t stream) {
    const bf16* x = (const bf16*)d_in[0]; const bf16* kc = (const bf16*)d_in[1]; const bf16* vc = (const bf16*)d_in[2];
    const bf16* wq = (const bf16*)d_in[3]; const bf16* wo = (const bf16*)d_in[4];
    (void)hipFuncSetAttribute((const void*)k1_attn, hipFuncAttributeMaxDynamicSharedMemorySize, LDS_BYTES);
    k1_attn<<<256, 512, LDS_BYTES, stream>>>(x, kc, vc, wq, wo, (char*)d_ws, (bf16*)d_out);
}
```

```cpp
#include <hip/hip_runtime.h>
#include <hip/hip_bf16.h>
#include <stdint.h>
#include <type_traits>

typedef __hip_bfloat16 bf16;
using bf16x8 = __attribute__((ext_vector_type(8))) short;
using s16x4  = __attribute__((ext_vector_type(4))) short;
using f32x16 = __attribute__((ext_vector_type(16))) float;
using f32x4  = __attribute__((ext_vector_type(4))) float;
using u32x4  = __attribute__((ext_vector_type(4))) unsigned;
typedef short v4i16_t __attribute__((ext_vector_type(4)));
#define LAS __attribute__((address_space(3)))
typedef LAS const char* lds_cptr;
typedef LAS char* lds_ptr;
typedef __amdgpu_buffer_rsrc_t srd_t;
#define DI __device__ __forceinline__
#define SBAR() __builtin_amdgcn_sched_barrier(0)
#define PIN(x) asm volatile("" : "+v"(x))
#define MFMA(a, b, c) __builtin_amdgcn_mfma_f32_32x32x16_bf16(a, b, c, 0, 0, 0)

constexpr int NB = 8, SQ = 192, DM = 640, NH = 32, SK = 512, DH = 128, HD = NH * DH;
constexpr float QSCALE = 0.08838834764831845f * 1.4426950408889634f;
#define PSLIM 65536.0f

#define TS(i)

typedef float f32x2 __attribute__((ext_vector_type(2)));
typedef __bf16 bf16x2 __attribute__((ext_vector_type(2)));
DI unsigned cvtpk(float lo, float hi) { f32x2 v = {lo, hi}; bf16x2 b = __builtin_convertvector(v, bf16x2); return __builtin_bit_cast(unsigned, b); }
DI float fmul_s(float a, float b) { return a * b; }
template <bool NT>
DI void dma16(srd_t srd, unsigned voff, unsigned soff, unsigned lds_dst_) {
    const unsigned lds_dst = __builtin_amdgcn_readfirstlane(lds_dst_);
    if constexpr (NT) asm volatile("s_mov_b32 m0, %3\n\ts_nop 0\n\tbuffer_load_dwordx4 %0, %1, %2 offen nt lds" :: "v"(voff), "s"(srd), "s"(soff), "s"(lds_dst) : "m0", "memory");
    else              asm volatile("s_mov_b32 m0, %3\n\ts_nop 0\n\tbuffer_load_dwordx4 %0, %1, %2 offen lds" :: "v"(voff), "s"(srd), "s"(soff), "s"(lds_dst) : "m0", "memory");
}
template <bool NT>
DI void ld16_async(u32x4& r, srd_t srd, unsigned voff, unsigned soff) {
    if constexpr (NT) asm volatile("buffer_load_dwordx4 %0, %1, %2, %3 offen nt" : "=v"(r) : "v"(voff), "s"(srd), "s"(soff) : "memory");
    else              asm volatile("buffer_load_dwordx4 %0, %1, %2, %3 offen" : "=v"(r) : "v"(voff), "s"(srd), "s"(soff) : "memory");
}
template <bool NT>
DI void touch4(srd_t srd, unsigned voff, unsigned soff, unsigned lds_dst) {
    if constexpr (NT) asm volatile("s_mov_b32 m0, %3\n\ts_nop 0\n\tbuffer_load_dword %0, %1, %2 offen nt lds" :: "v"(voff), "s"(srd), "s"(soff), "s"(lds_dst) : "m0", "memory");
    else              asm volatile("s_mov_b32 m0, %3\n\ts_nop 0\n\tbuffer_load_dword %0, %1, %2 offen lds" :: "v"(voff), "s"(srd), "s"(soff), "s"(lds_dst) : "m0", "memory");
}
DI bf16x8 ldsr128(lds_cptr p) { return *(const LAS bf16x8*)p; }
DI s16x4 ldstr(lds_cptr p) { return __builtin_bit_cast(s16x4, __builtin_amdgcn_ds_read_tr16_b64_v4i16((LAS v4i16_t*)p)); }
DI bf16x8 cat8(s16x4 a, s16x4 b) { return __builtin_shufflevector(a, b, 0, 1, 2, 3, 4, 5, 6, 7); }
#define WAITV(N) asm volatile("s_waitcnt vmcnt(" #N ")" ::: "memory")
#define BAR_L() asm volatile("s_waitcnt lgkmcnt(0)\n\ts_barrier" ::: "memory")
DI float xhalf_max(float v) { auto rr = __builtin_amdgcn_permlane32_swap(__float_as_uint(v), __float_as_uint(v), false, false); return fmaxf(__uint_as_float(rr[0]), __uint_as_float(rr[1])); }
DI float xhalf_sum(float v) { auto rr = __builtin_amdgcn_permlane32_swap(__float_as_uint(v), __float_as_uint(v), false, false); return __uint_as_float(rr[0]) + __uint_as_float(rr[1]); }

#define KV_NT true
#define XW_NT false

#define GAS __attribute__((address_space(1)))
typedef GAS unsigned gu32;
#define RLX_AGENT __ATOMIC_RELAXED, __HIP_MEMORY_SCOPE_AGENT
#define MFMA16(a, b, c) __builtin_amdgcn_mfma_f32_16x16x32_bf16(a, b, c, 0, 0, 0)
DI void st16_wt(u32x4 v, srd_t srd, unsigned voff, unsigned soff) {
    __builtin_amdgcn_raw_buffer_store_b128(v, srd, voff, soff, 16); SBAR(); asm volatile("s_nop 1"); SBAR();
}
constexpr unsigned ECHO_KEY = 0x5A17C3E9u;
constexpr unsigned SPIN_CAP = 1u << 20;
constexpr size_t WS_O = 0, WS_PART = 16u << 20, WS_SYNC = 48u << 20;
constexpr int PAIR_OF_STEP[16] = {8, 9, 0, 1, 2, 4, 5, 6, 7, 3, 10, 11, 12, 13, 14, 15};
constexpr unsigned long long STEP_OF_PAIR = 0xFEDCBA1087659432ull;
#define WO_NT false
#define TOUCH_NT false
#define CODE_TOUCH 32768
#define NO_SELFWARM
#define OWIN 5
#define COMMON0
#define ROTF ((((hl + 2 * bl) & 7) * 9) / 8)
#define HEADF(g_, l_) ((g_) * 8 + (l_))
#define KVB 0
#define PPRIO
#define WRS 2
#define TOUCH_P 99
constexpr int SLOT = 16384;
constexpr int MOFF = 32768;
constexpr int RING0 = 40960, RSTG = 40960, RXB = 24576;
constexpr int POFF = 106496;
constexpr int LDS_BYTES = 163840;
constexpr unsigned KSLOTS = 0x04215310u;
constexpr unsigned VSLOTS = 0x32153042u;
DI int slot_addr(unsigned s) { return s * SLOT + (s >= 2 ? 8192 : 0); }
DI int own_addr(int pr) { return pr < 20 ? POFF + (pr / 5) * 12288 + (pr % 5) * 2048 : 155648 + (pr - 20) * 2048; }
DI int kslot(int t) { return slot_addr((KSLOTS >> (4 * t)) & 15u); }
DI int vslot(int t) { return slot_addr((VSLOTS >> (4 * t)) & 15u); }
#define GROUPB(bl, hl) (((bl) + (hl)) & 1)

__global__ __launch_bounds__(512, 2) void k1_attn(const bf16* __restrict__ x, const bf16* __restrict__ kc, const bf16* __restrict__ vc,
                                                  const bf16* __restrict__ wq, const bf16* __restrict__ wo, char* __restrict__ ws, bf16* __restrict__ out) {
    extern __shared__ __attribute__((aligned(16))) char lds[];
    TS(0);
    unsigned long long pc0; asm volatile("s_getpc_b64 %0" : "=s"(pc0));
    const int tid = threadIdx.x, lane = tid & 63, r32 = lane & 31, hh = lane >> 5;
    const int wid = __builtin_amdgcn_readfirstlane(tid >> 6), w4 = wid & 3;
    const int xcd = blockIdx.x & 7, jj = blockIdx.x >> 3;
    const int bl = jj >> 3, hl = jj & 7;
    const int b = (xcd >> 2) * 4 + bl, h = HEADF(xcd & 3, hl);
    const unsigned lds0 = (unsigned)(uintptr_t)lds;
    const lds_cptr L = (lds_cptr)lds;
    const lds_ptr LW = (lds_ptr)lds;
    const bf16* Kg = kc + (size_t)(b * NH + h) * SK * DH;
    const bf16* Vg = vc + (size_t)(b * NH + h) * SK * DH;
    const int qh = w4 & 1, kh = w4 >> 1;
    const int q4 = (lane & 15) >> 2, p4 = lane & 3, blk = (lane >> 4) & 1;
    LAS unsigned* flagw = (LAS unsigned*)(LW + MOFF);
    LAS unsigned* basew = (LAS unsigned*)(LW + MOFF + 128);
    const int hg = xcd & 3;
    gu32* const F = (gu32*)(ws + WS_SYNC);
    gu32* const E = (gu32*)(ws + WS_SYNC + 65536);
#define PEER(l_) ((l_) < 8 ? xcd + 8 * (bl * 8 + (l_)) : ((xcd & 4) | ((l_) & 3)) + 8 * jj)
#define PFLAG(l_) (F + (((l_) < 8 ? 0 : 256) + PEER(l_)) * 32)
    LAS float* prop = (LAS float*)(LW + MOFF + 256);
    LAS float* lsum = (LAS float*)(LW + MOFF + 2048);

    const int hdir = (bl & 1) ? -1 : 1;
    const srd_t srdWo = __builtin_amdgcn_make_buffer_rsrc((void*)(wo + hl * 80), (short)0, HD * DM * 2 - hl * 160, 0x00020000);
    auto wo_soff = [&](int g) __attribute__((always_inline)) -> unsigned {
        const int st = (int)((STEP_OF_PAIR >> (4 * (g >> 1))) & 15u);
        return (unsigned)((HEADF(hg, (hl + hdir * (st >> 1)) & 7) * 128 + (2 * (g & 1) + (st & 1)) * 32) * DM * 2);
    };
    auto wo_groups = [&](int g0, int g1, int gs) __attribute__((always_inline)) {
        int l_; asm volatile("v_mbcnt_lo_u32_b32 %0, -1, 0\n\tv_mbcnt_hi_u32_b32 %0, -1, %0" : "=v"(l_));
        unsigned wv[5];
#pragma unroll
        for (int p = 0; p < 5; ++p) { const int o_ = p * 1024 + l_ * 16, row = o_ / 160, ch = (o_ % 160) >> 4; wv[p] = row * (DM * 2) + ch * 16; }
#pragma unroll 1
        for (int g = g0; g < g1; g += gs) {
            const unsigned so = wo_soff(g);
#pragma unroll
            for (int p = 0; p < 5; ++p) dma16<WO_NT>(srdWo, wv[p], so, lds0 + g * 5120 + p * 1024);
        }
    };
    bool slow = false;
    unsigned base = 0;

    if (wid < 4) {
        if (wid == 0) {
            if (lane < 12) {
                const unsigned base = __hip_atomic_load(PFLAG(lane), RLX_AGENT);
                basew[lane] = base;
                __hip_atomic_store(E + blockIdx.x * 32 + lane, base ^ ECHO_KEY, RLX_AGENT);
            }
        }
        if (wid == 1) {
            const srd_t srdC = __builtin_amdgcn_make_buffer_rsrc((void*)(pc0 & ~127ull), (short)0, CODE_TOUCH, 0x00020000);
#pragma unroll
            for (int k = 0; k < 4; ++k) touch4<false>(srdC, lane * 128, (unsigned)(k * 8192), lds0 + MOFF + 4096u);
        }
        bf16x8 qf[3][8];
        {
            f32x16 acc[3][2];
#pragma unroll
            for (int i = 0; i < 3; ++i) { acc[i][0] = f32x16{}; acc[i][1] = f32x16{}; }
            const int xsw = (r32 >> 1) & 7;
            int xa[4];
#pragma unroll
            for (int ks = 0; ks < 4; ++ks) xa[ks] = (96 * qh + r32) * 128 + (((2 * ks + hh) ^ xsw) << 4);
            const int ps = ((p4 & 1) << 1) | (p4 >> 1);
            const int wa0 = RXB + 2048 * hh + 1024 * kh + 64 * q4 + (((2 * blk + (ps >> 1)) ^ (2 * hh)) << 4) + 8 * (ps & 1);
            const int wa1 = RXB + 2048 * hh + 1024 * kh + 64 * q4 + 256 + (((2 * blk + (ps >> 1)) ^ (2 * hh + 1)) << 4) + 8 * (ps & 1);
            bf16x8 xf[2][3]; s16x4 wl[2][2], wh[2][2];
#define RDX(buf, base, ks, i) xf[buf][i] = ldsr128(base + xa[ks] + i * 4096)
#define RDW(buf, base, ks, db) do { wl[buf][db] = ldstr(base + wa0 + ks * 4096 + db * 512); wh[buf][db] = ldstr(base + wa1 + ks * 4096 + db * 512); } while (0)
#define MM(buf, i, db) acc[i][db] = MFMA(cat8(wl[buf][db], wh[buf][db]), xf[buf][i], acc[i][db])
#define HALF(cb, base, ks) do { \
                MM(cb, 0, 0); RDW(cb ^ 1, base, ks, 0); SBAR(); \
                MM(cb, 0, 1); RDX(cb ^ 1, base, ks, 0); SBAR(); \
                MM(cb, 1, 0); RDW(cb ^ 1, base, ks, 1); SBAR(); \
                MM(cb, 1, 1); RDX(cb ^ 1, base, ks, 1); SBAR(); \
                MM(cb, 2, 0); RDX(cb ^ 1, base, ks, 2); SBAR(); \
                MM(cb, 2, 1); SBAR(); } while (0)
            BAR_L();
            TS(4);
            { const lds_cptr base = L + RING0;
              RDW(0, base, 0, 0); RDX(0, base, 0, 0); RDW(0, base, 0, 1); RDX(0, base, 0, 1); RDX(0, base, 0, 2); SBAR();
              HALF(0, base, 1); HALF(1, base, 2); HALF(0, base, 3); }
            int rb = RSTG;
#pragma unroll 1
            for (int s = 1; s < 10; ++s) {
                SBAR(); BAR_L(); SBAR();
                const lds_cptr base = L + RING0 + rb;
                HALF(1, base, 0); HALF(0, base, 1); HALF(1, base, 2); HALF(0, base, 3);
                rb = (rb == 2 * RSTG) ? 0 : rb + RSTG;
            }
            BAR_L();
            MM(1, 0, 0); MM(1, 0, 1); MM(1, 1, 0); MM(1, 1, 1); MM(1, 2, 0); MM(1, 2, 1);
            TS(5);
#pragma unroll
            for (int i = 0; i < 3; ++i)
#pragma unroll
                for (int db = 0; db < 2; ++db)
#pragma unroll
                    for (int s = 0; s < 2; ++s) {
                        u32x4 w;
#pragma unroll
                        for (int e = 0; e < 4; ++e) w[e] = cvtpk(acc[i][db][8 * s + 2 * e] * QSCALE, acc[i][db][8 * s + 2 * e + 1] * QSCALE);
                        *(LAS u32x4*)(LW + POFF + (w4 ^ 2) * 12288 + (i * 4 + db * 2 + s) * 1024 + lane * 16) = w;
                        qf[i][db * 2 + s] = __builtin_bit_cast(bf16x8, w);
                    }
            BAR_L();
#pragma unroll
            for (int i = 0; i < 3; ++i)
#pragma unroll
                for (int j = 0; j < 4; ++j) qf[i][4 + j] = ldsr128(L + POFF + w4 * 12288 + (i * 4 + j) * 1024 + lane * 16);
        }
        TS(1);
        const int krow = 32 * kh + r32;
        const int kb0 = 2048 * (krow >> 3) + 64 * (krow & 7) + ((hh ^ ((krow >> 2) & 3)) << 4);
        const int kb1 = 2048 * (krow >> 3) + 64 * (krow & 7) + (((2 + hh) ^ ((krow >> 2) & 3)) << 4);
        const int kown = kh * 1024, koth = (1 - kh) * 1024;
        bf16x8 kf[8];
        unsigned ka[4];
#define KRD(j) asm volatile("ds_read_b128 %0, %1 offset:%2" : "+v"(kf[j]) : "v"(ka[((j) >> 2) * 2 + ((j) & 1)]), "i"(512 * (((j) >> 1) & 1)) : "memory")
#define KADDR(t_) do { const unsigned kb_ = lds0 + kslot(t_); ka[0] = kb_ + kown + kb0; ka[1] = kb_ + kown + kb1; ka[2] = kb_ + koth + kb0; ka[3] = kb_ + koth + kb1; } while (0)
#define KRDALL() do { KRD(0); KRD(1); KRD(2); KRD(3); KRD(4); KRD(5); KRD(6); KRD(7); } while (0)
        f32x16 negm[3], sc;
        float l[3], m[3], lold[3];
#pragma unroll
        for (int j = 0; j < 8; ++j) kf[j] = bf16x8{};
        KADDR(0); KRDALL();
        asm volatile("s_waitcnt lgkmcnt(0)" ::: "memory"); SBAR();
        {
            const lds_ptr pw = LW + POFF + w4 * 12288 + lane * 16;
            f32x16 s0[3]; float d[3];
#pragma unroll
            for (int i = 0; i < 3; ++i) {
                s0[i] = f32x16{};
#pragma unroll
                for (int j = 0; j < 8; ++j) s0[i] = MFMA(kf[j], qf[i][j], s0[i]);
            }
            SBAR();
            KADDR(1); KRDALL();
#pragma unroll
            for (int i = 0; i < 3; ++i) {
                float pm = s0[i][0];
#pragma unroll
                for (int r = 1; r < 16; ++r) pm = fmaxf(pm, s0[i][r]);
                d[i] = xhalf_max(pm);
                prop[(w4 * 3 + i) * 32 + r32] = d[i];
            }
            BAR_L();
#pragma unroll
            for (int i = 0; i < 3; ++i) {
                m[i] = fmaxf(d[i], prop[((w4 ^ 2) * 3 + i) * 32 + r32]);
                float psum = 0.f;
#pragma unroll
                for (int r = 0; r < 16; ++r) { negm[i][r] = -m[i]; s0[i][r] -= m[i]; }
                if (i < 2) {
#pragma unroll
                    for (int r = 0; r < 16; ++r) { s0[i][r] = __builtin_amdgcn_exp2f(s0[i][r]); psum += s0[i][r]; }
#pragma unroll
                    for (int s = 0; s < 2; ++s) { u32x4 w;
#pragma unroll
                        for (int e = 0; e < 4; ++e) w[e] = cvtpk(s0[i][8 * s + 2 * e], s0[i][8 * s + 2 * e + 1]);
                        *(LAS u32x4*)(pw + (i * 2 + s) * 1024) = w; }
                }
                l[i] = psum; lold[i] = psum;
            }
            sc = s0[2];
            BAR_L(); SBAR();
        }
        TS(8);
        __builtin_amdgcn_s_setprio(1);
#define EX2(X, g) do { X[2 * (g)] = __builtin_amdgcn_exp2f(X[2 * (g)]); X[2 * (g) + 1] = __builtin_amdgcn_exp2f(X[2 * (g) + 1]); } while (0)
#define AP(X, g) do { if ((g) == 0) pa = X[0] + X[1]; else { pa += X[2 * (g)]; pa += X[2 * (g) + 1]; } w[(g) & 3] = cvtpk(X[2 * (g)], X[2 * (g) + 1]); PIN(pa); PIN(w); } while (0)
#define PST(PW, i, s) *(LAS u32x4*)(PW + ((i) * 2 + (s)) * 1024) = w
#define SMS(X, PW, i, g) do { EX2(X, g); if ((g) > 0) AP(X, (g) - 1); if ((g) == 4) PST(PW, i, 0); PIN(X); } while (0)
#define SMFIN(X, PW, i) do { AP(X, 7); PST(PW, i, 1); bad |= !(pa <= PSLIM); lold[i] = l[i]; l[i] += pa; PIN(l[i]); } while (0)
#define QK(S, i, j) S = MFMA(kf[j], qf[i][j], (j) ? S : negm[i])
        u32x4 w = {};
        int tf = 9;
#pragma unroll 1
        for (int t = 1; t < 8; ++t) {
            const lds_ptr pw = LW + POFF + w4 * 12288 + (t & 1) * 6144 + lane * 16;
            const lds_ptr pc = LW + POFF + w4 * 12288 + ((t & 1) ^ 1) * 6144 + lane * 16;
            f32x16 sa, sb; float pa; bool bad = false;
            const unsigned flv = flagw[t - 1];
            SBAR();
            QK(sa, 0, 0); SMS(sc, pc, 2, 0); SBAR(); QK(sa, 0, 1); SMS(sc, pc, 2, 1); SBAR(); QK(sa, 0, 2); SMS(sc, pc, 2, 2); SBAR();
            if (__builtin_expect(__builtin_amdgcn_readfirstlane(flv) != 0u, 0)) { tf = t - 1; break; }
            QK(sa, 0, 3); SMS(sc, pc, 2, 3); SBAR(); QK(sa, 0, 4); SMS(sc, pc, 2, 4); SBAR(); QK(sa, 0, 5); SMS(sc, pc, 2, 5); SBAR();
            QK(sa, 0, 6); SMS(sc, pc, 2, 6); SBAR(); QK(sa, 0, 7); SMS(sc, pc, 2, 7); SBAR();
            QK(sb, 1, 0); SMFIN(sc, pc, 2); KADDR(t < 7 ? t + 1 : 7); SBAR();
            QK(sb, 1, 1); SBAR(); SMS(sa, pw, 0, 0); SBAR(); QK(sb, 1, 2); SMS(sa, pw, 0, 1); SBAR(); QK(sb, 1, 3); SMS(sa, pw, 0, 2); SBAR();
            QK(sb, 1, 4); SMS(sa, pw, 0, 3); SBAR(); QK(sb, 1, 5); SMS(sa, pw, 0, 4); SBAR(); QK(sb, 1, 6); SMS(sa, pw, 0, 5); SBAR(); QK(sb, 1, 7); SMS(sa, pw, 0, 6); SBAR();
            QK(sc, 2, 0); SBAR(); KRD(0); SMS(sa, pw, 0, 7); SBAR(); SMFIN(sa, pw, 0); SBAR();
            QK(sc, 2, 1); SBAR(); KRD(1); SMS(sb, pw, 1, 0); SBAR(); SMS(sb, pw, 1, 1); SBAR();
            QK(sc, 2, 2); SBAR(); KRD(2); SMS(sb, pw, 1, 2); SBAR(); QK(sc, 2, 3); SBAR(); KRD(3); SMS(sb, pw, 1, 3); SBAR();
            QK(sc, 2, 4); SBAR(); KRD(4); SMS(sb, pw, 1, 4); SBAR(); QK(sc, 2, 5); SBAR(); KRD(5); SMS(sb, pw, 1, 5); SBAR(); SMS(sb, pw, 1, 6); SBAR();
            QK(sc, 2, 6); SBAR(); KRD(6); SMS(sb, pw, 1, 7); SBAR(); QK(sc, 2, 7); SBAR(); KRD(7); SMFIN(sb, pw, 1); SBAR();
            if (__builtin_expect(__any(bad), 0)) { if (lane == 0) flagw[t] = 1u; }
            if (t == 7) { lsum[(w4 * 3 + 0) * 32 + r32] = xhalf_sum(l[0]); lsum[(w4 * 3 + 1) * 32 + r32] = xhalf_sum(l[1]); }
            BAR_L(); SBAR();
            if (t == 1) TS(9); if (t == 3) TS(10); if (t == 5) TS(11);
        }
        if (tf == 9) {
            if (__builtin_amdgcn_readfirstlane(flagw[7]) != 0u) tf = 7;
            else {
                const lds_ptr pc = LW + POFF + w4 * 12288 + 6144 + lane * 16;
                float pa; bool bad = false;
                lold[0] = l[0]; lold[1] = l[1];
                SMS(sc, pc, 2, 0); SMS(sc, pc, 2, 1); SMS(sc, pc, 2, 2); SMS(sc, pc, 2, 3); SMS(sc, pc, 2, 4); SMS(sc, pc, 2, 5); SMS(sc, pc, 2, 6); SMS(sc, pc, 2, 7); SMFIN(sc, pc, 2);
                if (__builtin_expect(__any(bad), 0)) { if (lane == 0) flagw[8] = 1u; }
                lsum[(w4 * 3 + 2) * 32 + r32] = xhalf_sum(l[2]);
                BAR_L();
                if (__builtin_amdgcn_readfirstlane(flagw[8]) != 0u) tf = 8;
            }
        }
        slow = tf < 9;
        if (__builtin_expect(slow, 0)) {
#pragma unroll
            for (int i = 0; i < 3; ++i) l[i] = lold[i];
            BAR_L();
#pragma unroll 1
            for (int t = tf - 1; t < 8; ++t) {
                const bool all = t >= tf;
                const lds_ptr pw = LW + POFF + w4 * 12288 + (t & 1) * 6144 + lane * 16;
#pragma unroll
                for (int j = 0; j < 8; ++j) { const int s = (j < 4) ? 4 * kh + j : 4 * (1 - kh) + (j - 4);
                    kf[j] = *(const bf16x8*)(Kg + (size_t)(t * 64 + krow) * DH + s * 16 + hh * 8); }
                f32x16 s0[3]; float d[3];
#pragma unroll
                for (int i = 0; i < 3; ++i) {
                    s0[i] = f32x16{};
#pragma unroll
                    for (int j = 0; j < 8; ++j) s0[i] = MFMA(kf[j], qf[i][j], s0[i]);
                    float pm = s0[i][0];
#pragma unroll
                    for (int r = 1; r < 16; ++r) pm = fmaxf(pm, s0[i][r]);
                    d[i] = (all || i == 2) ? fmaxf(xhalf_max(pm) - m[i], 0.f) : 0.f;
                    prop[(w4 * 3 + i) * 32 + r32] = d[i];
                }
                BAR_L();
#pragma unroll
                for (int i = 0; i < 3; ++i) {
                    const float dl = fmaxf(d[i], prop[((w4 ^ 2) * 3 + i) * 32 + r32]);
                    m[i] += dl;
                    float psum = l[i] * __builtin_amdgcn_exp2f(-dl);
#pragma unroll
                    for (int r = 0; r < 16; ++r) { s0[i][r] = (all || i == 2) ? __builtin_amdgcn_exp2f(s0[i][r] - m[i]) : 0.f; psum += s0[i][r]; }
                    l[i] = psum;
#pragma unroll
                    for (int s = 0; s < 2; ++s) { u32x4 w2;
#pragma unroll
                        for (int e = 0; e < 4; ++e) w2[e] = cvtpk(s0[i][8 * s + 2 * e], s0[i][8 * s + 2 * e + 1]);
                        *(LAS u32x4*)(pw + (i * 2 + s) * 1024) = w2; }
                    lsum[(w4 * 3 + i) * 32 + r32] = xhalf_sum(psum);
                }
                BAR_L();
            }
            BAR_L();
        }
        if (wid == 0) {
            base = basew[lane < 12 ? lane : 0];
            const unsigned want = (lane < 8 ? basew[hl] : basew[8 + hg]) ^ ECHO_KEY;
            gu32* const pecho = E + PEER(lane) * 32 + (lane < 8 ? hl : 8 + hg);
#pragma unroll 1
            for (unsigned sp = 0; sp < SPIN_CAP; ++sp) {
                const unsigned echo = __hip_atomic_load(pecho, RLX_AGENT);
                if (!__any(lane < 12 && echo != want)) break;
                __builtin_amdgcn_s_sleep(2);
            }
        }
        TS(2);
        __builtin_amdgcn_s_setprio(0);
        BAR_L();
    } else {
        const srd_t srdK = __builtin_amdgcn_make_buffer_rsrc((void*)Kg, (short)0, SK * DH * 2, 0x00020000);
        const srd_t srdV = __builtin_amdgcn_make_buffer_rsrc((void*)Vg, (short)0, SK * DH * 2, 0x00020000);
        const int arow = 8 * (w4 >> 1) + ((lane >> 2) & 7), ach = 4 * (2 * (w4 & 1) + hh) + (p4 ^ ((2 * (w4 >> 1) + blk) & 3));
        const unsigned kvoff = arow * 256 + ach * 16;
        auto kv_unit = [&](bool isV, int t) {
#pragma unroll
            for (int i = 0; i < 4; ++i)
                dma16<KV_NT>(isV ? srdV : srdK, kvoff, (unsigned)(t * 16384 + i * 4096), lds0 + (isV ? vslot(t) : kslot(t)) + (4 * i + w4) * 1024);
        };
        u32x4 pre[8];
        {
            const int rot = ROTF;
            const srd_t srdX = __builtin_amdgcn_make_buffer_rsrc((void*)(x + (size_t)b * SQ * DM), (short)0, SQ * DM * 2, 0x00020000);
            const srd_t srdW = __builtin_amdgcn_make_buffer_rsrc((void*)(wq + h * DH), (short)0, DM * HD * 2 - h * DH * 2, 0x00020000);
            const unsigned xvoff = (8 * w4 + (lane >> 3)) * (DM * 2) + (((lane & 7) ^ ((4 * (w4 & 1) + (lane >> 4)) & 7)) << 4);
            const unsigned wvoff = arow * (HD * 2) + ach * 16;
            auto issue = [&](int s, int buf) __attribute__((always_inline)) {
                int cs = s + rot; if (cs >= 10) cs -= 9; if (s == 0) cs = 0;
                const unsigned dst = lds0 + RING0 + buf;
#pragma unroll
                for (int i = 0; i < 4; ++i) dma16<XW_NT>(srdW, wvoff, (unsigned)((cs * 64 + i * 16) * HD * 2), dst + RXB + (4 * i + w4) * 1024);
#pragma unroll
                for (int i = 0; i < 6; ++i) dma16<XW_NT>(srdX, xvoff, (unsigned)(i * 32 * DM * 2 + cs * 128), dst + (4 * i + w4) * 1024);
            };
            if (wid == 4 && lane < 16) flagw[lane] = 0u;
            issue(0, 0); issue(1, RSTG);
            int rb = 2 * RSTG;
#pragma unroll 1
            for (int s = 0; s < 8; ++s) { WAITV(10); BAR_L(); issue(s + 2, rb); rb = (rb == 2 * RSTG) ? 0 : rb + RSTG; }
#define PRELD(n0, n1) _Pragma("unroll") for (int n = n0; n < n1; ++n) ld16_async<KV_NT>(pre[n], n < 4 ? srdV : srdK, kvoff, (unsigned)((n < 4 ? 0 : 2) * 16384 + (n & 3) * 4096))
            kv_unit(false, 0);
            WAITV(14); BAR_L(); kv_unit(false, 1); PRELD(0, 4);
            WAITV(12); BAR_L(); PRELD(4, 8);
            BAR_L();
            kv_unit(true, 1); kv_unit(false, 3);
            WAITV(16);
            BAR_L();
        }
        f32x16 o[3][2];
#pragma unroll
        for (int i = 0; i < 3; ++i) { o[i][0] = f32x16{}; o[i][1] = f32x16{}; }
        const int vb0 = 1024 * kh + 64 * (4 * hh + q4) + (((2 * blk + (p4 >> 1)) ^ hh) << 4) + 8 * (p4 & 1);
        const int vb1 = 1024 * kh + 2048 + 64 * (4 * hh + q4) + (((2 * blk + (p4 >> 1)) ^ (2 + hh)) << 4) + 8 * (p4 & 1);
        const lds_cptr pb0 = L + POFF + qh * 12288 + lane * 16;
        s16x4 vl[2][4][2], vh[2][4][2];
        bf16x8 pf[2][3];
        unsigned wv[5];
#pragma unroll
        for (int p = 0; p < 5; ++p) { const int o_ = p * 1024 + lane * 16, row = o_ / 160, ch = (o_ % 160) >> 4; wv[p] = row * (DM * 2) + ch * 16; }
        const srd_t srdO = __builtin_amdgcn_make_buffer_rsrc((void*)(ws + WS_O + (size_t)(b * NH + hg * 8 + hl) * 49152), (short)0, 49152, 0x00020000);
        u32x4 wr[10];
        const int wg0 = 10 + w4, wg1 = w4 < 2 ? 14 + w4 : 4 + w4;
        u32x4 ow[3][4];
        auto opack = [&](int i) __attribute__((always_inline)) {
            const float rl = 1.0f / (lsum[(qh * 3 + i) * 32 + r32] + lsum[((qh + 2) * 3 + i) * 32 + r32]);
#pragma unroll
            for (int db = 0; db < 2; ++db)
#pragma unroll
                for (int s = 0; s < 2; ++s) {
#pragma unroll
                    for (int e = 0; e < 4; ++e) ow[i][db * 2 + s][e] = cvtpk(o[i][db][8 * s + 2 * e] * rl, o[i][db][8 * s + 2 * e + 1] * rl);
                    st16_wt(ow[i][db * 2 + s], srdO, (r32 >> 4) * 4096 + hh * 256 + (r32 & 15) * 16, (3 * qh + i) * 8192 + (kh * 2 + db) * 1024 + s * 512); }
        };
        auto olds = [&](int i) __attribute__((always_inline)) {
#pragma unroll
            for (int db = 0; db < 2; ++db) { const lds_ptr p_ = LW + own_addr((3 * qh + i) * 4 + kh * 2 + db) + lane * 16;
                *(LAS u32x4*)p_ = ow[i][db * 2]; *(LAS u32x4*)(p_ + 1024) = ow[i][db * 2 + 1]; }
        };
#define RDV(X, vb, ks, db) do { vl[X][ks][db] = ldstr(vb + vb0 + (ks) * 4096 + (db) * 512); vh[X][ks][db] = ldstr(vb + vb1 + (ks) * 4096 + (db) * 512); } while (0)
#define RDP(buf, blk_, ks, i) pf[buf][i] = ldsr128(pb0 + ((blk_) & 1) * 6144 + ((ks) >> 1) * 24576 + ((i) * 2 + ((ks) & 1)) * 1024)
#define PVM(X, buf, ks, i, db) o[i][db] = MFMA(cat8(vl[X][ks][db], vh[X][ks][db]), pf[buf][i], o[i][db])
        auto period = [&](auto tag) __attribute__((always_inline)) -> bool {
            constexpr int P = decltype(tag)::value, X = P & 1;
            constexpr bool AB = P <= 8, C = P >= 2;
            const lds_cptr vb = L + vslot(AB ? P - 1 : 0);
            auto dm = [&](int n) __attribute__((always_inline)) {
                if constexpr (P <= 5) {
                    if (n < 4) dma16<KV_NT>(srdV, kvoff, (unsigned)((P + 1) * 16384 + n * 4096), lds0 + vslot(P + 1) + (4 * n + w4) * 1024);
                    else if constexpr (P < 5) dma16<KV_NT>(srdK, kvoff, (unsigned)((P + 3) * 16384 + (n - 4) * 4096), lds0 + kslot(P + 3) + (4 * (n - 4) + w4) * 1024);
                    else dma16<KV_NT>(srdV, kvoff, (unsigned)(7 * 16384 + (n - 4) * 4096), lds0 + vslot(7) + (4 * (n - 4) + w4) * 1024);
                } else if constexpr (P <= 8) {
                    const int g = (P == 6) ? 16 + w4 : (P == 7) ? w4 : (w4 < 2 ? 4 + w4 : 6 + w4);
                    if (n < 5) dma16<WO_NT>(srdWo, wv[n], wo_soff(g), lds0 + g * 5120 + n * 1024);
                }
            };
            auto rd = [&](int buf, int ks, int part) __attribute__((always_inline)) {
                if (part == 0) { if (C) RDP(buf, P - 2, ks, 2); }
                if (part == 1) { if (AB) RDV(X, vb, ks, 0); }
                if (part == 2) { if (AB) RDP(buf, P - 1, ks, 0); }
                if (part == 3) { if (AB) RDV(X, vb, ks, 1); }
                if (part == 4) { if (AB) RDP(buf, P - 1, ks, 1); }
            };
            auto wrl = [&](int n) __attribute__((always_inline)) {
                if constexpr (P == 6 || P == 7) { if (n < 5) ld16_async<false>(wr[(P - 6) * 5 + n], srdWo, wv[n], wo_soff(P == 6 ? wg0 : wg1)); }
            };
            const unsigned flv = flagw[P - 1];
            rd(0, 0, 0); rd(0, 0, 1); rd(0, 0, 2); rd(0, 0, 3); rd(0, 0, 4); SBAR();
            if (__builtin_expect(__builtin_amdgcn_readfirstlane(flv) != 0u, 0)) {
                if constexpr (P == 7 || P == 8) asm volatile("s_waitcnt vmcnt(0)" :: "v"(wr[0]), "v"(wr[1]), "v"(wr[2]), "v"(wr[3]), "v"(wr[4]) : "memory");
                if constexpr (P == 8) asm volatile("" :: "v"(wr[5]), "v"(wr[6]), "v"(wr[7]), "v"(wr[8]), "v"(wr[9]) : "memory");
                return true;
            }
            if constexpr (P == 9) { olds(0); olds(1); }
#pragma unroll
            for (int ks = 0; ks < 4; ++ks) {
                const int cb = ks & 1; const bool more = ks < 3;
                if (C) PVM(X ^ 1, cb, ks, 2, 0); if (more) rd(cb ^ 1, ks + 1, 0); SBAR();
                if (C) PVM(X ^ 1, cb, ks, 2, 1); if (more) rd(cb ^ 1, ks + 1, 1); wrl(2 * ks); SBAR();
                if (AB) PVM(X, cb, ks, 0, 0); if (more) rd(cb ^ 1, ks + 1, 2); SBAR();
                if (AB) PVM(X, cb, ks, 0, 1); if (more) rd(cb ^ 1, ks + 1, 3); dm(2 * ks); SBAR();
                if (AB) PVM(X, cb, ks, 1, 0); if (more) rd(cb ^ 1, ks + 1, 4); wrl(2 * ks + 1); SBAR();
                if (AB) PVM(X, cb, ks, 1, 1); dm(2 * ks + 1); SBAR();
            }
            if constexpr (P <= 5) WAITV(8); else if constexpr (P == 6) WAITV(14); else if constexpr (P == 7) WAITV(20);
            else if constexpr (P == 8) asm volatile("s_waitcnt vmcnt(5)" : "+v"(wr[0]), "+v"(wr[1]), "+v"(wr[2]), "+v"(wr[3]), "+v"(wr[4]), "+v"(wr[5]), "+v"(wr[6]), "+v"(wr[7]), "+v"(wr[8]), "+v"(wr[9]) :: "memory");
            if constexpr (P == 8) { opack(0); opack(1); }
            if constexpr (P <= 8) BAR_L();
            return false;
        };
        BAR_L();
        asm volatile("s_waitcnt vmcnt(8)" : "+v"(pre[0]), "+v"(pre[1]), "+v"(pre[2]), "+v"(pre[3]), "+v"(pre[4]), "+v"(pre[5]), "+v"(pre[6]), "+v"(pre[7]) :: "memory");
#pragma unroll
        for (int n = 0; n < 8; ++n) *(LAS u32x4*)(LW + slot_addr(2 + (n >> 2)) + (4 * (n & 3) + w4) * 1024 + lane * 16) = pre[n];
        BAR_L();
        int tf = 9;
#define PERIOD(P) if (tf == 9) { if (period(std::integral_constant<int, P>{})) tf = P - 1; }
        PERIOD(1) PERIOD(2) PERIOD(3) PERIOD(4) PERIOD(5) PERIOD(6) PERIOD(7) PERIOD(8) PERIOD(9)
        slow = tf < 9;
        if (__builtin_expect(slow, 0)) {
            WAITV(0); BAR_L();
#pragma unroll 1
            for (int t = tf - 1; t < 8; ++t) {
                const int sl = slot_addr(2 + (t & 1));
#pragma unroll
                for (int n = 0; n < 4; ++n) dma16<KV_NT>(srdV, kvoff, (unsigned)(t * 16384 + n * 4096), lds0 + sl + (4 * n + w4) * 1024);
                BAR_L();
#pragma unroll
                for (int i = 0; i < 3; ++i) {
                    const float dl = fmaxf(prop[(qh * 3 + i) * 32 + r32], prop[((qh + 2) * 3 + i) * 32 + r32]);
                    const float f = __builtin_amdgcn_exp2f(-dl);
#pragma unroll
                    for (int r = 0; r < 16; ++r) { o[i][0][r] *= f; o[i][1][r] *= f; }
                }
                WAITV(0);
                BAR_L();
                const lds_cptr vb = L + sl;
#pragma unroll
                for (int ks = 0; ks < 4; ++ks) {
                    RDV(0, vb, ks, 0); RDV(0, vb, ks, 1); RDP(0, t, ks, 0); RDP(0, t, ks, 1); RDP(0, t, ks, 2);
#pragma unroll
                    for (int i = 0; i < 3; ++i) { PVM(0, 0, ks, i, 0); PVM(0, 0, ks, i, 1); }
                }
            }
            BAR_L();
#pragma unroll
            for (int p = 0; p < 5; ++p) { wr[p] = __builtin_amdgcn_raw_buffer_load_b128(srdWo, wv[p], wo_soff(wg0), 0); wr[5 + p] = __builtin_amdgcn_raw_buffer_load_b128(srdWo, wv[p], wo_soff(wg1), 0); }
#pragma unroll
            for (int p = 0; p < 10; ++p) PIN(wr[p]);
            opack(0); opack(1); olds(0); olds(1);
        }
        TS(6);
        opack(2); olds(2);
        TS(3);
        TS(16);
        BAR_L();
#pragma unroll
        for (int p = 0; p < 5; ++p) { *(LAS u32x4*)(LW + wg0 * 5120 + p * 1024 + lane * 16) = wr[p]; *(LAS u32x4*)(LW + wg1 * 5120 + p * 1024 + lane * 16) = wr[5 + p]; }
    }
    int ln; asm volatile("v_mbcnt_lo_u32_b32 %0, -1, 0\n\tv_mbcnt_hi_u32_b32 %0, -1, %0" : "=v"(ln));
    const int qg = wid & 3, kh2 = wid >> 2;
    gu32* const pflag = PFLAG(ln);
    TS(17);
    if (__builtin_expect(slow, 0)) { if (wid < 4) { wo_groups(w4, 4, 4); wo_groups(w4 < 2 ? 4 + w4 : 6 + w4, 10, 8); wo_groups(16 + w4, 20, 4); } WAITV(0); BAR_L(); }
    {
        const int g = ln >> 4, qq = ln & 15;
        const srd_t srdOg = __builtin_amdgcn_make_buffer_rsrc((void*)(ws + WS_O + (size_t)(b * NH + hg * 8) * 49152), (short)0, 8 * 49152, 0x00020000);
        const unsigned ov0 = (g >> 1) * 1024 + (g & 1) * 512 + qq * 16, ov = ln * 16 + kh2 * 2048;
#define T16(qg_, i) (6 * ((qg_) >> 1) + ((i) < 2 ? 2 * ((qg_) & 1) + (i) : 4 + ((qg_) & 1)))
#define JISC(j) ((j) >= 8 && (j) < 20 && ((j) & 1))
#define JSTEP(j) ((j) < 8 ? (j) : (j) >= 20 ? (j) - 6 : JISC(j) ? ((j) - 5) / 2 : 4 + (j) / 2)
#define JHAS(j, i) (JISC(j) ? (i) == 2 : ((j) >= 2 && (j) < 8) ? (i) < 2 : true)
        constexpr int OW = OWIN;
        u32x4 of[OW][3];
#define LDOG(st, i) __builtin_amdgcn_raw_buffer_load_b128(srdOg, ov, ((hl + hdir * ((st) >> 1)) & 7) * 49152 + T16(qg, i) * 4096 + ((st) & 1) * 1024, 16)
#define LDO1(j, i) do { if (JHAS(j, i)) of[(j) % OW][i] = LDOG(JSTEP(j), i); } while (0)
#pragma unroll
        for (int st = 0; st < 2; ++st)
#pragma unroll
            for (int i = 0; i < 3; ++i) { const int t16 = T16(qg, i);
                of[st][i] = *(const LAS u32x4*)(L + own_addr((t16 >> 1) * 4 + kh2 * 2 + st) + (t16 & 1) * 256 + ov0); }
        f32x4 acc[5][3];
#pragma unroll
        for (int n = 0; n < 5; ++n)
#pragma unroll
            for (int i = 0; i < 3; ++i) acc[n][i] = f32x4{};
        lds_cptr wb[3];
        wb[0] = L + kh2 * 5120 + (16 * (g >> 1) + 4 * (g & 1) + (qq >> 2)) * 160 + (ln & 3) * 8;
        wb[1] = wb[0] + 61440; wb[2] = wb[0] + 122880;
        PIN(wb[1]); PIN(wb[2]);
        s16x4 wl[2][5], wh[2][5];
#define RDWO(buf, st, n) do { const lds_cptr p_ = wb[PAIR_OF_STEP[st] / 6] + (PAIR_OF_STEP[st] % 6) * 10240 + (n) * 32; wl[buf][n] = ldstr(p_); wh[buf][n] = ldstr(p_ + 1280); } while (0)
#pragma unroll
        for (int n = 0; n < 5; ++n) RDWO(0, 0, n);
        if (wid >= 4) WAITV(4);
        BAR_L();
        TS(18);
        if (wid == 0) { if (ln == hl) __hip_atomic_store(pflag, base + 1u, RLX_AGENT); }
        else if (wid < 4) wo_groups(19 + w4, 32, 3);
        SBAR();
        unsigned fc = 0;
#pragma unroll
        for (int j = 0; j < 22; ++j) {
            if (j == 1) {
                if (wid >= 4) WAITV(0);
                BAR_L();
                TS(7);
                if (wid == 0 && ln == hl) __hip_atomic_store(pflag, base + 2u, RLX_AGENT);
                SBAR();
            }
            if (j == 2) {
                TS(21);
                if (wid == 0) {
#pragma unroll 1
                    for (unsigned sp = 0; sp < SPIN_CAP; ++sp) {
                        const unsigned v = __hip_atomic_load(pflag, RLX_AGENT);
                        if (!__any(ln < 8 && v == base)) break;
                        __builtin_amdgcn_s_sleep(1);
                    }
                }
                TS(22);
                BAR_L();
                TS(12);
                if (kh2) __builtin_amdgcn_s_setprio(1);
#pragma unroll
                for (int j2 = 2; j2 < 2 + OW - 1; ++j2) { LDO1(j2, 0); LDO1(j2, 1); LDO1(j2, 2); }
                SBAR();
            }
            if (j == 9 - OW - 1 && wid == 0) fc = __hip_atomic_load(pflag, RLX_AGENT);
            if (j == 9 - OW) {
                if (wid == 0) {
#pragma unroll 1
                    for (unsigned sp = 0; __any(ln < 8 && fc - base < 2u) && sp < SPIN_CAP; ++sp) { __builtin_amdgcn_s_sleep(1); fc = __hip_atomic_load(pflag, RLX_AGENT); }
                }
                BAR_L();
                TS(24);
            }
            if (j == 20) { TS(25); BAR_L(); TS(26); }
#pragma unroll
            for (int n = 0; n < 5; ++n) {
#pragma unroll
                for (int i = 0; i < 3; ++i)
                    if (JHAS(j, i)) acc[n][i] = MFMA16(cat8(wl[j & 1][n], wh[j & 1][n]), __builtin_bit_cast(bf16x8, of[j % OW][i]), acc[n][i]);
                if (j < 21) RDWO((j + 1) & 1, JSTEP(j + 1), n);
                if (j >= 2 && j + OW - 1 < 22 && n < 3) LDO1(j + OW - 1, n);
                SBAR();
            }
        }
        TS(13);
#pragma unroll
        for (int tl = 0; tl < 15; ++tl)
            if ((tl < 8) != (kh2 == 0)) *(LAS f32x4*)(LW + ((qg * 15 + tl) * 64 + ln) * 16) = acc[tl / 3][tl % 3];
        BAR_L(); TS(27);
        const int dst = ((xcd & 4) | qg) + 8 * jj;
        const srd_t srdP = __builtin_amdgcn_make_buffer_rsrc((void*)(ws + WS_PART + (size_t)dst * 24576 + ((hg - qg - 1) & 3) * 8192), (short)0, 8192, 0x00020000);
#pragma unroll
        for (int u = 0; u < 8; ++u)
            if ((u < 4) == (kh2 == 0)) {
                u32x4 w = {};
#pragma unroll
                for (int k = 0; k < 2; ++k) { const int tl = 2 * u + k;
                    if (tl < 15) {
                        const f32x4 v = acc[tl / 3][tl % 3] + *(const LAS f32x4*)(L + ((qg * 15 + tl) * 64 + ln) * 16);
                        if (qg == hg) *(LAS f32x4*)(LW + ((qg * 15 + tl) * 64 + ln) * 16) = v;
                        w[2 * k] = cvtpk(v[0], v[1]); w[2 * k + 1] = cvtpk(v[2], v[3]);
                    } }
                if (qg != hg) st16_wt(w, srdP, ln * 16, u * 1024);
            }
        TS(28);
        WAITV(0);
        BAR_L();
        TS(14);
        if (wid == 0) {
            if (ln == 8 + hg) __hip_atomic_store(pflag, base + 1u, RLX_AGENT);
#pragma unroll 1
            for (unsigned sp = 0; sp < SPIN_CAP; ++sp) {
                const unsigned v = __hip_atomic_load(pflag, RLX_AGENT);
                if (!__any(ln >= 8 && ln < 12 && v == base)) break;
                __builtin_amdgcn_s_sleep(1);
            }
        }
        BAR_L();
        TS(29);
        const srd_t srdI = __builtin_amdgcn_make_buffer_rsrc((void*)(ws + WS_PART + (size_t)blockIdx.x * 24576), (short)0, 24576, 0x00020000);
        u32x4 pv_[3];
#pragma unroll
        for (int j = 0; j < 3; ++j) pv_[j] = __builtin_amdgcn_raw_buffer_load_b128(srdI, ln * 16, j * 8192 + wid * 1024, 16);
#pragma unroll
        for (int k = 0; k < 2; ++k) {
            const int tl = 2 * wid + k;
            if (tl < 15) {
                f32x4 sum = *(const LAS f32x4*)(L + ((hg * 15 + tl) * 64 + ln) * 16);
#pragma unroll
                for (int j = 0; j < 3; ++j) {
                    sum[0] += __uint_as_float(pv_[j][2 * k] << 16); sum[1] += __uint_as_float(pv_[j][2 * k] & 0xffff0000u);
                    sum[2] += __uint_as_float(pv_[j][2 * k + 1] << 16); sum[3] += __uint_as_float(pv_[j][2 * k + 1] & 0xffff0000u);
                }
                const int n = tl / 3, i = tl % 3;
                const int q = T16(hg, i) * 16 + qq, col = hl * 80 + n * 16 + g * 4;
                uint2 w; w.x = cvtpk(sum[0], sum[1]); w.y = cvtpk(sum[2], sum[3]);
                *(uint2*)(out + (size_t)(b * SQ + q) * DM + col) = w;
            }
        }
        TS(15);
    }
}

extern "C" void kernel_launch(void* const* d_in, const int* in_sizes, int n_in, void* d_out, int out_size, void* d_ws, size_t ws_size, hipStream_t stream) {
    const bf16* x = (const bf16*)d_in[0]; const bf16* kc = (const bf16*)d_in[1]; const bf16* vc = (const bf16*)d_in[2];
    const bf16* wq = (const bf16*)d_in[3]; const bf16* wo = (const bf16*)d_in[4];
    (void)hipFuncSetAttribute((const void*)k1_attn, hipFuncAttributeMaxDynamicSharedMemorySize, LDS_BYTES);
    k1_attn<<<256, 512, LDS_BYTES, stream>>>(x, kc, vc, wq, wo, (char*)d_ws, (bf16*)d_out);
}
```

```cpp
#include <hip/hip_runtime.h>
#include <hip/hip_bf16.h>
#include <stdint.h>
#include <type_traits>

typedef __hip_bfloat16 bf16;
using bf16x8 = __attribute__((ext_vector_type(8))) short;
using s16x4  = __attribute__((ext_vector_type(4))) short;
using f32x16 = __attribute__((ext_vector_type(16))) float;
using f32x4  = __attribute__((ext_vector_type(4))) float;
using u32x4  = __attribute__((ext_vector_type(4))) unsigned;
using u32x2  = __attribute__((ext_vector_type(2))) unsigned;
typedef short v4i16_t __attribute__((ext_vector_type(4)));
#define LAS __attribute__((address_space(3)))
typedef LAS const char* lds_cptr;
typedef LAS char* lds_ptr;
typedef __amdgpu_buffer_rsrc_t srd_t;
#define DI __device__ __forceinline__
#define SBAR() __builtin_amdgcn_sched_barrier(0)
#define PIN(x) asm volatile("" : "+v"(x))
#define MFMA(a, b, c) __builtin_amdgcn_mfma_f32_32x32x16_bf16(a, b, c, 0, 0, 0)

constexpr int NB = 8, SQ = 192, DM = 640, NH = 32, SK = 512, DH = 128, HD = NH * DH;
constexpr float QSCALE = 0.08838834764831845f * 1.4426950408889634f;
#define PSLIM 65536.0f

typedef float f32x2 __attribute__((ext_vector_type(2)));
typedef __bf16 bf16x2 __attribute__((ext_vector_type(2)));
DI unsigned cvtpk(float lo, float hi) { f32x2 v = {lo, hi}; bf16x2 b = __builtin_convertvector(v, bf16x2); return __builtin_bit_cast(unsigned, b); }
template <bool NT>
DI void dma16(srd_t srd, unsigned voff, unsigned soff, unsigned lds_dst_) {
    const unsigned lds_dst = __builtin_amdgcn_readfirstlane(lds_dst_);
    if constexpr (NT) asm volatile("s_mov_b32 m0, %3\n\ts_nop 0\n\tbuffer_load_dwordx4 %0, %1, %2 offen nt lds" :: "v"(voff), "s"(srd), "s"(soff), "s"(lds_dst) : "m0", "memory");
    else              asm volatile("s_mov_b32 m0, %3\n\ts_nop 0\n\tbuffer_load_dwordx4 %0, %1, %2 offen lds" :: "v"(voff), "s"(srd), "s"(soff), "s"(lds_dst) : "m0", "memory");
}
template <bool NT>
DI void ld16_async(u32x4& r, srd_t srd, unsigned voff, unsigned soff) {
    if constexpr (NT) asm volatile("buffer_load_dwordx4 %0, %1, %2, %3 offen nt" : "=v"(r) : "v"(voff), "s"(srd), "s"(soff) : "memory");
    else              asm volatile("buffer_load_dwordx4 %0, %1, %2, %3 offen" : "=v"(r) : "v"(voff), "s"(srd), "s"(soff) : "memory");
}
DI void touch4(srd_t srd, unsigned voff, unsigned soff, unsigned lds_dst) {
    asm volatile("s_mov_b32 m0, %3\n\ts_nop 0\n\tbuffer_load_dword %0, %1, %2 offen lds" :: "v"(voff), "s"(srd), "s"(soff), "s"(lds_dst) : "m0", "memory");
}
DI bf16x8 ldsr128(lds_cptr p) { return *(const LAS bf16x8*)p; }
DI s16x4 ldstr(lds_cptr p) { return __builtin_bit_cast(s16x4, __builtin_amdgcn_ds_read_tr16_b64_v4i16((LAS v4i16_t*)p)); }
DI bf16x8 cat8(s16x4 a, s16x4 b) { return __builtin_shufflevector(a, b, 0, 1, 2, 3, 4, 5, 6, 7); }
#define WAITV(N) asm volatile("s_waitcnt vmcnt(" #N ")" ::: "memory")
#define BAR_L() asm volatile("s_waitcnt lgkmcnt(0)\n\ts_barrier" ::: "memory")
DI float xhalf_max(float v) { auto rr = __builtin_amdgcn_permlane32_swap(__float_as_uint(v), __float_as_uint(v), false, false); return fmaxf(__uint_as_float(rr[0]), __uint_as_float(rr[1])); }
DI float xhalf_sum(float v) { auto rr = __builtin_amdgcn_permlane32_swap(__float_as_uint(v), __float_as_uint(v), false, false); return __uint_as_float(rr[0]) + __uint_as_float(rr[1]); }

#define GAS __attribute__((address_space(1)))
typedef GAS unsigned gu32;
#define RLX_AGENT __ATOMIC_RELAXED, __HIP_MEMORY_SCOPE_AGENT
#define MFMA16(a, b, c) __builtin_amdgcn_mfma_f32_16x16x32_bf16(a, b, c, 0, 0, 0)
DI void st16_wt(u32x4 v, srd_t srd, unsigned voff, unsigned soff) {
    __builtin_amdgcn_raw_buffer_store_b128(v, srd, voff, soff, 16); SBAR(); asm volatile("s_nop 1"); SBAR();
}
constexpr unsigned ECHO_KEY = 0x5A17C3E9u;
constexpr unsigned SPIN_CAP = 1u << 19;
constexpr size_t WS_O = 0, WS_PART = 16u << 20, WS_SYNC = 48u << 20;
constexpr int PAIR_OF_STEP[16] = {8, 9, 0, 1, 2, 4, 5, 6, 7, 3, 10, 11, 12, 13, 14, 15};
constexpr unsigned long long STEP_OF_PAIR = 0xFEDCBA1087659432ull;
constexpr unsigned CODE_TOUCH = 32768;
constexpr int SLOT = 16384;
constexpr int MOFF = 32768;
constexpr int RING0 = 40960, RSTG = 40960, RXB = 24576;
constexpr int POFF = 106496;
constexpr int LDS_BYTES = 163840;
constexpr unsigned KSLOTS = 0x04215310u;
constexpr unsigned VSLOTS = 0x32153042u;
DI int slot_addr(unsigned s) { return s * SLOT + (s >= 2 ? 8192 : 0); }
DI int own_addr(int pr) { return pr < 20 ? POFF + (pr / 5) * 12288 + (pr % 5) * 2048 : 155648 + (pr - 20) * 2048; }
DI int kslot(int t) { return slot_addr((KSLOTS >> (4 * t)) & 15u); }
DI int vslot(int t) { return slot_addr((VSLOTS >> (4 * t)) & 15u); }

extern "C" __global__ __launch_bounds__(512, 2) void k1_attn(const bf16* __restrict__ x, const bf16* __restrict__ kc, const bf16* __restrict__ vc,
                                                  const bf16* __restrict__ wq, const bf16* __restrict__ wo, char* __restrict__ ws, bf16* __restrict__ out) {
    extern __shared__ __attribute__((aligned(16))) char lds[];
    unsigned long long pc0; asm volatile("s_getpc_b64 %0" : "=s"(pc0));
    const int tid = threadIdx.x, lane = tid & 63, r32 = lane & 31, hh = lane >> 5;
    const int wid = __builtin_amdgcn_readfirstlane(tid >> 6), w4 = wid & 3;
    const int xcd = blockIdx.x & 7, jj = blockIdx.x >> 3;
    const int bl = jj >> 3, hl = jj & 7;
    const int b = (xcd >> 2) * 4 + bl, h = (xcd & 3) * 8 + hl;
    const unsigned lds0 = (unsigned)(uintptr_t)lds;
    const lds_cptr L = (lds_cptr)lds;
    const lds_ptr LW = (lds_ptr)lds;
    const bf16* Kg = kc + (size_t)(b * NH + h) * SK * DH;
    const bf16* Vg = vc + (size_t)(b * NH + h) * SK * DH;
    const int qh = w4 & 1, kh = w4 >> 1;
    const int q4 = (lane & 15) >> 2, p4 = lane & 3, blk = (lane >> 4) & 1;
    LAS unsigned* flagw = (LAS unsigned*)(LW + MOFF);
    LAS unsigned* basew = (LAS unsigned*)(LW + MOFF + 128);
    const int hg = xcd & 3;
    gu32* const F = (gu32*)(ws + WS_SYNC);
    gu32* const E = (gu32*)(ws + WS_SYNC + 65536);
#define PEER(l_) ((l_) < 8 ? xcd + 8 * (bl * 8 + (l_)) : ((xcd & 4) | ((l_) & 3)) + 8 * jj)
#define PFLAG(l_) (F + (((l_) < 8 ? 0 : 256) + PEER(l_)) * 32)
    LAS float* prop = (LAS float*)(LW + MOFF + 256);
    LAS float* lsum = (LAS float*)(LW + MOFF + 2048);

    const int hdir = (bl & 1) ? -1 : 1;
    const srd_t srdWo = __builtin_amdgcn_make_buffer_rsrc((void*)(wo + hl * 80), (short)0, HD * DM * 2 - hl * 160, 0x00020000);
    auto wo_soff = [&](int g) __attribute__((always_inline)) -> unsigned {
        const int st = (int)((STEP_OF_PAIR >> (4 * (g >> 1))) & 15u);
        return (unsigned)(((hg * 8 + ((hl + hdir * (st >> 1)) & 7)) * 128 + (2 * (g & 1) + (st & 1)) * 32) * DM * 2);
    };
    auto wo_groups = [&](int g0, int g1, int gs) __attribute__((always_inline)) {
        int l_; asm volatile("v_mbcnt_lo_u32_b32 %0, -1, 0\n\tv_mbcnt_hi_u32_b32 %0, -1, %0" : "=v"(l_));
        unsigned wv[5];
#pragma unroll
        for (int p = 0; p < 5; ++p) { const int o_ = p * 1024 + l_ * 16, row = o_ / 160, ch = (o_ % 160) >> 4; wv[p] = row * (DM * 2) + ch * 16; }
#pragma unroll 1
        for (int g = g0; g < g1; g += gs) {
            const unsigned so = wo_soff(g);
#pragma unroll
            for (int p = 0; p < 5; ++p) dma16<false>(srdWo, wv[p], so, lds0 + g * 5120 + p * 1024);
        }
    };
    bool slow = false;
    unsigned base = 0;

    if (wid < 4) {
        if (wid == 0) {
            if (lane < 12) {
                const unsigned base = __hip_atomic_load(PFLAG(lane), RLX_AGENT);
                basew[lane] = base;
                __hip_atomic_store(E + blockIdx.x * 32 + lane, base ^ ECHO_KEY, RLX_AGENT);
            }
        }
        if (wid == 1) {
            const srd_t srdC = __builtin_amdgcn_make_buffer_rsrc((void*)pc0, (short)0, CODE_TOUCH, 0x00020000);
#pragma unroll 1
            for (unsigned k = jj & 1; k * 8192u < CODE_TOUCH; k += 2) touch4(srdC, lane * 128, k * 8192u, lds0 + MOFF + 4096u);
        }
        bf16x8 qf[3][8];
        {
            f32x16 acc[3][2];
#pragma unroll
            for (int i = 0; i < 3; ++i) { acc[i][0] = f32x16{}; acc[i][1] = f32x16{}; }
            const int xsw = (r32 >> 1) & 7;
            int xa[4];
#pragma unroll
            for (int ks = 0; ks < 4; ++ks) xa[ks] = (96 * qh + r32) * 128 + (((2 * ks + hh) ^ xsw) << 4);
            const int ps = ((p4 & 1) << 1) | (p4 >> 1);
            const int wa0 = RXB + 2048 * hh + 1024 * kh + 64 * q4 + (((2 * blk + (ps >> 1)) ^ (2 * hh)) << 4) + 8 * (ps & 1);
            const int wa1 = RXB + 2048 * hh + 1024 * kh + 64 * q4 + 256 + (((2 * blk + (ps >> 1)) ^ (2 * hh + 1)) << 4) + 8 * (ps & 1);
            bf16x8 xf[2][3]; s16x4 wl[2][2], wh[2][2];
#define RDX(buf, base, ks, i) xf[buf][i] = ldsr128(base + xa[ks] + i * 4096)
#define RDW(buf, base, ks, db) do { wl[buf][db] = ldstr(base + wa0 + ks * 4096 + db * 512); wh[buf][db] = ldstr(base + wa1 + ks * 4096 + db * 512); } while (0)
#define MM(buf, i, db) acc[i][db] = MFMA(cat8(wl[buf][db], wh[buf][db]), xf[buf][i], acc[i][db])
#define HALF(cb, base, ks) do { \
                MM(cb, 0, 0); RDW(cb ^ 1, base, ks, 0); SBAR(); \
                MM(cb, 0, 1); RDX(cb ^ 1, base, ks, 0); SBAR(); \
                MM(cb, 1, 0); RDW(cb ^ 1, base, ks, 1); SBAR(); \
                MM(cb, 1, 1); RDX(cb ^ 1, base, ks, 1); SBAR(); \
                MM(cb, 2, 0); RDX(cb ^ 1, base, ks, 2); SBAR(); \
                MM(cb, 2, 1); SBAR(); } while (0)
            BAR_L();
            { const lds_cptr base = L + RING0;
              RDW(0, base, 0, 0); RDX(0, base, 0, 0); RDW(0, base, 0, 1); RDX(0, base, 0, 1); RDX(0, base, 0, 2); SBAR();
              HALF(0, base, 1); HALF(1, base, 2); HALF(0, base, 3); }
            int rb = RSTG;
#pragma unroll 1
            for (int s = 1; s < 10; ++s) {
                SBAR(); BAR_L(); SBAR();
                const lds_cptr base = L + RING0 + rb;
                HALF(1, base, 0); HALF(0, base, 1); HALF(1, base, 2); HALF(0, base, 3);
                rb = (rb == 2 * RSTG) ? 0 : rb + RSTG;
            }
            BAR_L();
            MM(1, 0, 0); MM(1, 0, 1); MM(1, 1, 0); MM(1, 1, 1); MM(1, 2, 0); MM(1, 2, 1);
#pragma unroll
            for (int i = 0; i < 3; ++i)
#pragma unroll
                for (int db = 0; db < 2; ++db)
#pragma unroll
                    for (int s = 0; s < 2; ++s) {
                        u32x4 w;
#pragma unroll
                        for (int e = 0; e < 4; ++e) w[e] = cvtpk(acc[i][db][8 * s + 2 * e] * QSCALE, acc[i][db][8 * s + 2 * e + 1] * QSCALE);
                        *(LAS u32x4*)(LW + POFF + (w4 ^ 2) * 12288 + (i * 4 + db * 2 + s) * 1024 + lane * 16) = w;
                        qf[i][db * 2 + s] = __builtin_bit_cast(bf16x8, w);
                    }
            if (wid == 1) WAITV(0);
            BAR_L();
#pragma unroll
            for (int i = 0; i < 3; ++i)
#pragma unroll
                for (int j = 0; j < 4; ++j) qf[i][4 + j] = ldsr128(L + POFF + w4 * 12288 + (i * 4 + j) * 1024 + lane * 16);
        }
        const int krow = 32 * kh + r32;
        const int kb0 = 2048 * (krow >> 3) + 64 * (krow & 7) + ((hh ^ ((krow >> 2) & 3)) << 4);
        const int kb1 = 2048 * (krow >> 3) + 64 * (krow & 7) + (((2 + hh) ^ ((krow >> 2) & 3)) << 4);
        const int kown = kh * 1024, koth = (1 - kh) * 1024;
        bf16x8 kf[8];
        unsigned ka[4];
#define KRD(j) asm volatile("ds_read_b128 %0, %1 offset:%2" : "+v"(kf[j]) : "v"(ka[((j) >> 2) * 2 + ((j) & 1)]), "i"(512 * (((j) >> 1) & 1)) : "memory")
#define KADDR(t_) do { const unsigned kb_ = lds0 + kslot(t_); ka[0] = kb_ + kown + kb0; ka[1] = kb_ + kown + kb1; ka[2] = kb_ + koth + kb0; ka[3] = kb_ + koth + kb1; } while (0)
#define KRDALL() do { KRD(0); KRD(1); KRD(2); KRD(3); KRD(4); KRD(5); KRD(6); KRD(7); } while (0)
        f32x16 negm[3], sc;
        float l[3], m[3], lold[3];
#pragma unroll
        for (int j = 0; j < 8; ++j) kf[j] = bf16x8{};
        KADDR(0); KRDALL();
        asm volatile("s_waitcnt lgkmcnt(0)" ::: "memory"); SBAR();
        {
            const lds_ptr pw = LW + POFF + w4 * 12288 + lane * 16;
            f32x16 s0[3]; float d[3];
#pragma unroll
            for (int i = 0; i < 3; ++i) {
                s0[i] = f32x16{};
#pragma unroll
                for (int j = 0; j < 8; ++j) s0[i] = MFMA(kf[j], qf[i][j], s0[i]);
            }
            SBAR();
            KADDR(1); KRDALL();
#pragma unroll
            for (int i = 0; i < 3; ++i) {
                float pm = s0[i][0];
#pragma unroll
                for (int r = 1; r < 16; ++r) pm = fmaxf(pm, s0[i][r]);
                d[i] = xhalf_max(pm);
                prop[(w4 * 3 + i) * 32 + r32] = d[i];
            }
            BAR_L();
#pragma unroll
            for (int i = 0; i < 3; ++i) {
                m[i] = fmaxf(d[i], prop[((w4 ^ 2) * 3 + i) * 32 + r32]);
                float psum = 0.f;
#pragma unroll
                for (int r = 0; r < 16; ++r) { negm[i][r] = -m[i]; s0[i][r] -= m[i]; }
                if (i < 2) {
#pragma unroll
                    for (int r = 0; r < 16; ++r) { s0[i][r] = __builtin_amdgcn_exp2f(s0[i][r]); psum += s0[i][r]; }
#pragma unroll
                    for (int s = 0; s < 2; ++s) { u32x4 w;
#pragma unroll
                        for (int e = 0; e < 4; ++e) w[e] = cvtpk(s0[i][8 * s + 2 * e], s0[i][8 * s + 2 * e + 1]);
                        *(LAS u32x4*)(pw + (i * 2 + s) * 1024) = w; }
                }
                l[i] = psum; lold[i] = psum;
            }
            sc = s0[2];
            BAR_L(); SBAR();
        }
        __builtin_amdgcn_s_setprio(1);
#define EX2(X, g) do { X[2 * (g)] = __builtin_amdgcn_exp2f(X[2 * (g)]); X[2 * (g) + 1] = __builtin_amdgcn_exp2f(X[2 * (g) + 1]); } while (0)
#define AP(X, g) do { if ((g) == 0) pa = X[0] + X[1]; else { pa += X[2 * (g)]; pa += X[2 * (g) + 1]; } w[(g) & 3] = cvtpk(X[2 * (g)], X[2 * (g) + 1]); PIN(pa); PIN(w); } while (0)
#define PST(PW, i, s) *(LAS u32x4*)(PW + ((i) * 2 + (s)) * 1024) = w
#define SMS(X, PW, i, g) do { EX2(X, g); if ((g) > 0) AP(X, (g) - 1); if ((g) == 4) PST(PW, i, 0); PIN(X); } while (0)
#define SMFIN(X, PW, i) do { AP(X, 7); PST(PW, i, 1); bad |= !(pa <= PSLIM); lold[i] = l[i]; l[i] += pa; PIN(l[i]); } while (0)
#define QK(S, i, j) S = MFMA(kf[j], qf[i][j], (j) ? S : negm[i])
        auto echo_check = [&]() __attribute__((always_inline)) {
            if (wid == 0) {
                base = basew[lane < 12 ? lane : 0];
                const unsigned want = (lane < 8 ? basew[hl] : basew[8 + hg]) ^ ECHO_KEY;
                gu32* const pecho = E + PEER(lane) * 32 + (lane < 8 ? hl : 8 + hg);
#pragma unroll 1
                for (unsigned sp = 0; sp < SPIN_CAP; ++sp) {
                    const unsigned echo = __hip_atomic_load(pecho, RLX_AGENT);
                    if (!__any(lane < 12 && echo != want)) break;
                    __builtin_amdgcn_s_sleep(2);
                }
            }
        };
        u32x4 w = {};
        int tf = 9;
#pragma unroll 1
        for (int t = 1; t < 8; ++t) {
            const lds_ptr pw = LW + POFF + w4 * 12288 + (t & 1) * 6144 + lane * 16;
            const lds_ptr pc = LW + POFF + w4 * 12288 + ((t & 1) ^ 1) * 6144 + lane * 16;
            f32x16 sa, sb; float pa; bool bad = false;
            const unsigned flv = flagw[t - 1];
            SBAR();
            QK(sa, 0, 0); SMS(sc, pc, 2, 0); SBAR(); QK(sa, 0, 1); SMS(sc, pc, 2, 1); SBAR(); QK(sa, 0, 2); SMS(sc, pc, 2, 2); SBAR();
            if (__builtin_expect(__builtin_amdgcn_readfirstlane(flv) != 0u, 0)) { tf = t - 1; break; }
            QK(sa, 0, 3); SMS(sc, pc, 2, 3); SBAR(); QK(sa, 0, 4); SMS(sc, pc, 2, 4); SBAR(); QK(sa, 0, 5); SMS(sc, pc, 2, 5); SBAR();
            QK(sa, 0, 6); SMS(sc, pc, 2, 6); SBAR(); QK(sa, 0, 7); SMS(sc, pc, 2, 7); SBAR();
            QK(sb, 1, 0); SMFIN(sc, pc, 2); KADDR(t < 7 ? t + 1 : 7); SBAR();
            QK(sb, 1, 1); SBAR(); SMS(sa, pw, 0, 0); SBAR(); QK(sb, 1, 2); SMS(sa, pw, 0, 1); SBAR(); QK(sb, 1, 3); SMS(sa, pw, 0, 2); SBAR();
            QK(sb, 1, 4); SMS(sa, pw, 0, 3); SBAR(); QK(sb, 1, 5); SMS(sa, pw, 0, 4); SBAR(); QK(sb, 1, 6); SMS(sa, pw, 0, 5); SBAR(); QK(sb, 1, 7); SMS(sa, pw, 0, 6); SBAR();
            QK(sc, 2, 0); SBAR(); KRD(0); SMS(sa, pw, 0, 7); SBAR(); SMFIN(sa, pw, 0); SBAR();
            QK(sc, 2, 1); SBAR(); KRD(1); SMS(sb, pw, 1, 0); SBAR(); SMS(sb, pw, 1, 1); SBAR();
            QK(sc, 2, 2); SBAR(); KRD(2); SMS(sb, pw, 1, 2); SBAR(); QK(sc, 2, 3); SBAR(); KRD(3); SMS(sb, pw, 1, 3); SBAR();
            QK(sc, 2, 4); SBAR(); KRD(4); SMS(sb, pw, 1, 4); SBAR(); QK(sc, 2, 5); SBAR(); KRD(5); SMS(sb, pw, 1, 5); SBAR(); SMS(sb, pw, 1, 6); SBAR();
            QK(sc, 2, 6); SBAR(); KRD(6); SMS(sb, pw, 1, 7); SBAR(); QK(sc, 2, 7); SBAR(); KRD(7); SMFIN(sb, pw, 1); SBAR();
            if (__builtin_expect(__any(bad), 0)) { if (lane == 0) flagw[t] = 1u; }
            if (t == 7) { lsum[(w4 * 3 + 0) * 32 + r32] = xhalf_sum(l[0]); lsum[(w4 * 3 + 1) * 32 + r32] = xhalf_sum(l[1]); }
            BAR_L(); SBAR();
        }
        if (__builtin_expect(tf == 9, 1)) {
            if (__builtin_expect(__builtin_amdgcn_readfirstlane(flagw[7]) != 0u, 0)) tf = 7;
            else {
                const lds_ptr pc = LW + POFF + w4 * 12288 + 6144 + lane * 16;
                float pa; bool bad = false;
                lold[0] = l[0]; lold[1] = l[1];
                SMS(sc, pc, 2, 0); SMS(sc, pc, 2, 1); SMS(sc, pc, 2, 2); SMS(sc, pc, 2, 3); SMS(sc, pc, 2, 4); SMS(sc, pc, 2, 5); SMS(sc, pc, 2, 6); SMS(sc, pc, 2, 7); SMFIN(sc, pc, 2);
                if (__builtin_expect(__any(bad), 0)) { if (lane == 0) flagw[8] = 1u; }
                lsum[(w4 * 3 + 2) * 32 + r32] = xhalf_sum(l[2]);
                echo_check();
                BAR_L();
                if (__builtin_expect(__builtin_amdgcn_readfirstlane(flagw[8]) != 0u, 0)) tf = 8;
            }
        }
        slow = tf < 9;
        if (__builtin_expect(slow, 0)) {
#pragma unroll
            for (int i = 0; i < 3; ++i) l[i] = lold[i];
            BAR_L();
#pragma unroll 1
            for (int t = tf - 1; t < 8; ++t) {
                const bool all = t >= tf;
                const lds_ptr pw = LW + POFF + w4 * 12288 + (t & 1) * 6144 + lane * 16;
#pragma unroll
                for (int j = 0; j < 8; ++j) { const int s = (j < 4) ? 4 * kh + j : 4 * (1 - kh) + (j - 4);
                    kf[j] = *(const bf16x8*)(Kg + (size_t)(t * 64 + krow) * DH + s * 16 + hh * 8); }
                f32x16 s0[3]; float d[3];
#pragma unroll
                for (int i = 0; i < 3; ++i) {
                    s0[i] = f32x16{};
#pragma unroll
                    for (int j = 0; j < 8; ++j) s0[i] = MFMA(kf[j], qf[i][j], s0[i]);
                    float pm = s0[i][0];
#pragma unroll
                    for (int r = 1; r < 16; ++r) pm = fmaxf(pm, s0[i][r]);
                    d[i] = (all || i == 2) ? fmaxf(xhalf_max(pm) - m[i], 0.f) : 0.f;
                    prop[(w4 * 3 + i) * 32 + r32] = d[i];
                }
                BAR_L();
#pragma unroll
                for (int i = 0; i < 3; ++i) {
                    const float dl = fmaxf(d[i], prop[((w4 ^ 2) * 3 + i) * 32 + r32]);
                    m[i] += dl;
                    float psum = l[i] * __builtin_amdgcn_exp2f(-dl);
#pragma unroll
                    for (int r = 0; r < 16; ++r) { s0[i][r] = (all || i == 2) ? __builtin_amdgcn_exp2f(s0[i][r] - m[i]) : 0.f; psum += s0[i][r]; }
                    l[i] = psum;
#pragma unroll
                    for (int s = 0; s < 2; ++s) { u32x4 w2;
#pragma unroll
                        for (int e = 0; e < 4; ++e) w2[e] = cvtpk(s0[i][8 * s + 2 * e], s0[i][8 * s + 2 * e + 1]);
                        *(LAS u32x4*)(pw + (i * 2 + s) * 1024) = w2; }
                    lsum[(w4 * 3 + i) * 32 + r32] = xhalf_sum(psum);
                }
                BAR_L();
            }
            BAR_L();
        }
        if (__builtin_expect(slow, 0)) echo_check();
        __builtin_amdgcn_s_setprio(0);
        BAR_L();
    } else {
        const srd_t srdK = __builtin_amdgcn_make_buffer_rsrc((void*)Kg, (short)0, SK * DH * 2, 0x00020000);
        const srd_t srdV = __builtin_amdgcn_make_buffer_rsrc((void*)Vg, (short)0, SK * DH * 2, 0x00020000);
        const int arow = 8 * (w4 >> 1) + ((lane >> 2) & 7), ach = 4 * (2 * (w4 & 1) + hh) + (p4 ^ ((2 * (w4 >> 1) + blk) & 3));
        const unsigned kvoff = arow * 256 + ach * 16;
        auto kv_unit = [&](bool isV, int t) {
#pragma unroll
            for (int i = 0; i < 4; ++i)
                dma16<true>(isV ? srdV : srdK, kvoff, (unsigned)(t * 16384 + i * 4096), lds0 + (isV ? vslot(t) : kslot(t)) + (4 * i + w4) * 1024);
        };
        u32x4 pre[8];
        {
            const int rot = (((hl + 2 * bl) & 7) * 9) / 8;
            const srd_t srdX = __builtin_amdgcn_make_buffer_rsrc((void*)(x + (size_t)b * SQ * DM), (short)0, SQ * DM * 2, 0x00020000);
            const srd_t srdW = __builtin_amdgcn_make_buffer_rsrc((void*)(wq + h * DH), (short)0, DM * HD * 2 - h * DH * 2, 0x00020000);
            const unsigned xvoff = (8 * w4 + (lane >> 3)) * (DM * 2) + (((lane & 7) ^ ((4 * (w4 & 1) + (lane >> 4)) & 7)) << 4);
            const unsigned wvoff = arow * (HD * 2) + ach * 16;
            auto issue = [&](int s, int buf) __attribute__((always_inline)) {
                int cs = s + rot; if (cs >= 10) cs -= 9; if (s == 0) cs = 0;
                const unsigned dst = lds0 + RING0 + buf;
#pragma unroll
                for (int i = 0; i < 4; ++i) dma16<false>(srdW, wvoff, (unsigned)((cs * 64 + i * 16) * HD * 2), dst + RXB + (4 * i + w4) * 1024);
#pragma unroll
                for (int i = 0; i < 6; ++i) dma16<false>(srdX, xvoff, (unsigned)(i * 32 * DM * 2 + cs * 128), dst + (4 * i + w4) * 1024);
            };
            if (wid == 4 && lane < 16) flagw[lane] = 0u;
            issue(0, 0); issue(1, RSTG);
            int rb = 2 * RSTG;
#pragma unroll 1
            for (int s = 0; s < 8; ++s) { WAITV(10); BAR_L(); issue(s + 2, rb); rb = (rb == 2 * RSTG) ? 0 : rb + RSTG; }
#define PRELD(n0, n1) _Pragma("unroll") for (int n = n0; n < n1; ++n) ld16_async<true>(pre[n], n < 4 ? srdV : srdK, kvoff, (unsigned)((n < 4 ? 0 : 2) * 16384 + (n & 3) * 4096))
            kv_unit(false, 0);
            WAITV(14); BAR_L(); kv_unit(false, 1); PRELD(0, 4);
            WAITV(12); BAR_L(); PRELD(4, 8);
            BAR_L();
            kv_unit(true, 1);
            WAITV(12);
            BAR_L();
            kv_unit(false, 3);
        }
        f32x16 o[3][2];
#pragma unroll
        for (int i = 0; i < 3; ++i) { o[i][0] = f32x16{}; o[i][1] = f32x16{}; }
        const int vb0 = 1024 * kh + 64 * (4 * hh + q4) + (((2 * blk + (p4 >> 1)) ^ hh) << 4) + 8 * (p4 & 1);
        const int vb1 = 1024 * kh + 2048 + 64 * (4 * hh + q4) + (((2 * blk + (p4 >> 1)) ^ (2 + hh)) << 4) + 8 * (p4 & 1);
        const lds_cptr pb0 = L + POFF + qh * 12288 + lane * 16;
        s16x4 vl[2][4][2], vh[2][4][2];
        bf16x8 pf[2][3];
        unsigned wv[5];
#pragma unroll
        for (int p = 0; p < 5; ++p) { const int o_ = p * 1024 + lane * 16, row = o_ / 160, ch = (o_ % 160) >> 4; wv[p] = row * (DM * 2) + ch * 16; }
        const srd_t srdO = __builtin_amdgcn_make_buffer_rsrc((void*)(ws + WS_O + (size_t)(b * NH + hg * 8 + hl) * 49152), (short)0, 49152, 0x00020000);
        u32x4 wr[10];
        const int wg0 = 10 + w4, wg1 = w4 < 2 ? 14 + w4 : 4 + w4;
        u32x4 ow[3][4];
        auto opack = [&](int i) __attribute__((always_inline)) {
            const float rl = 1.0f / (lsum[(qh * 3 + i) * 32 + r32] + lsum[((qh + 2) * 3 + i) * 32 + r32]);
#pragma unroll
            for (int db = 0; db < 2; ++db)
#pragma unroll
                for (int s = 0; s < 2; ++s) {
#pragma unroll
                    for (int e = 0; e < 4; ++e) ow[i][db * 2 + s][e] = cvtpk(o[i][db][8 * s + 2 * e] * rl, o[i][db][8 * s + 2 * e + 1] * rl);
                    st16_wt(ow[i][db * 2 + s], srdO, (r32 >> 4) * 4096 + hh * 256 + (r32 & 15) * 16, (3 * qh + i) * 8192 + (kh * 2 + db) * 1024 + s * 512); }
        };
        auto olds = [&](int i) __attribute__((always_inline)) {
#pragma unroll
            for (int db = 0; db < 2; ++db) { const lds_ptr p_ = LW + own_addr((3 * qh + i) * 4 + kh * 2 + db) + lane * 16;
                *(LAS u32x4*)p_ = ow[i][db * 2]; *(LAS u32x4*)(p_ + 1024) = ow[i][db * 2 + 1]; }
        };
#define RDV(X, vb, ks, db) do { vl[X][ks][db] = ldstr(vb + vb0 + (ks) * 4096 + (db) * 512); vh[X][ks][db] = ldstr(vb + vb1 + (ks) * 4096 + (db) * 512); } while (0)
#define RDP(buf, blk_, ks, i) pf[buf][i] = ldsr128(pb0 + ((blk_) & 1) * 6144 + ((ks) >> 1) * 24576 + ((i) * 2 + ((ks) & 1)) * 1024)
#define PVM(X, buf, ks, i, db) o[i][db] = MFMA(cat8(vl[X][ks][db], vh[X][ks][db]), pf[buf][i], o[i][db])
        auto period = [&](auto tag) __attribute__((always_inline)) -> bool {
            constexpr int P = decltype(tag)::value, X = P & 1;
            constexpr bool AB = P <= 8, C = P >= 2;
            const lds_cptr vb = L + vslot(AB ? P - 1 : 0);
            auto dm = [&](int n) __attribute__((always_inline)) {
                if constexpr (P <= 5) {
                    if (n < 4) dma16<true>(srdV, kvoff, (unsigned)((P + 1) * 16384 + n * 4096), lds0 + vslot(P + 1) + (4 * n + w4) * 1024);
                    else if constexpr (P < 5) dma16<true>(srdK, kvoff, (unsigned)((P + 3) * 16384 + (n - 4) * 4096), lds0 + kslot(P + 3) + (4 * (n - 4) + w4) * 1024);
                    else dma16<true>(srdV, kvoff, (unsigned)(7 * 16384 + (n - 4) * 4096), lds0 + vslot(7) + (4 * (n - 4) + w4) * 1024);
                } else if constexpr (P <= 8) {
                    const int g = (P == 6) ? 16 + w4 : (P == 7) ? w4 : (w4 < 2 ? 4 + w4 : 6 + w4);
                    if (n < 5) dma16<false>(srdWo, wv[n], wo_soff(g), lds0 + g * 5120 + n * 1024);
                }
            };
            auto rd = [&](int buf, int ks, int part) __attribute__((always_inline)) {
                if (part == 0) { if (C) RDP(buf, P - 2, ks, 2); }
                if (part == 1) { if (AB) RDV(X, vb, ks, 0); }
                if (part == 2) { if (AB) RDP(buf, P - 1, ks, 0); }
                if (part == 3) { if (AB) RDV(X, vb, ks, 1); }
                if (part == 4) { if (AB) RDP(buf, P - 1, ks, 1); }
            };
            auto wrl = [&](int n) __attribute__((always_inline)) {
                if constexpr (P == 6 || P == 7) { if (n < 5) ld16_async<false>(wr[(P - 6) * 5 + n], srdWo, wv[n], wo_soff(P == 6 ? wg0 : wg1)); }
            };
            const unsigned flv = flagw[P - 1];
            rd(0, 0, 0); rd(0, 0, 1); rd(0, 0, 2); rd(0, 0, 3); rd(0, 0, 4); SBAR();
            if (__builtin_expect(__builtin_amdgcn_readfirstlane(flv) != 0u, 0)) {
                if constexpr (P == 7 || P == 8) asm volatile("s_waitcnt vmcnt(0)" :: "v"(wr[0]), "v"(wr[1]), "v"(wr[2]), "v"(wr[3]), "v"(wr[4]) : "memory");
                if constexpr (P == 8) asm volatile("" :: "v"(wr[5]), "v"(wr[6]), "v"(wr[7]), "v"(wr[8]), "v"(wr[9]) : "memory");
                return true;
            }
            if constexpr (P == 9) { olds(0); olds(1); }
#pragma unroll
            for (int ks = 0; ks < 4; ++ks) {
                const int cb = ks & 1; const bool more = ks < 3;
                if (C) PVM(X ^ 1, cb, ks, 2, 0); if (more) rd(cb ^ 1, ks + 1, 0); SBAR();
                if (C) PVM(X ^ 1, cb, ks, 2, 1); if (more) rd(cb ^ 1, ks + 1, 1); wrl(2 * ks); SBAR();
                if (AB) PVM(X, cb, ks, 0, 0); if (more) rd(cb ^ 1, ks + 1, 2); SBAR();
                if (AB) PVM(X, cb, ks, 0, 1); if (more) rd(cb ^ 1, ks + 1, 3); dm(2 * ks); SBAR();
                if (AB) PVM(X, cb, ks, 1, 0); if (more) rd(cb ^ 1, ks + 1, 4); wrl(2 * ks + 1); SBAR();
                if (AB) PVM(X, cb, ks, 1, 1); dm(2 * ks + 1); SBAR();
            }
            if constexpr (P <= 5) WAITV(8); else if constexpr (P == 6) WAITV(14); else if constexpr (P == 7) WAITV(20);
            else if constexpr (P == 8) asm volatile("s_waitcnt vmcnt(5)" : "+v"(wr[0]), "+v"(wr[1]), "+v"(wr[2]), "+v"(wr[3]), "+v"(wr[4]), "+v"(wr[5]), "+v"(wr[6]), "+v"(wr[7]), "+v"(wr[8]), "+v"(wr[9]) :: "memory");
            if constexpr (P == 8) { opack(0); opack(1); }
            if constexpr (P <= 8) BAR_L();
            return false;
        };
        BAR_L();
        asm volatile("s_waitcnt vmcnt(8)" : "+v"(pre[0]), "+v"(pre[1]), "+v"(pre[2]), "+v"(pre[3]), "+v"(pre[4]), "+v"(pre[5]), "+v"(pre[6]), "+v"(pre[7]) :: "memory");
#pragma unroll
        for (int n = 0; n < 8; ++n) *(LAS u32x4*)(LW + slot_addr(2 + (n >> 2)) + (4 * (n & 3) + w4) * 1024 + lane * 16) = pre[n];
        BAR_L();
        int tf = 9;
#define PERIOD(P) if (__builtin_expect(tf == 9, 1)) { if (period(std::integral_constant<int, P>{})) tf = P - 1; }
        PERIOD(1) PERIOD(2) PERIOD(3) PERIOD(4) PERIOD(5) PERIOD(6) PERIOD(7) PERIOD(8) PERIOD(9)
        slow = tf < 9;
        if (__builtin_expect(slow, 0)) {
            WAITV(0); BAR_L();
#pragma unroll 1
            for (int t = tf - 1; t < 8; ++t) {
                const int sl = slot_addr(2 + (t & 1));
#pragma unroll
                for (int n = 0; n < 4; ++n) dma16<true>(srdV, kvoff, (unsigned)(t * 16384 + n * 4096), lds0 + sl + (4 * n + w4) * 1024);
                BAR_L();
#pragma unroll
                for (int i = 0; i < 3; ++i) {
                    const float dl = fmaxf(prop[(qh * 3 + i) * 32 + r32], prop[((qh + 2) * 3 + i) * 32 + r32]);
                    const float f = __builtin_amdgcn_exp2f(-dl);
#pragma unroll
                    for (int r = 0; r < 16; ++r) { o[i][0][r] *= f; o[i][1][r] *= f; }
                }
                WAITV(0);
                BAR_L();
                const lds_cptr vb = L + sl;
#pragma unroll
                for (int ks = 0; ks < 4; ++ks) {
                    RDV(0, vb, ks, 0); RDV(0, vb, ks, 1); RDP(0, t, ks, 0); RDP(0, t, ks, 1); RDP(0, t, ks, 2);
#pragma unroll
                    for (int i = 0; i < 3; ++i) { PVM(0, 0, ks, i, 0); PVM(0, 0, ks, i, 1); }
                }
            }
            BAR_L();
#pragma unroll
            for (int p = 0; p < 5; ++p) { wr[p] = __builtin_amdgcn_raw_buffer_load_b128(srdWo, wv[p], wo_soff(wg0), 0); wr[5 + p] = __builtin_amdgcn_raw_buffer_load_b128(srdWo, wv[p], wo_soff(wg1), 0); }
#pragma unroll
            for (int p = 0; p < 10; ++p) PIN(wr[p]);
            opack(0); opack(1); olds(0); olds(1);
        }
        opack(2); olds(2);
        BAR_L();
#pragma unroll
        for (int p = 0; p < 5; ++p) { *(LAS u32x4*)(LW + wg0 * 5120 + p * 1024 + lane * 16) = wr[p]; *(LAS u32x4*)(LW + wg1 * 5120 + p * 1024 + lane * 16) = wr[5 + p]; }
    }
    int ln; asm volatile("v_mbcnt_lo_u32_b32 %0, -1, 0\n\tv_mbcnt_hi_u32_b32 %0, -1, %0" : "=v"(ln));
    const int qg = wid & 3, kh2 = wid >> 2;
    gu32* const pflag = PFLAG(ln);
    if (__builtin_expect(slow, 0)) { if (wid < 4) { wo_groups(w4, 4, 4); wo_groups(w4 < 2 ? 4 + w4 : 6 + w4, 10, 8); wo_groups(16 + w4, 20, 4); } WAITV(0); BAR_L(); }
    {
        const int g = ln >> 4, qq = ln & 15;
        const srd_t srdOg = __builtin_amdgcn_make_buffer_rsrc((void*)(ws + WS_O + (size_t)(b * NH + hg * 8) * 49152), (short)0, 8 * 49152, 0x00020000);
        const unsigned ov0 = (g >> 1) * 1024 + (g & 1) * 512 + qq * 16, ov = ln * 16 + kh2 * 2048;
#define T16(qg_, i) (6 * ((qg_) >> 1) + ((i) < 2 ? 2 * ((qg_) & 1) + (i) : 4 + ((qg_) & 1)))
#define JISC(j) ((j) >= 8 && (j) < 20 && ((j) & 1))
#define JSTEP(j) ((j) < 8 ? (j) : (j) >= 20 ? (j) - 6 : JISC(j) ? ((j) - 5) / 2 : 4 + (j) / 2)
#define JHAS(j, i) (JISC(j) ? (i) == 2 : ((j) >= 2 && (j) < 8) ? (i) < 2 : true)
        constexpr int OW = 5;
        u32x4 of[OW][3], fo[4][3];
#define LDOG(st, i) __builtin_amdgcn_raw_buffer_load_b128(srdOg, ov, ((hl + hdir * ((st) >> 1)) & 7) * 49152 + T16(qg, i) * 4096 + ((st) & 1) * 1024, 16)
#define LDO1(j, i) do { if (JHAS(j, i)) of[(j) % OW][i] = LDOG(JSTEP(j), i); } while (0)
#pragma unroll
        for (int st = 0; st < 2; ++st)
#pragma unroll
            for (int i = 0; i < 3; ++i) { const int t16 = T16(qg, i);
                of[st][i] = *(const LAS u32x4*)(L + own_addr((t16 >> 1) * 4 + kh2 * 2 + st) + (t16 & 1) * 256 + ov0); }
        f32x4 acc[5][3];
#pragma unroll
        for (int n = 0; n < 5; ++n)
#pragma unroll
            for (int i = 0; i < 3; ++i) acc[n][i] = f32x4{};
        lds_cptr wb[3];
        wb[0] = L + kh2 * 5120 + (16 * (g >> 1) + 4 * (g & 1) + (qq >> 2)) * 160 + (ln & 3) * 8;
        wb[1] = wb[0] + 61440; wb[2] = wb[0] + 122880;
        PIN(wb[1]); PIN(wb[2]);
        s16x4 wl[2][5], wh[2][5];
#define RDWO(buf, st, n) do { const lds_cptr p_ = wb[PAIR_OF_STEP[st] / 6] + (PAIR_OF_STEP[st] % 6) * 10240 + (n) * 32; wl[buf][n] = ldstr(p_); wh[buf][n] = ldstr(p_ + 1280); } while (0)
#pragma unroll
        for (int n = 0; n < 5; ++n) RDWO(0, 0, n);
        if (wid >= 4) WAITV(4);
        BAR_L();
        if (wid == 0) { if (ln == hl) __hip_atomic_store(pflag, base + 1u, RLX_AGENT); }
        else if (wid < 4) wo_groups(19 + w4, 32, 3);
        SBAR();
        unsigned fc = 0;
#pragma unroll
        for (int j = 0; j < 20; ++j) {
            if (j == 1) {
                if (wid >= 4) WAITV(0);
                BAR_L();
                if (wid == 0 && ln == hl) __hip_atomic_store(pflag, base + 2u, RLX_AGENT);
                SBAR();
            }
            if (j == 2) {
                if (wid == 0) {
#pragma unroll 1
                    for (unsigned sp = 0; sp < SPIN_CAP; ++sp) {
                        const unsigned v = __hip_atomic_load(pflag, RLX_AGENT);
                        if (!__any(ln < 8 && v == base)) break;
                        __builtin_amdgcn_s_sleep(1);
                    }
                }
                BAR_L();
                if (kh2) __builtin_amdgcn_s_setprio(1);
#pragma unroll
                for (int j2 = 2; j2 < 2 + OW - 1; ++j2) { LDO1(j2, 0); LDO1(j2, 1); LDO1(j2, 2); }
                SBAR();
            }
            if (j == 9 - OW - 1 && wid == 0) fc = __hip_atomic_load(pflag, RLX_AGENT);
            if (j == 9 - OW) {
                if (wid == 0) {
#pragma unroll 1
                    for (unsigned sp = 0; __any(ln < 8 && fc - base < 2u) && sp < SPIN_CAP; ++sp) { __builtin_amdgcn_s_sleep(1); fc = __hip_atomic_load(pflag, RLX_AGENT); }
                }
                BAR_L();
            }
            if (j == 18) BAR_L();
#pragma unroll
            for (int n = 0; n < 5; ++n) {
#pragma unroll
                for (int i = 0; i < 3; ++i)
                    if (JHAS(j, i)) acc[n][i] = MFMA16(cat8(wl[j & 1][n], wh[j & 1][n]), __builtin_bit_cast(bf16x8, of[j % OW][i]), acc[n][i]);
                if (j < 19) RDWO((j + 1) & 1, JSTEP(j + 1), n);
                if (j >= 2 && j + OW - 1 < 20 && n < 3) LDO1(j + OW - 1, n);
                if (j >= 16 && n < 3) fo[j - 16][n] = __builtin_amdgcn_raw_buffer_load_b128(srdOg, ln * 16, ((hl + hdir * 7) & 7) * 49152 + T16(qg, n) * 4096 + (j - 16) * 1024, 16);
                SBAR();
            }
        }
        const int dst = ((xcd & 4) | qg) + 8 * jj;
        const srd_t srdP = __builtin_amdgcn_make_buffer_rsrc((void*)(ws + WS_PART + (size_t)dst * 24576 + ((hg - qg - 1) & 3) * 8192), (short)0, 8192, 0x00020000);
        auto fin = [&](auto tag) __attribute__((always_inline)) {
            constexpr int H = decltype(tag)::value, N0 = H ? 3 : 0, N1 = H ? 5 : 3;
#pragma unroll
            for (int tl = 0; tl < 15; ++tl)
                if (tl / 3 < N0 || tl / 3 >= N1) *(LAS f32x4*)(LW + ((qg * 15 + tl) * 64 + ln) * 16) = acc[tl / 3][tl % 3];
            const lds_cptr wf = wb[2] - kh2 * 5120 + 20480;
#define RDWF(buf, db, n) do { const lds_cptr p_ = wf + ((db) & 1) * 10240 + ((db) >> 1) * 5120 + (n) * 32; wl[buf][n] = ldstr(p_); wh[buf][n] = ldstr(p_ + 1280); } while (0)
#pragma unroll
            for (int n = N0; n < N1; ++n) RDWF(0, 0, n);
            f32x4 pa_[9];
#pragma unroll
            for (int db = 0; db < 4; ++db) {
                if (db == 1) { BAR_L(); }
#pragma unroll
                for (int n = N0; n < N1; ++n) {
#pragma unroll
                    for (int i = 0; i < 3; ++i) acc[n][i] = MFMA16(cat8(wl[db & 1][n], wh[db & 1][n]), __builtin_bit_cast(bf16x8, fo[db][i]), acc[n][i]);
                    if (db < 3) RDWF((db + 1) & 1, db + 1, n);
                    if (db == 2) {
#pragma unroll
                        for (int i = 0; i < 3; ++i) pa_[(n - N0) * 3 + i] = *(const LAS f32x4*)(L + ((qg * 15 + n * 3 + i) * 64 + ln) * 16);
                    }
                    SBAR();
                }
            }
#pragma unroll
            for (int u = (H ? 5 : 0); u < (H ? 8 : 5); ++u) {
                u32x4 w = {};
#pragma unroll
                for (int k = 0; k < 2; ++k) { const int tl = H ? 2 * u - 1 + k : 2 * u + k;
                    if (tl < 3 * N1) {
                        const f32x4 v = acc[tl / 3][tl % 3] + pa_[tl - 3 * N0];
                        if (qg == hg) *(LAS f32x4*)(LW + ((qg * 15 + tl) * 64 + ln) * 16) = v;
                        w[2 * k] = cvtpk(v[0], v[1]); w[2 * k + 1] = cvtpk(v[2], v[3]);
                    } }
                if (qg != hg) st16_wt(w, srdP, ln * 16, u * 1024);
            }
        };
        if (kh2) fin(std::integral_constant<int, 1>{}); else fin(std::integral_constant<int, 0>{});
        WAITV(0);
        BAR_L();
        if (wid == 0) {
            if (ln == 8 + hg) __hip_atomic_store(pflag, base + 1u, RLX_AGENT);
#pragma unroll 1
            for (unsigned sp = 0; sp < SPIN_CAP; ++sp) {
                const unsigned v = __hip_atomic_load(pflag, RLX_AGENT);
                if (!__any(ln >= 8 && ln < 12 && v == base)) break;
                __builtin_amdgcn_s_sleep(1);
            }
        }
        BAR_L();
        const srd_t srdI = __builtin_amdgcn_make_buffer_rsrc((void*)(ws + WS_PART + (size_t)blockIdx.x * 24576), (short)0, 24576, 0x00020000);
        const srd_t srdOut = __builtin_amdgcn_make_buffer_rsrc((void*)(out + (size_t)b * SQ * DM), (short)0, SQ * DM * 2, 0x00020000);
        u32x4 pv_[3];
#pragma unroll
        for (int j = 0; j < 3; ++j) pv_[j] = __builtin_amdgcn_raw_buffer_load_b128(srdI, ln * 16, j * 8192 + wid * 1024, 16);
#pragma unroll
        for (int k = 0; k < 2; ++k) {
            const int tl = wid < 5 ? 2 * wid + k : 2 * wid - 1 + k;
            if (!(wid == 4 && k == 1)) {
                f32x4 sum = *(const LAS f32x4*)(L + ((hg * 15 + tl) * 64 + ln) * 16);
#pragma unroll
                for (int j = 0; j < 3; ++j) {
                    sum[0] += __uint_as_float(pv_[j][2 * k] << 16); sum[1] += __uint_as_float(pv_[j][2 * k] & 0xffff0000u);
                    sum[2] += __uint_as_float(pv_[j][2 * k + 1] << 16); sum[3] += __uint_as_float(pv_[j][2 * k + 1] & 0xffff0000u);
                }
                const int n = tl / 3, i = tl % 3;
                const int q = T16(hg, i) * 16 + qq, col = hl * 80 + n * 16 + g * 4;
                u32x2 w; w[0] = cvtpk(sum[0], sum[1]); w[1] = cvtpk(sum[2], sum[3]);
                __builtin_amdgcn_raw_buffer_store_b64(w, srdOut, (q * DM + col) * 2, 0, 0);
            }
        }
    }
}

extern "C" void kernel_launch(void* const* d_in, const int* in_sizes, int n_in, void* d_out, int out_size, void* d_ws, size_t ws_size, hipStream_t stream) {
    const bf16* x = (const bf16*)d_in[0]; const bf16* kc = (const bf16*)d_in[1]; const bf16* vc = (const bf16*)d_in[2];
    const bf16* wq = (const bf16*)d_in[3]; const bf16* wo = (const bf16*)d_in[4];
    (void)hipFuncSetAttribute((const void*)k1_attn, hipFuncAttributeMaxDynamicSharedMemorySize, LDS_BYTES);
    k1_attn<<<256, 512, LDS_BYTES, stream>>>(x, kc, vc, wq, wo, (char*)d_ws, (bf16*)d_out);
}
```

```cpp
#include <hip/hip_runtime.h>
#include <hip/hip_bf16.h>
#include <stdint.h>
#include <type_traits>

typedef __hip_bfloat16 bf16;
using bf16x8 = __attribute__((ext_vector_type(8))) short;
using s16x4  = __attribute__((ext_vector_type(4))) short;
using f32x16 = __attribute__((ext_vector_type(16))) float;
using f32x4  = __attribute__((ext_vector_type(4))) float;
using u32x4  = __attribute__((ext_vector_type(4))) unsigned;
using u32x2  = __attribute__((ext_vector_type(2))) unsigned;
typedef short v4i16_t __attribute__((ext_vector_type(4)));
#define LAS __attribute__((address_space(3)))
typedef LAS const char* lds_cptr;
typedef LAS char* lds_ptr;
typedef __amdgpu_buffer_rsrc_t srd_t;
#define DI __device__ __forceinline__
#define SBAR() __builtin_amdgcn_sched_barrier(0)
#define PIN(x) asm volatile("" : "+v"(x))
#define MFMA(a, b, c) __builtin_amdgcn_mfma_f32_32x32x16_bf16(a, b, c, 0, 0, 0)

constexpr int NB = 8, SQ = 192, DM = 640, NH = 32, SK = 512, DH = 128, HD = NH * DH;
constexpr float QSCALE = 0.08838834764831845f * 1.4426950408889634f;
#define PSLIM 65536.0f

typedef float f32x2 __attribute__((ext_vector_type(2)));
typedef __bf16 bf16x2 __attribute__((ext_vector_type(2)));
DI unsigned cvtpk(float lo, float hi) { f32x2 v = {lo, hi}; bf16x2 b = __builtin_convertvector(v, bf16x2); return __builtin_bit_cast(unsigned, b); }
template <bool NT>
DI void dma16(srd_t srd, unsigned voff, unsigned soff, unsigned lds_dst_) {
    const unsigned lds_dst = __builtin_amdgcn_readfirstlane(lds_dst_);
    if constexpr (NT) asm volatile("s_mov_b32 m0, %3\n\ts_nop 0\n\tbuffer_load_dwordx4 %0, %1, %2 offen nt lds" :: "v"(voff), "s"(srd), "s"(soff), "s"(lds_dst) : "m0", "memory");
    else              asm volatile("s_mov_b32 m0, %3\n\ts_nop 0\n\tbuffer_load_dwordx4 %0, %1, %2 offen lds" :: "v"(voff), "s"(srd), "s"(soff), "s"(lds_dst) : "m0", "memory");
}
template <bool NT>
DI void ld16_async(u32x4& r, srd_t srd, unsigned voff, unsigned soff) {
    if constexpr (NT) asm volatile("buffer_load_dwordx4 %0, %1, %2, %3 offen nt" : "=v"(r) : "v"(voff), "s"(srd), "s"(soff) : "memory");
    else              asm volatile("buffer_load_dwordx4 %0, %1, %2, %3 offen" : "=v"(r) : "v"(voff), "s"(srd), "s"(soff) : "memory");
}
DI void touch4(srd_t srd, unsigned voff, unsigned soff, unsigned lds_dst) {
    asm volatile("s_mov_b32 m0, %3\n\ts_nop 0\n\tbuffer_load_dword %0, %1, %2 offen lds" :: "v"(voff), "s"(srd), "s"(soff), "s"(lds_dst) : "m0", "memory");
}
DI bf16x8 ldsr128(lds_cptr p) { return *(const LAS bf16x8*)p; }
DI s16x4 ldstr(lds_cptr p) { return __builtin_bit_cast(s16x4, __builtin_amdgcn_ds_read_tr16_b64_v4i16((LAS v4i16_t*)p)); }
DI bf16x8 cat8(s16x4 a, s16x4 b) { return __builtin_shufflevector(a, b, 0, 1, 2, 3, 4, 5, 6, 7); }
#define WAITV(N) asm volatile("s_waitcnt vmcnt(" #N ")" ::: "memory")
#define BAR_L() asm volatile("s_waitcnt lgkmcnt(0)\n\ts_barrier" ::: "memory")
DI float xhalf_max(float v) { auto rr = __builtin_amdgcn_permlane32_swap(__float_as_uint(v), __float_as_uint(v), false, false); return fmaxf(__uint_as_float(rr[0]), __uint_as_float(rr[1])); }
DI float xhalf_sum(float v) { auto rr = __builtin_amdgcn_permlane32_swap(__float_as_uint(v), __float_as_uint(v), false, false); return __uint_as_float(rr[0]) + __uint_as_float(rr[1]); }

#define GAS __attribute__((address_space(1)))
typedef GAS unsigned gu32;
#define RLX_AGENT __ATOMIC_RELAXED, __HIP_MEMORY_SCOPE_AGENT
#define MFMA16(a, b, c) __builtin_amdgcn_mfma_f32_16x16x32_bf16(a, b, c, 0, 0, 0)
DI void st16_wt(u32x4 v, srd_t srd, unsigned voff, unsigned soff) {
    __builtin_amdgcn_raw_buffer_store_b128(v, srd, voff, soff, 16); SBAR(); asm volatile("s_nop 1"); SBAR();
}
constexpr unsigned ECHO_KEY = 0x5A17C3E9u;
constexpr unsigned SPIN_CAP = 1u << 19;
constexpr size_t WS_O = 0, WS_PART = 16u << 20, WS_SYNC = 48u << 20;
constexpr int PAIR_OF_STEP[16] = {8, 9, 0, 1, 2, 4, 5, 6, 7, 3, 10, 11, 12, 13, 14, 15};
constexpr unsigned long long STEP_OF_PAIR = 0xFEDCBA1087659432ull;
constexpr unsigned CODE_TOUCH = 28672;
constexpr int SLOT = 16384;
constexpr int MOFF = 32768;
constexpr int RING0 = 40960, RSTG = 40960, RXB = 24576;
constexpr int POFF = 106496;
constexpr int LDS_BYTES = 163840;
constexpr unsigned KSLOTS = 0x04215310u;
constexpr unsigned VSLOTS = 0x32153042u;
DI int slot_addr(unsigned s) { return s * SLOT + (s >= 2 ? 8192 : 0); }
DI int own_addr(int pr) { return pr < 20 ? POFF + (pr / 5) * 12288 + (pr % 5) * 2048 : 155648 + (pr - 20) * 2048; }
DI int kslot(int t) { return slot_addr((KSLOTS >> (4 * t)) & 15u); }
DI int vslot(int t) { return slot_addr((VSLOTS >> (4 * t)) & 15u); }

extern "C" __global__ __launch_bounds__(512, 2) void k1_attn(const bf16* __restrict__ x, const bf16* __restrict__ kc, const bf16* __restrict__ vc,
                                                  const bf16* __restrict__ wq, const bf16* __restrict__ wo, char* __restrict__ ws, bf16* __restrict__ out) {
    extern __shared__ __attribute__((aligned(16))) char lds[];
    unsigned long long pc0; asm volatile("s_getpc_b64 %0" : "=s"(pc0));
    const int tid = threadIdx.x, lane = tid & 63, r32 = lane & 31, hh = lane >> 5;
    const int wid = __builtin_amdgcn_readfirstlane(tid >> 6), w4 = wid & 3;
    const int xcd = blockIdx.x & 7, jj = blockIdx.x >> 3;
    const int bl = jj >> 3, hl = jj & 7;
    const int b = (xcd >> 2) * 4 + bl, h = (xcd & 3) * 8 + hl;
    const unsigned lds0 = (unsigned)(uintptr_t)lds;
    const lds_cptr L = (lds_cptr)lds;
    const lds_ptr LW = (lds_ptr)lds;
    const bf16* Kg = kc + (size_t)(b * NH + h) * SK * DH;
    const bf16* Vg = vc + (size_t)(b * NH + h) * SK * DH;
    const int kvrot = (jj + xcd) & 7;
#define KB(t_) (((t_) + kvrot) & 7)
    const int qh = w4 & 1, kh = w4 >> 1;
    const int q4 = (lane & 15) >> 2, p4 = lane & 3, blk = (lane >> 4) & 1;
    LAS unsigned* flagw = (LAS unsigned*)(LW + MOFF);
    LAS unsigned* basew = (LAS unsigned*)(LW + MOFF + 128);
    const int hg = xcd & 3;
    gu32* const F = (gu32*)(ws + WS_SYNC);
    gu32* const E = (gu32*)(ws + WS_SYNC + 65536);
#define PEER(l_) ((l_) < 8 ? xcd + 8 * (bl * 8 + (l_)) : ((xcd & 4) | ((l_) & 3)) + 8 * jj)
#define PFLAG(l_) (F + (((l_) < 8 ? 0 : 256) + PEER(l_)) * 32)
    LAS float* prop = (LAS float*)(LW + MOFF + 256);
    LAS float* lsum = (LAS float*)(LW + MOFF + 2048);

    const int hdir = (bl & 1) ? -1 : 1;
    const srd_t srdWo = __builtin_amdgcn_make_buffer_rsrc((void*)(wo + hl * 80), (short)0, HD * DM * 2 - hl * 160, 0x00020000);
    auto wo_soff = [&](int g) __attribute__((always_inline)) -> unsigned {
        const int st = (int)((STEP_OF_PAIR >> (4 * (g >> 1))) & 15u);
        return (unsigned)(((hg * 8 + ((hl + hdir * (st >> 1)) & 7)) * 128 + (2 * (g & 1) + (st & 1)) * 32) * DM * 2);
    };
    auto wo_groups = [&](int g0, int g1, int gs) __attribute__((always_inline)) {
        int l_; asm volatile("v_mbcnt_lo_u32_b32 %0, -1, 0\n\tv_mbcnt_hi_u32_b32 %0, -1, %0" : "=v"(l_));
        unsigned wv[5];
#pragma unroll
        for (int p = 0; p < 5; ++p) { const int o_ = p * 1024 + l_ * 16, row = o_ / 160, ch = (o_ % 160) >> 4; wv[p] = row * (DM * 2) + ch * 16; }
#pragma unroll 1
        for (int g = g0; g < g1; g += gs) {
            const unsigned so = wo_soff(g);
#pragma unroll
            for (int p = 0; p < 5; ++p) dma16<false>(srdWo, wv[p], so, lds0 + g * 5120 + p * 1024);
        }
    };
    bool slow = false;
    unsigned base = 0;

    if (wid < 4) {
        if (wid == 0) {
            if (lane < 12) {
                const unsigned base = __hip_atomic_load(PFLAG(lane), RLX_AGENT);
                basew[lane] = base;
                __hip_atomic_store(E + blockIdx.x * 32 + lane, base ^ ECHO_KEY, RLX_AGENT);
            }
        }
        if (wid == 1) {
            const srd_t srdC = __builtin_amdgcn_make_buffer_rsrc((void*)pc0, (short)0, CODE_TOUCH, 0x00020000);
#pragma unroll 1
            for (unsigned k = jj & 1; k * 8192u < CODE_TOUCH; k += 2) touch4(srdC, lane * 128, k * 8192u, lds0 + MOFF + 4096u);
        }
        bf16x8 qf[3][8];
        {
            f32x16 acc[3][2];
#pragma unroll
            for (int i = 0; i < 3; ++i) { acc[i][0] = f32x16{}; acc[i][1] = f32x16{}; }
            const int xsw = (r32 >> 1) & 7;
            int xa[4];
#pragma unroll
            for (int ks = 0; ks < 4; ++ks) xa[ks] = (96 * qh + r32) * 128 + (((2 * ks + hh) ^ xsw) << 4);
            const int ps = ((p4 & 1) << 1) | (p4 >> 1);
            const int wa0 = RXB + 2048 * hh + 1024 * kh + 64 * q4 + (((2 * blk + (ps >> 1)) ^ (2 * hh)) << 4) + 8 * (ps & 1);
            const int wa1 = RXB + 2048 * hh + 1024 * kh + 64 * q4 + 256 + (((2 * blk + (ps >> 1)) ^ (2 * hh + 1)) << 4) + 8 * (ps & 1);
            bf16x8 xf[2][3]; s16x4 wl[2][2], wh[2][2];
#define RDX(buf, base, ks, i) xf[buf][i] = ldsr128(base + xa[ks] + i * 4096)
#define RDW(buf, base, ks, db) do { wl[buf][db] = ldstr(base + wa0 + ks * 4096 + db * 512); wh[buf][db] = ldstr(base + wa1 + ks * 4096 + db * 512); } while (0)
#define MM(buf, i, db) acc[i][db] = MFMA(cat8(wl[buf][db], wh[buf][db]), xf[buf][i], acc[i][db])
#define HALF(cb, base, ks) do { \
                MM(cb, 0, 0); RDW(cb ^ 1, base, ks, 0); SBAR(); \
                MM(cb, 0, 1); RDX(cb ^ 1, base, ks, 0); SBAR(); \
                MM(cb, 1, 0); RDW(cb ^ 1, base, ks, 1); SBAR(); \
                MM(cb, 1, 1); RDX(cb ^ 1, base, ks, 1); SBAR(); \
                MM(cb, 2, 0); RDX(cb ^ 1, base, ks, 2); SBAR(); \
                MM(cb, 2, 1); SBAR(); } while (0)
            BAR_L();
            { const lds_cptr base = L + RING0;
              RDW(0, base, 0, 0); RDX(0, base, 0, 0); RDW(0, base, 0, 1); RDX(0, base, 0, 1); RDX(0, base, 0, 2); SBAR();
              HALF(0, base, 1); HALF(1, base, 2); HALF(0, base, 3); }
            int rb = RSTG;
#pragma unroll 1
            for (int s = 1; s < 10; ++s) {
                SBAR(); BAR_L(); SBAR();
                const lds_cptr base = L + RING0 + rb;
                HALF(1, base, 0); HALF(0, base, 1); HALF(1, base, 2); HALF(0, base, 3);
                rb = (rb == 2 * RSTG) ? 0 : rb + RSTG;
            }
            BAR_L();
            MM(1, 0, 0); MM(1, 0, 1); MM(1, 1, 0); MM(1, 1, 1); MM(1, 2, 0); MM(1, 2, 1);
#pragma unroll
            for (int i = 0; i < 3; ++i)
#pragma unroll
                for (int db = 0; db < 2; ++db)
#pragma unroll
                    for (int s = 0; s < 2; ++s) {
                        u32x4 w;
#pragma unroll
                        for (int e = 0; e < 4; ++e) w[e] = cvtpk(acc[i][db][8 * s + 2 * e] * QSCALE, acc[i][db][8 * s + 2 * e + 1] * QSCALE);
                        *(LAS u32x4*)(LW + POFF + (w4 ^ 2) * 12288 + (i * 4 + db * 2 + s) * 1024 + lane * 16) = w;
                        qf[i][db * 2 + s] = __builtin_bit_cast(bf16x8, w);
                    }
            if (wid == 1) WAITV(0);
            BAR_L();
#pragma unroll
            for (int i = 0; i < 3; ++i)
#pragma unroll
                for (int j = 0; j < 4; ++j) qf[i][4 + j] = ldsr128(L + POFF + w4 * 12288 + (i * 4 + j) * 1024 + lane * 16);
        }
        const int krow = 32 * kh + r32;
        const int kb0 = 2048 * (krow >> 3) + 64 * (krow & 7) + ((hh ^ ((krow >> 2) & 3)) << 4);
        const int kb1 = 2048 * (krow >> 3) + 64 * (krow & 7) + (((2 + hh) ^ ((krow >> 2) & 3)) << 4);
        const int kown = kh * 1024, koth = (1 - kh) * 1024;
        bf16x8 kf[8];
        unsigned ka[4];
#define KRD(j) asm volatile("ds_read_b128 %0, %1 offset:%2" : "+v"(kf[j]) : "v"(ka[((j) >> 2) * 2 + ((j) & 1)]), "i"(512 * (((j) >> 1) & 1)) : "memory")
#define KADDR(t_) do { const unsigned kb_ = lds0 + kslot(t_); ka[0] = kb_ + kown + kb0; ka[1] = kb_ + kown + kb1; ka[2] = kb_ + koth + kb0; ka[3] = kb_ + koth + kb1; } while (0)
#define KRDALL() do { KRD(0); KRD(1); KRD(2); KRD(3); KRD(4); KRD(5); KRD(6); KRD(7); } while (0)
        f32x16 negm[3], sc;
        float l[3], m[3], lold[3];
#pragma unroll
        for (int j = 0; j < 8; ++j) kf[j] = bf16x8{};
        KADDR(0); KRDALL();
        asm volatile("s_waitcnt lgkmcnt(0)" ::: "memory"); SBAR();
        {
            const lds_ptr pw = LW + POFF + w4 * 12288 + lane * 16;
            f32x16 s0[3]; float d[3];
#pragma unroll
            for (int i = 0; i < 3; ++i) {
                s0[i] = f32x16{};
#pragma unroll
                for (int j = 0; j < 8; ++j) s0[i] = MFMA(kf[j], qf[i][j], s0[i]);
            }
            SBAR();
            KADDR(1); KRDALL();
#pragma unroll
            for (int i = 0; i < 3; ++i) {
                float pm = s0[i][0];
#pragma unroll
                for (int r = 1; r < 16; ++r) pm = fmaxf(pm, s0[i][r]);
                d[i] = xhalf_max(pm);
                prop[(w4 * 3 + i) * 32 + r32] = d[i];
            }
            BAR_L();
#pragma unroll
            for (int i = 0; i < 3; ++i) {
                m[i] = fmaxf(d[i], prop[((w4 ^ 2) * 3 + i) * 32 + r32]);
                float psum = 0.f;
#pragma unroll
                for (int r = 0; r < 16; ++r) { negm[i][r] = -m[i]; s0[i][r] -= m[i]; }
                if (i < 2) {
#pragma unroll
                    for (int r = 0; r < 16; ++r) { s0[i][r] = __builtin_amdgcn_exp2f(s0[i][r]); psum += s0[i][r]; }
#pragma unroll
                    for (int s = 0; s < 2; ++s) { u32x4 w;
#pragma unroll
                        for (int e = 0; e < 4; ++e) w[e] = cvtpk(s0[i][8 * s + 2 * e], s0[i][8 * s + 2 * e + 1]);
                        *(LAS u32x4*)(pw + (i * 2 + s) * 1024) = w; }
                }
                l[i] = psum; lold[i] = psum;
            }
            sc = s0[2];
            BAR_L(); SBAR();
        }
        __builtin_amdgcn_s_setprio(1);
#define EX2(X, g) do { X[2 * (g)] = __builtin_amdgcn_exp2f(X[2 * (g)]); X[2 * (g) + 1] = __builtin_amdgcn_exp2f(X[2 * (g) + 1]); } while (0)
#define AP(X, g) do { if ((g) == 0) pa = X[0] + X[1]; else { pa += X[2 * (g)]; pa += X[2 * (g) + 1]; } w[(g) & 3] = cvtpk(X[2 * (g)], X[2 * (g) + 1]); PIN(pa); PIN(w); } while (0)
#define PST(PW, i, s) *(LAS u32x4*)(PW + ((i) * 2 + (s)) * 1024) = w
#define SMS(X, PW, i, g) do { EX2(X, g); if ((g) > 0) AP(X, (g) - 1); if ((g) == 4) PST(PW, i, 0); PIN(X); } while (0)
#define SMFIN(X, PW, i) do { AP(X, 7); PST(PW, i, 1); bad |= !(pa <= PSLIM); lold[i] = l[i]; l[i] += pa; PIN(l[i]); } while (0)
#define QK(S, i, j) S = MFMA(kf[j], qf[i][j], (j) ? S : negm[i])
        auto echo_check = [&]() __attribute__((always_inline)) {
            if (wid == 0) {
                base = basew[lane < 12 ? lane : 0];
                const unsigned want = (lane < 8 ? basew[hl] : basew[8 + hg]) ^ ECHO_KEY;
                gu32* const pecho = E + PEER(lane) * 32 + (lane < 8 ? hl : 8 + hg);
#pragma unroll 1
                for (unsigned sp = 0; sp < SPIN_CAP; ++sp) {
                    const unsigned echo = __hip_atomic_load(pecho, RLX_AGENT);
                    if (!__any(lane < 12 && echo != want)) break;
                    __builtin_amdgcn_s_sleep(2);
                }
            }
        };
        u32x4 w = {};
        int tf = 9;
#pragma unroll 1
        for (int t = 1; t < 8; ++t) {
            const lds_ptr pw = LW + POFF + w4 * 12288 + (t & 1) * 6144 + lane * 16;
            const lds_ptr pc = LW + POFF + w4 * 12288 + ((t & 1) ^ 1) * 6144 + lane * 16;
            f32x16 sa, sb; float pa; bool bad = false;
            const unsigned flv = flagw[t - 1];
            SBAR();
            QK(sa, 0, 0); SMS(sc, pc, 2, 0); SBAR(); QK(sa, 0, 1); SMS(sc, pc, 2, 1); SBAR(); QK(sa, 0, 2); SMS(sc, pc, 2, 2); SBAR();
            if (__builtin_expect(__builtin_amdgcn_readfirstlane(flv) != 0u, 0)) { tf = t - 1; break; }
            QK(sa, 0, 3); SMS(sc, pc, 2, 3); SBAR(); QK(sa, 0, 4); SMS(sc, pc, 2, 4); SBAR(); QK(sa, 0, 5); SMS(sc, pc, 2, 5); SBAR();
            QK(sa, 0, 6); SMS(sc, pc, 2, 6); SBAR(); QK(sa, 0, 7); SMS(sc, pc, 2, 7); SBAR();
            QK(sb, 1, 0); SMFIN(sc, pc, 2); KADDR(t < 7 ? t + 1 : 7); SBAR();
            QK(sb, 1, 1); SBAR(); SMS(sa, pw, 0, 0); SBAR(); QK(sb, 1, 2); SMS(sa, pw, 0, 1); SBAR(); QK(sb, 1, 3); SMS(sa, pw, 0, 2); SBAR();
            QK(sb, 1, 4); SMS(sa, pw, 0, 3); SBAR(); QK(sb, 1, 5); SMS(sa, pw, 0, 4); SBAR(); QK(sb, 1, 6); SMS(sa, pw, 0, 5); SBAR(); QK(sb, 1, 7); SMS(sa, pw, 0, 6); SBAR();
            QK(sc, 2, 0); SBAR(); KRD(0); SMS(sa, pw, 0, 7); SBAR(); SMFIN(sa, pw, 0); SBAR();
            QK(sc, 2, 1); SBAR(); KRD(1); SMS(sb, pw, 1, 0); SBAR(); SMS(sb, pw, 1, 1); SBAR();
            QK(sc, 2, 2); SBAR(); KRD(2); SMS(sb, pw, 1, 2); SBAR(); QK(sc, 2, 3); SBAR(); KRD(3); SMS(sb, pw, 1, 3); SBAR();
            QK(sc, 2, 4); SBAR(); KRD(4); SMS(sb, pw, 1, 4); SBAR(); QK(sc, 2, 5); SBAR(); KRD(5); SMS(sb, pw, 1, 5); SBAR(); SMS(sb, pw, 1, 6); SBAR();
            QK(sc, 2, 6); SBAR(); KRD(6); SMS(sb, pw, 1, 7); SBAR(); QK(sc, 2, 7); SBAR(); KRD(7); SMFIN(sb, pw, 1); SBAR();
            if (__builtin_expect(__any(bad), 0)) { if (lane == 0) flagw[t] = 1u; }
            if (t == 7) { lsum[(w4 * 3 + 0) * 32 + r32] = xhalf_sum(l[0]); lsum[(w4 * 3 + 1) * 32 + r32] = xhalf_sum(l[1]); }
            BAR_L(); SBAR();
        }
        if (__builtin_expect(tf == 9, 1)) {
            if (__builtin_expect(__builtin_amdgcn_readfirstlane(flagw[7]) != 0u, 0)) tf = 7;
            else {
                const lds_ptr pc = LW + POFF + w4 * 12288 + 6144 + lane * 16;
                float pa; bool bad = false;
                lold[0] = l[0]; lold[1] = l[1];
                SMS(sc, pc, 2, 0); SMS(sc, pc, 2, 1); SMS(sc, pc, 2, 2); SMS(sc, pc, 2, 3); SMS(sc, pc, 2, 4); SMS(sc, pc, 2, 5); SMS(sc, pc, 2, 6); SMS(sc, pc, 2, 7); SMFIN(sc, pc, 2);
                if (__builtin_expect(__any(bad), 0)) { if (lane == 0) flagw[8] = 1u; }
                lsum[(w4 * 3 + 2) * 32 + r32] = xhalf_sum(l[2]);
                echo_check();
                BAR_L();
                if (__builtin_expect(__builtin_amdgcn_readfirstlane(flagw[8]) != 0u, 0)) tf = 8;
            }
        }
        slow = tf < 9;
        if (__builtin_expect(slow, 0)) {
#pragma unroll
            for (int i = 0; i < 3; ++i) l[i] = lold[i];
            BAR_L();
#pragma unroll 1
            for (int t = tf - 1; t < 8; ++t) {
                const bool all = t >= tf;
                const lds_ptr pw = LW + POFF + w4 * 12288 + (t & 1) * 6144 + lane * 16;
#pragma unroll
                for (int j = 0; j < 8; ++j) { const int s = (j < 4) ? 4 * kh + j : 4 * (1 - kh) + (j - 4);
                    kf[j] = *(const bf16x8*)(Kg + (size_t)(KB(t) * 64 + krow) * DH + s * 16 + hh * 8); }
                f32x16 s0[3]; float d[3];
#pragma unroll
                for (int i = 0; i < 3; ++i) {
                    s0[i] = f32x16{};
#pragma unroll
                    for (int j = 0; j < 8; ++j) s0[i] = MFMA(kf[j], qf[i][j], s0[i]);
                    float pm = s0[i][0];
#pragma unroll
                    for (int r = 1; r < 16; ++r) pm = fmaxf(pm, s0[i][r]);
                    d[i] = (all || i == 2) ? fmaxf(xhalf_max(pm) - m[i], 0.f) : 0.f;
                    prop[(w4 * 3 + i) * 32 + r32] = d[i];
                }
                BAR_L();
#pragma unroll
                for (int i = 0; i < 3; ++i) {
                    const float dl = fmaxf(d[i], prop[((w4 ^ 2) * 3 + i) * 32 + r32]);
                    m[i] += dl;
                    float psum = l[i] * __builtin_amdgcn_exp2f(-dl);
#pragma unroll
                    for (int r = 0; r < 16; ++r) { s0[i][r] = (all || i == 2) ? __builtin_amdgcn_exp2f(s0[i][r] - m[i]) : 0.f; psum += s0[i][r]; }
                    l[i] = psum;
#pragma unroll
                    for (int s = 0; s < 2; ++s) { u32x4 w2;
#pragma unroll
                        for (int e = 0; e < 4; ++e) w2[e] = cvtpk(s0[i][8 * s + 2 * e], s0[i][8 * s + 2 * e + 1]);
                        *(LAS u32x4*)(pw + (i * 2 + s) * 1024) = w2; }
                    lsum[(w4 * 3 + i) * 32 + r32] = xhalf_sum(psum);
                }
                BAR_L();
            }
            BAR_L();
        }
        if (__builtin_expect(slow, 0)) echo_check();
        __builtin_amdgcn_s_setprio(0);
        BAR_L();
    } else {
        const srd_t srdK = __builtin_amdgcn_make_buffer_rsrc((void*)Kg, (short)0, SK * DH * 2, 0x00020000);
        const srd_t srdV = __builtin_amdgcn_make_buffer_rsrc((void*)Vg, (short)0, SK * DH * 2, 0x00020000);
        const int arow = 8 * (w4 >> 1) + ((lane >> 2) & 7), ach = 4 * (2 * (w4 & 1) + hh) + (p4 ^ ((2 * (w4 >> 1) + blk) & 3));
        const unsigned kvoff = arow * 256 + ach * 16;
        auto kv_unit = [&](bool isV, int t) {
#pragma unroll
            for (int i = 0; i < 4; ++i)
                dma16<true>(isV ? srdV : srdK, kvoff, (unsigned)(KB(t) * 16384 + i * 4096), lds0 + (isV ? vslot(t) : kslot(t)) + (4 * i + w4) * 1024);
        };
        u32x4 pre[8];
        {
            const int rot = (((hl + 2 * bl) & 7) * 9) / 8;
            const srd_t srdX = __builtin_amdgcn_make_buffer_rsrc((void*)(x + (size_t)b * SQ * DM), (short)0, SQ * DM * 2, 0x00020000);
            const srd_t srdW = __builtin_amdgcn_make_buffer_rsrc((void*)(wq + h * DH), (short)0, DM * HD * 2 - h * DH * 2, 0x00020000);
            const unsigned xvoff = (8 * w4 + (lane >> 3)) * (DM * 2) + (((lane & 7) ^ ((4 * (w4 & 1) + (lane >> 4)) & 7)) << 4);
            const unsigned wvoff = arow * (HD * 2) + ach * 16;
            auto issue = [&](int s, int buf) __attribute__((always_inline)) {
                int cs = s + rot; if (cs >= 10) cs -= 9; if (s == 0) cs = 0;
                const unsigned dst = lds0 + RING0 + buf;
#pragma unroll
                for (int i = 0; i < 4; ++i) dma16<false>(srdW, wvoff, (unsigned)((cs * 64 + i * 16) * HD * 2), dst + RXB + (4 * i + w4) * 1024);
#pragma unroll
                for (int i = 0; i < 6; ++i) dma16<false>(srdX, xvoff, (unsigned)(i * 32 * DM * 2 + cs * 128), dst + (4 * i + w4) * 1024);
            };
            if (wid == 4 && lane < 16) flagw[lane] = 0u;
            issue(0, 0); issue(1, RSTG);
            int rb = 2 * RSTG;
#pragma unroll 1
            for (int s = 0; s < 8; ++s) { WAITV(10); BAR_L(); issue(s + 2, rb); rb = (rb == 2 * RSTG) ? 0 : rb + RSTG; }
#define PRELD(n0, n1) _Pragma("unroll") for (int n = n0; n < n1; ++n) ld16_async<true>(pre[n], n < 4 ? srdV : srdK, kvoff, (unsigned)(KB(n < 4 ? 0 : 2) * 16384 + (n & 3) * 4096))
            kv_unit(false, 0);
            WAITV(14); BAR_L(); kv_unit(false, 1); PRELD(0, 4);
            WAITV(12); BAR_L(); PRELD(4, 8);
            BAR_L();
            kv_unit(true, 1);
            WAITV(12);
            BAR_L();
            kv_unit(false, 3);
        }
        f32x16 o[3][2];
#pragma unroll
        for (int i = 0; i < 3; ++i) { o[i][0] = f32x16{}; o[i][1] = f32x16{}; }
        const int vb0 = 1024 * kh + 64 * (4 * hh + q4) + (((2 * blk + (p4 >> 1)) ^ hh) << 4) + 8 * (p4 & 1);
        const int vb1 = 1024 * kh + 2048 + 64 * (4 * hh + q4) + (((2 * blk + (p4 >> 1)) ^ (2 + hh)) << 4) + 8 * (p4 & 1);
        const lds_cptr pb0 = L + POFF + qh * 12288 + lane * 16;
        s16x4 vl[2][4][2], vh[2][4][2];
        bf16x8 pf[2][3];
        unsigned wv[5];
#pragma unroll
        for (int p = 0; p < 5; ++p) { const int o_ = p * 1024 + lane * 16, row = o_ / 160, ch = (o_ % 160) >> 4; wv[p] = row * (DM * 2) + ch * 16; }
        const srd_t srdO = __builtin_amdgcn_make_buffer_rsrc((void*)(ws + WS_O + (size_t)(b * NH + hg * 8 + hl) * 49152), (short)0, 49152, 0x00020000);
        u32x4 wr[10];
        const int wg0 = 10 + w4, wg1 = w4 < 2 ? 14 + w4 : 4 + w4;
        u32x4 ow[3][4];
        auto opack = [&](int i) __attribute__((always_inline)) {
            const float rl = 1.0f / (lsum[(qh * 3 + i) * 32 + r32] + lsum[((qh + 2) * 3 + i) * 32 + r32]);
#pragma unroll
            for (int db = 0; db < 2; ++db)
#pragma unroll
                for (int s = 0; s < 2; ++s) {
#pragma unroll
                    for (int e = 0; e < 4; ++e) ow[i][db * 2 + s][e] = cvtpk(o[i][db][8 * s + 2 * e] * rl, o[i][db][8 * s + 2 * e + 1] * rl);
                    st16_wt(ow[i][db * 2 + s], srdO, (r32 >> 4) * 4096 + hh * 256 + (r32 & 15) * 16, (3 * qh + i) * 8192 + (kh * 2 + db) * 1024 + s * 512); }
        };
        auto olds = [&](int i) __attribute__((always_inline)) {
#pragma unroll
            for (int db = 0; db < 2; ++db) { const lds_ptr p_ = LW + own_addr((3 * qh + i) * 4 + kh * 2 + db) + lane * 16;
                *(LAS u32x4*)p_ = ow[i][db * 2]; *(LAS u32x4*)(p_ + 1024) = ow[i][db * 2 + 1]; }
        };
#define RDV(X, vb, ks, db) do { vl[X][ks][db] = ldstr(vb + vb0 + (ks) * 4096 + (db) * 512); vh[X][ks][db] = ldstr(vb + vb1 + (ks) * 4096 + (db) * 512); } while (0)
#define RDP(buf, blk_, ks, i) pf[buf][i] = ldsr128(pb0 + ((blk_) & 1) * 6144 + ((ks) >> 1) * 24576 + ((i) * 2 + ((ks) & 1)) * 1024)
#define PVM(X, buf, ks, i, db) o[i][db] = MFMA(cat8(vl[X][ks][db], vh[X][ks][db]), pf[buf][i], o[i][db])
        auto epilogue = [&]() __attribute__((always_inline)) {
            opack(2); olds(2);
            BAR_L();
#pragma unroll
            for (int p = 0; p < 5; ++p) { *(LAS u32x4*)(LW + wg0 * 5120 + p * 1024 + lane * 16) = wr[p]; *(LAS u32x4*)(LW + wg1 * 5120 + p * 1024 + lane * 16) = wr[5 + p]; }
        };
        auto period = [&](auto tag) __attribute__((always_inline)) -> bool {
            constexpr int P = decltype(tag)::value, X = P & 1;
            constexpr bool AB = P <= 8, C = P >= 2;
            const lds_cptr vb = L + vslot(AB ? P - 1 : 0);
            auto dm = [&](int n) __attribute__((always_inline)) {
                if constexpr (P <= 5) {
                    if (n < 4) dma16<true>(srdV, kvoff, (unsigned)(KB(P + 1) * 16384 + n * 4096), lds0 + vslot(P + 1) + (4 * n + w4) * 1024);
                    else if constexpr (P < 5) dma16<true>(srdK, kvoff, (unsigned)(KB(P + 3) * 16384 + (n - 4) * 4096), lds0 + kslot(P + 3) + (4 * (n - 4) + w4) * 1024);
                    else dma16<true>(srdV, kvoff, (unsigned)(KB(7) * 16384 + (n - 4) * 4096), lds0 + vslot(7) + (4 * (n - 4) + w4) * 1024);
                } else if constexpr (P <= 8) {
                    const int g = (P == 6) ? 16 + w4 : (P == 7) ? w4 : (w4 < 2 ? 4 + w4 : 6 + w4);
                    if (n < 5) dma16<false>(srdWo, wv[n], wo_soff(g), lds0 + g * 5120 + n * 1024);
                }
            };
            auto rd = [&](int buf, int ks, int part) __attribute__((always_inline)) {
                if (part == 0) { if (C) RDP(buf, P - 2, ks, 2); }
                if (part == 1) { if (AB) RDV(X, vb, ks, 0); }
                if (part == 2) { if (AB) RDP(buf, P - 1, ks, 0); }
                if (part == 3) { if (AB) RDV(X, vb, ks, 1); }
                if (part == 4) { if (AB) RDP(buf, P - 1, ks, 1); }
            };
            auto wrl = [&](int n) __attribute__((always_inline)) {
                if constexpr (P == 6 || P == 7) { if (n < 5) ld16_async<false>(wr[(P - 6) * 5 + n], srdWo, wv[n], wo_soff(P == 6 ? wg0 : wg1)); }
            };
            const unsigned flv = flagw[P - 1];
            rd(0, 0, 0); rd(0, 0, 1); rd(0, 0, 2); rd(0, 0, 3); rd(0, 0, 4); SBAR();
            if (__builtin_expect(__builtin_amdgcn_readfirstlane(flv) != 0u, 0)) {
                if constexpr (P == 7 || P == 8) asm volatile("s_waitcnt vmcnt(0)" :: "v"(wr[0]), "v"(wr[1]), "v"(wr[2]), "v"(wr[3]), "v"(wr[4]) : "memory");
                if constexpr (P == 8) asm volatile("" :: "v"(wr[5]), "v"(wr[6]), "v"(wr[7]), "v"(wr[8]), "v"(wr[9]) : "memory");
                return true;
            }
            if constexpr (P == 9) { olds(0); olds(1); }
#pragma unroll
            for (int ks = 0; ks < 4; ++ks) {
                const int cb = ks & 1; const bool more = ks < 3;
                if (C) PVM(X ^ 1, cb, ks, 2, 0); if (more) rd(cb ^ 1, ks + 1, 0); SBAR();
                if (C) PVM(X ^ 1, cb, ks, 2, 1); if (more) rd(cb ^ 1, ks + 1, 1); wrl(2 * ks); SBAR();
                if (AB) PVM(X, cb, ks, 0, 0); if (more) rd(cb ^ 1, ks + 1, 2); SBAR();
                if (AB) PVM(X, cb, ks, 0, 1); if (more) rd(cb ^ 1, ks + 1, 3); dm(2 * ks); SBAR();
                if (AB) PVM(X, cb, ks, 1, 0); if (more) rd(cb ^ 1, ks + 1, 4); wrl(2 * ks + 1); SBAR();
                if (AB) PVM(X, cb, ks, 1, 1); dm(2 * ks + 1); SBAR();
            }
            if constexpr (P <= 5) WAITV(8); else if constexpr (P == 6) WAITV(14); else if constexpr (P == 7) WAITV(20);
            else if constexpr (P == 8) asm volatile("s_waitcnt vmcnt(5)" : "+v"(wr[0]), "+v"(wr[1]), "+v"(wr[2]), "+v"(wr[3]), "+v"(wr[4]), "+v"(wr[5]), "+v"(wr[6]), "+v"(wr[7]), "+v"(wr[8]), "+v"(wr[9]) :: "memory");
            if constexpr (P == 8) { opack(0); opack(1); }
            if constexpr (P <= 8) BAR_L(); else epilogue();
            return false;
        };
        BAR_L();
        asm volatile("s_waitcnt vmcnt(8)" : "+v"(pre[0]), "+v"(pre[1]), "+v"(pre[2]), "+v"(pre[3]), "+v"(pre[4]), "+v"(pre[5]), "+v"(pre[6]), "+v"(pre[7]) :: "memory");
#pragma unroll
        for (int n = 0; n < 8; ++n) *(LAS u32x4*)(LW + slot_addr(2 + (n >> 2)) + (4 * (n & 3) + w4) * 1024 + lane * 16) = pre[n];
        BAR_L();
        int tf = 9;
#define PERIOD(P) if (__builtin_expect(tf == 9, 1)) { if (period(std::integral_constant<int, P>{})) tf = P - 1; }
        PERIOD(1) PERIOD(2) PERIOD(3) PERIOD(4) PERIOD(5) PERIOD(6) PERIOD(7) PERIOD(8) PERIOD(9)
        slow = tf < 9;
        if (__builtin_expect(slow, 0)) {
            WAITV(0); BAR_L();
#pragma unroll 1
            for (int t = tf - 1; t < 8; ++t) {
                const int sl = slot_addr(2 + (t & 1));
#pragma unroll
                for (int n = 0; n < 4; ++n) dma16<true>(srdV, kvoff, (unsigned)(KB(t) * 16384 + n * 4096), lds0 + sl + (4 * n + w4) * 1024);
                BAR_L();
#pragma unroll
                for (int i = 0; i < 3; ++i) {
                    const float dl = fmaxf(prop[(qh * 3 + i) * 32 + r32], prop[((qh + 2) * 3 + i) * 32 + r32]);
                    const float f = __builtin_amdgcn_exp2f(-dl);
#pragma unroll
                    for (int r = 0; r < 16; ++r) { o[i][0][r] *= f; o[i][1][r] *= f; }
                }
                WAITV(0);
                BAR_L();
                const lds_cptr vb = L + sl;
#pragma unroll
                for (int ks = 0; ks < 4; ++ks) {
                    RDV(0, vb, ks, 0); RDV(0, vb, ks, 1); RDP(0, t, ks, 0); RDP(0, t, ks, 1); RDP(0, t, ks, 2);
#pragma unroll
                    for (int i = 0; i < 3; ++i) { PVM(0, 0, ks, i, 0); PVM(0, 0, ks, i, 1); }
                }
            }
            BAR_L();
#pragma unroll
            for (int p = 0; p < 5; ++p) { wr[p] = __builtin_amdgcn_raw_buffer_load_b128(srdWo, wv[p], wo_soff(wg0), 0); wr[5 + p] = __builtin_amdgcn_raw_buffer_load_b128(srdWo, wv[p], wo_soff(wg1), 0); }
#pragma unroll
            for (int p = 0; p < 10; ++p) PIN(wr[p]);
            opack(0); opack(1); olds(0); olds(1);
            epilogue();
        }
    }
    int ln; asm volatile("v_mbcnt_lo_u32_b32 %0, -1, 0\n\tv_mbcnt_hi_u32_b32 %0, -1, %0" : "=v"(ln));
    const int qg = wid & 3, kh2 = wid >> 2;
    gu32* const pflag = PFLAG(ln);
    if (__builtin_expect(slow, 0)) { if (wid < 4) { wo_groups(w4, 4, 4); wo_groups(w4 < 2 ? 4 + w4 : 6 + w4, 10, 8); wo_groups(16 + w4, 20, 4); } WAITV(0); BAR_L(); }
    {
        const int g = ln >> 4, qq = ln & 15;
        const srd_t srdOg = __builtin_amdgcn_make_buffer_rsrc((void*)(ws + WS_O + (size_t)(b * NH + hg * 8) * 49152), (short)0, 8 * 49152, 0x00020000);
        const unsigned ov0 = (g >> 1) * 1024 + (g & 1) * 512 + qq * 16, ov = ln * 16 + kh2 * 2048;
#define T16(qg_, i) (6 * ((qg_) >> 1) + ((i) < 2 ? 2 * ((qg_) & 1) + (i) : 4 + ((qg_) & 1)))
#define JISC(j) ((j) >= 8 && (j) < 20 && ((j) & 1))
#define JSTEP(j) ((j) < 8 ? (j) : (j) >= 20 ? (j) - 6 : JISC(j) ? ((j) - 5) / 2 : 4 + (j) / 2)
#define JHAS(j, i) (JISC(j) ? (i) == 2 : ((j) >= 2 && (j) < 8) ? (i) < 2 : true)
        constexpr int XC = 6;
        constexpr int OW = 5;
        u32x4 of[OW][3], fo[4][3];
#define LDOG(st, i) __builtin_amdgcn_raw_buffer_load_b128(srdOg, ov, ((hl + hdir * ((st) >> 1)) & 7) * 49152 + T16(qg, i) * 4096 + ((st) & 1) * 1024, 16)
#define LDO1(j, i) do { if (JHAS(j, i)) of[(j) % OW][i] = LDOG(JSTEP(j), i); } while (0)
#pragma unroll
        for (int st = 0; st < 2; ++st)
#pragma unroll
            for (int i = 0; i < 3; ++i) { const int t16 = T16(qg, i);
                of[st][i] = *(const LAS u32x4*)(L + own_addr((t16 >> 1) * 4 + kh2 * 2 + st) + (t16 & 1) * 256 + ov0); }
        f32x4 acc[5][3];
#pragma unroll
        for (int n = 0; n < 5; ++n)
#pragma unroll
            for (int i = 0; i < 3; ++i) acc[n][i] = f32x4{};
        lds_cptr wb[3];
        wb[0] = L + kh2 * 5120 + (16 * (g >> 1) + 4 * (g & 1) + (qq >> 2)) * 160 + (ln & 3) * 8;
        wb[1] = wb[0] + 61440; wb[2] = wb[0] + 122880;
        PIN(wb[1]); PIN(wb[2]);
        s16x4 wl[2][5], wh[2][5];
#define RDWO(buf, st, n) do { const lds_cptr p_ = wb[PAIR_OF_STEP[st] / 6] + (PAIR_OF_STEP[st] % 6) * 10240 + (n) * 32; wl[buf][n] = ldstr(p_); wh[buf][n] = ldstr(p_ + 1280); } while (0)
#pragma unroll
        for (int n = 0; n < 5; ++n) RDWO(0, 0, n);
        if (wid >= 4) WAITV(4);
        BAR_L();
        if (wid == 0) { if (ln == hl) __hip_atomic_store(pflag, base + 1u, RLX_AGENT); }
        else if (wid < 4) wo_groups(19 + w4, 32, 3);
        SBAR();
        unsigned fc = 0;
#pragma unroll
        for (int j = 0; j < 20; ++j) {
            if (j == 1) {
                if (wid >= 4) WAITV(0);
                BAR_L();
                if (wid == 0 && ln == hl) __hip_atomic_store(pflag, base + 2u, RLX_AGENT);
                SBAR();
            }
            if (j == 2) {
                if (wid == 0) {
#pragma unroll 1
                    for (unsigned sp = 0; sp < SPIN_CAP; ++sp) {
                        const unsigned v = __hip_atomic_load(pflag, RLX_AGENT);
                        if (!__any(ln < 8 && v == base)) break;
                        __builtin_amdgcn_s_sleep(1);
                    }
                }
                BAR_L();
#pragma unroll
                for (int j2 = 2; j2 < 2 + OW - 1; ++j2) { LDO1(j2, 0); LDO1(j2, 1); LDO1(j2, 2); }
                SBAR();
            }
            if (j == XC - 1 && wid == 0) fc = __hip_atomic_load(pflag, RLX_AGENT);
            if (j == XC) {
                if (wid == 0) {
#pragma unroll 1
                    for (unsigned sp = 0; __any(ln < 8 && fc - base < 2u) && sp < SPIN_CAP; ++sp) { __builtin_amdgcn_s_sleep(1); fc = __hip_atomic_load(pflag, RLX_AGENT); }
                }
                BAR_L();
            }
            if (j >= 2 && (j & 1) == 0) { if (kh2 == ((j >> 1) & 1)) __builtin_amdgcn_s_setprio(1); else __builtin_amdgcn_s_setprio(0); }
            if (j == 18) BAR_L();
#pragma unroll
            for (int n = 0; n < 5; ++n) {
#pragma unroll
                for (int i = 0; i < 3; ++i)
                    if (JHAS(j, i)) acc[n][i] = MFMA16(cat8(wl[j & 1][n], wh[j & 1][n]), __builtin_bit_cast(bf16x8, of[j % OW][i]), acc[n][i]);
                if (j < 19) RDWO((j + 1) & 1, JSTEP(j + 1), n);
                if (j >= 2 && j + OW - 1 < 20 && n < 3 && (n < 2 || j >= XC)) LDO1(j + OW - 1, n);
                if (j == XC && n >= 3 && n + 5 < XC + OW - 1) LDO1(n + 5, 2);
                if (j >= 16 && n < 3) fo[j - 16][n] = __builtin_amdgcn_raw_buffer_load_b128(srdOg, ln * 16, ((hl + hdir * 7) & 7) * 49152 + T16(qg, n) * 4096 + (j - 16) * 1024, 16);
                SBAR();
            }
        }
        const int dst = ((xcd & 4) | qg) + 8 * jj;
        const srd_t srdP = __builtin_amdgcn_make_buffer_rsrc((void*)(ws + WS_PART + (size_t)dst * 24576 + ((hg - qg - 1) & 3) * 8192), (short)0, 8192, 0x00020000);
        auto fin = [&](auto tag) __attribute__((always_inline)) {
            constexpr int H = decltype(tag)::value, N0 = H ? 3 : 0, N1 = H ? 5 : 3;
            const lds_cptr wf = wb[2] - kh2 * 5120 + 20480;
#define RDWF(buf, db, n) do { const lds_cptr p_ = wf + ((db) & 1) * 10240 + ((db) >> 1) * 5120 + (n) * 32; wl[buf][n] = ldstr(p_); wh[buf][n] = ldstr(p_ + 1280); } while (0)
#pragma unroll
            for (int n = N0; n < N1; ++n) RDWF(0, 0, n);
            f32x4 pa_[9];
#pragma unroll
            for (int db = 0; db < 4; ++db) {
                if (db == 2) { BAR_L(); }
#pragma unroll
                for (int n = N0; n < N1; ++n) {
#pragma unroll
                    for (int i = 0; i < 3; ++i) acc[n][i] = MFMA16(cat8(wl[db & 1][n], wh[db & 1][n]), __builtin_bit_cast(bf16x8, fo[db][i]), acc[n][i]);
                    if (db < 3) RDWF((db + 1) & 1, db + 1, n);
                    if (db < 2) {
                        constexpr int NO = H ? 9 : 6, PER = H ? 3 : 1;
#pragma unroll
                        for (int k = 0; k < PER; ++k) { const int q_ = (db * (N1 - N0) + (n - N0)) * PER + k, tl = (H ? 0 : 9) + q_;
                            if (q_ < NO) *(LAS f32x4*)(LW + ((qg * 15 + tl) * 64 + ln) * 16) = acc[tl / 3][tl % 3]; }
                    }
                    if (db == 2) {
#pragma unroll
                        for (int i = 0; i < 3; ++i) pa_[(n - N0) * 3 + i] = *(const LAS f32x4*)(L + ((qg * 15 + n * 3 + i) * 64 + ln) * 16);
                    }
                    SBAR();
                }
            }
#pragma unroll
            for (int u = (H ? 5 : 0); u < (H ? 8 : 5); ++u) {
                u32x4 w = {};
#pragma unroll
                for (int k = 0; k < 2; ++k) { const int tl = H ? 2 * u - 1 + k : 2 * u + k;
                    if (tl < 3 * N1) {
                        const f32x4 v = acc[tl / 3][tl % 3] + pa_[tl - 3 * N0];
                        if (qg == hg) *(LAS f32x4*)(LW + ((qg * 15 + tl) * 64 + ln) * 16) = v;
                        w[2 * k] = cvtpk(v[0], v[1]); w[2 * k + 1] = cvtpk(v[2], v[3]);
                    } }
                if (qg != hg) st16_wt(w, srdP, ln * 16, u * 1024);
            }
        };
        if (kh2) fin(std::integral_constant<int, 1>{}); else fin(std::integral_constant<int, 0>{});
        WAITV(0);
        BAR_L();
        if (wid == 0) {
            if (ln == 8 + hg) __hip_atomic_store(pflag, base + 1u, RLX_AGENT);
#pragma unroll 1
            for (unsigned sp = 0; sp < SPIN_CAP; ++sp) {
                const unsigned v = __hip_atomic_load(pflag, RLX_AGENT);
                if (!__any(ln >= 8 && ln < 12 && v == base)) break;
                __builtin_amdgcn_s_sleep(1);
            }
        }
        BAR_L();
        const srd_t srdI = __builtin_amdgcn_make_buffer_rsrc((void*)(ws + WS_PART + (size_t)blockIdx.x * 24576), (short)0, 24576, 0x00020000);
        const srd_t srdOut = __builtin_amdgcn_make_buffer_rsrc((void*)(out + (size_t)b * SQ * DM), (short)0, SQ * DM * 2, 0x00020000);
        u32x4 pv_[3];
#pragma unroll
        for (int j = 0; j < 3; ++j) pv_[j] = __builtin_amdgcn_raw_buffer_load_b128(srdI, ln * 16, j * 8192 + wid * 1024, 16);
#pragma unroll
        for (int k = 0; k < 2; ++k) {
            const int tl = wid < 5 ? 2 * wid + k : 2 * wid - 1 + k;
            if (!(wid == 4 && k == 1)) {
                f32x4 sum = *(const LAS f32x4*)(L + ((hg * 15 + tl) * 64 + ln) * 16);
#pragma unroll
                for (int j = 0; j < 3; ++j) {
                    sum[0] += __uint_as_float(pv_[j][2 * k] << 16); sum[1] += __uint_as_float(pv_[j][2 * k] & 0xffff0000u);
                    sum[2] += __uint_as_float(pv_[j][2 * k + 1] << 16); sum[3] += __uint_as_float(pv_[j][2 * k + 1] & 0xffff0000u);
                }
                const int n = tl / 3, i = tl % 3;
                const int q = T16(hg, i) * 16 + qq, col = hl * 80 + n * 16 + g * 4;
                u32x2 w; w[0] = cvtpk(sum[0], sum[1]); w[1] = cvtpk(sum[2], sum[3]);
                __builtin_amdgcn_raw_buffer_store_b64(w, srdOut, (q * DM + col) * 2, 0, 0);
            }
        }
    }
}

extern "C" void kernel_launch(void* const* d_in, const int* in_sizes, int n_in, void* d_out, int out_size, void* d_ws, size_t ws_size, hipStream_t stream) {
    const bf16* x = (const bf16*)d_in[0]; const bf16* kc = (const bf16*)d_in[1]; const bf16* vc = (const bf16*)d_in[2];
    const bf16* wq = (const bf16*)d_in[3]; const bf16* wo = (const bf16*)d_in[4];
    (void)hipFuncSetAttribute((const void*)k1_attn, hipFuncAttributeMaxDynamicSharedMemorySize, LDS_BYTES);
    k1_attn<<<256, 512, LDS_BYTES, stream>>>(x, kc, vc, wq, wo, (char*)d_ws, (bf16*)d_out);
}
```

```cpp
#include <hip/hip_runtime.h>
#include <hip/hip_bf16.h>
#include <stdint.h>
#include <type_traits>

typedef __hip_bfloat16 bf16;
using bf16x8 = __attribute__((ext_vector_type(8))) short;
using s16x4  = __attribute__((ext_vector_type(4))) short;
using f32x16 = __attribute__((ext_vector_type(16))) float;
using f32x4  = __attribute__((ext_vector_type(4))) float;
using u32x4  = __attribute__((ext_vector_type(4))) unsigned;
using u32x2  = __attribute__((ext_vector_type(2))) unsigned;
typedef short v4i16_t __attribute__((ext_vector_type(4)));
#define LAS __attribute__((address_space(3)))
typedef LAS const char* lds_cptr;
typedef LAS char* lds_ptr;
typedef __amdgpu_buffer_rsrc_t srd_t;
#define DI __device__ __forceinline__
#define SBAR() __builtin_amdgcn_sched_barrier(0)
#define PIN(x) asm volatile("" : "+v"(x))
#define MFMA(a, b, c) __builtin_amdgcn_mfma_f32_32x32x16_bf16(a, b, c, 0, 0, 0)

constexpr int NB = 8, SQ = 192, DM = 640, NH = 32, SK = 512, DH = 128, HD = NH * DH;
constexpr float QSCALE = 0.08838834764831845f * 1.4426950408889634f;
#define PSLIM 65536.0f

typedef float f32x2 __attribute__((ext_vector_type(2)));
typedef __bf16 bf16x2 __attribute__((ext_vector_type(2)));
DI unsigned cvtpk(float lo, float hi) { f32x2 v = {lo, hi}; bf16x2 b = __builtin_convertvector(v, bf16x2); return __builtin_bit_cast(unsigned, b); }
template <bool NT>
DI void dma16(srd_t srd, unsigned voff, unsigned soff, unsigned lds_dst_) {
    const unsigned lds_dst = __builtin_amdgcn_readfirstlane(lds_dst_);
    if constexpr (NT) asm volatile("s_mov_b32 m0, %3\n\ts_nop 0\n\tbuffer_load_dwordx4 %0, %1, %2 offen nt lds" :: "v"(voff), "s"(srd), "s"(soff), "s"(lds_dst) : "m0", "memory");
    else              asm volatile("s_mov_b32 m0, %3\n\ts_nop 0\n\tbuffer_load_dwordx4 %0, %1, %2 offen lds" :: "v"(voff), "s"(srd), "s"(soff), "s"(lds_dst) : "m0", "memory");
}
template <bool NT>
DI void ld16_async(u32x4& r, srd_t srd, unsigned voff, unsigned soff) {
    if constexpr (NT) asm volatile("buffer_load_dwordx4 %0, %1, %2, %3 offen nt" : "=v"(r) : "v"(voff), "s"(srd), "s"(soff) : "memory");
    else              asm volatile("buffer_load_dwordx4 %0, %1, %2, %3 offen" : "=v"(r) : "v"(voff), "s"(srd), "s"(soff) : "memory");
}
DI void touch4(srd_t srd, unsigned voff, unsigned soff, unsigned lds_dst) {
    asm volatile("s_mov_b32 m0, %3\n\ts_nop 0\n\tbuffer_load_dword %0, %1, %2 offen lds" :: "v"(voff), "s"(srd), "s"(soff), "s"(lds_dst) : "m0", "memory");
}
DI bf16x8 ldsr128(lds_cptr p) { return *(const LAS bf16x8*)p; }
DI s16x4 ldstr(lds_cptr p) { return __builtin_bit_cast(s16x4, __builtin_amdgcn_ds_read_tr16_b64_v4i16((LAS v4i16_t*)p)); }
DI bf16x8 cat8(s16x4 a, s16x4 b) { return __builtin_shufflevector(a, b, 0, 1, 2, 3, 4, 5, 6, 7); }
#define WAITV(N) asm volatile("s_waitcnt vmcnt(" #N ")" ::: "memory")
#define BAR_L() asm volatile("s_waitcnt lgkmcnt(0)\n\ts_barrier" ::: "memory")
DI float xhalf_max(float v) { auto rr = __builtin_amdgcn_permlane32_swap(__float_as_uint(v), __float_as_uint(v), false, false); return fmaxf(__uint_as_float(rr[0]), __uint_as_float(rr[1])); }
DI float xhalf_sum(float v) { auto rr = __builtin_amdgcn_permlane32_swap(__float_as_uint(v), __float_as_uint(v), false, false); return __uint_as_float(rr[0]) + __uint_as_float(rr[1]); }

#define GAS __attribute__((address_space(1)))
typedef GAS unsigned gu32;
#define RLX_AGENT __ATOMIC_RELAXED, __HIP_MEMORY_SCOPE_AGENT
#define MFMA16(a, b, c) __builtin_amdgcn_mfma_f32_16x16x32_bf16(a, b, c, 0, 0, 0)
DI void st16_wt(u32x4 v, srd_t srd, unsigned voff, unsigned soff) {
    __builtin_amdgcn_raw_buffer_store_b128(v, srd, voff, soff, 16); SBAR(); asm volatile("s_nop 1"); SBAR();
}
constexpr unsigned ECHO_KEY = 0x5A17C3E9u;
constexpr unsigned SPIN_CAP = 1u << 19;
constexpr size_t WS_O = 0, WS_PART = 16u << 20, WS_SYNC = 48u << 20;
constexpr int PAIR_OF_STEP[16] = {8, 9, 0, 1, 2, 4, 5, 6, 7, 3, 10, 11, 12, 13, 14, 15};
constexpr unsigned long long STEP_OF_PAIR = 0xFEDCBA1087659432ull;
constexpr unsigned CODE_TOUCH = 28672;
constexpr int SLOT = 16384;
constexpr int MOFF = 32768;
constexpr int RING0 = 40960, RSTG = 40960, RXB = 24576;
constexpr int POFF = 106496;
constexpr int LDS_BYTES = 163840;
constexpr unsigned KSLOTS = 0x04215310u;
constexpr unsigned VSLOTS = 0x32153042u;
DI int slot_addr(unsigned s) { return s * SLOT + (s >= 2 ? 8192 : 0); }
DI int own_addr(int pr) { return pr < 20 ? POFF + (pr / 5) * 12288 + (pr % 5) * 2048 : 155648 + (pr - 20) * 2048; }
DI int kslot(int t) { return slot_addr((KSLOTS >> (4 * t)) & 15u); }
DI int vslot(int t) { return slot_addr((VSLOTS >> (4 * t)) & 15u); }

extern "C" __global__ __launch_bounds__(512, 2) void k1_attn(const bf16* __restrict__ x, const bf16* __restrict__ kc, const bf16* __restrict__ vc,
                                                  const bf16* __restrict__ wq, const bf16* __restrict__ wo, char* __restrict__ ws, bf16* __restrict__ out) {
    extern __shared__ __attribute__((aligned(16))) char lds[];
    unsigned long long pc0; asm volatile("s_getpc_b64 %0" : "=s"(pc0));
    const int tid = threadIdx.x, lane = tid & 63, r32 = lane & 31, hh = lane >> 5;
    const int wid = __builtin_amdgcn_readfirstlane(tid >> 6), w4 = wid & 3;
    const int xcd = blockIdx.x & 7, jj = blockIdx.x >> 3;
    const int bl = jj >> 3, hl = jj & 7;
    const int b = (xcd >> 2) * 4 + bl, h = (xcd & 3) * 8 + hl;
    const unsigned lds0 = (unsigned)(uintptr_t)lds;
    const lds_cptr L = (lds_cptr)lds;
    const lds_ptr LW = (lds_ptr)lds;
    const bf16* Kg = kc + (size_t)(b * NH + h) * SK * DH;
    const bf16* Vg = vc + (size_t)(b * NH + h) * SK * DH;
    const int kvrot = (jj + xcd) & 7;
#define KB(t_) (((t_) + kvrot) & 7)
    const int qh = w4 & 1, kh = w4 >> 1;
    const int q4 = (lane & 15) >> 2, p4 = lane & 3, blk = (lane >> 4) & 1;
    LAS unsigned* flagw = (LAS unsigned*)(LW + MOFF);
    LAS unsigned* basew = (LAS unsigned*)(LW + MOFF + 128);
    const int hg = xcd & 3;
    gu32* const F = (gu32*)(ws + WS_SYNC);
    gu32* const E = (gu32*)(ws + WS_SYNC + 65536);
#define PEER(l_) ((l_) < 8 ? xcd + 8 * (bl * 8 + (l_)) : ((xcd & 4) | ((l_) & 3)) + 8 * jj)
#define PFLAG(l_) (F + (((l_) < 8 ? 0 : 256) + PEER(l_)) * 32)
    LAS float* prop = (LAS float*)(LW + MOFF + 256);
    LAS float* lsum = (LAS float*)(LW + MOFF + 2048);

    const int hdir = (bl & 1) ? -1 : 1;
    const srd_t srdWo = __builtin_amdgcn_make_buffer_rsrc((void*)(wo + hl * 80), (short)0, HD * DM * 2 - hl * 160, 0x00020000);
    auto wo_soff = [&](int g) __attribute__((always_inline)) -> unsigned {
        const int st = (int)((STEP_OF_PAIR >> (4 * (g >> 1))) & 15u);
        return (unsigned)(((hg * 8 + ((hl + hdir * (st >> 1)) & 7)) * 128 + (2 * (g & 1) + (st & 1)) * 32) * DM * 2);
    };
    auto wo_groups = [&](int g0, int g1, int gs) __attribute__((always_inline)) {
        int l_; asm volatile("v_mbcnt_lo_u32_b32 %0, -1, 0\n\tv_mbcnt_hi_u32_b32 %0, -1, %0" : "=v"(l_));
        unsigned wv[5];
#pragma unroll
        for (int p = 0; p < 5; ++p) { const int o_ = p * 1024 + l_ * 16, row = o_ / 160, ch = (o_ % 160) >> 4; wv[p] = row * (DM * 2) + ch * 16; }
#pragma unroll 1
        for (int g = g0; g < g1; g += gs) {
            const unsigned so = wo_soff(g);
#pragma unroll
            for (int p = 0; p < 5; ++p) dma16<false>(srdWo, wv[p], so, lds0 + g * 5120 + p * 1024);
        }
    };
    bool slow = false;
    unsigned base = 0;

    if (wid < 4) {
        if (wid == 0) {
            if (lane < 12) {
                const unsigned base = __hip_atomic_load(PFLAG(lane), RLX_AGENT);
                basew[lane] = base;
                __hip_atomic_store(E + blockIdx.x * 32 + lane, base ^ ECHO_KEY, RLX_AGENT);
            }
        }
        if (wid == 1) {
            const srd_t srdC = __builtin_amdgcn_make_buffer_rsrc((void*)pc0, (short)0, CODE_TOUCH, 0x00020000);
#pragma unroll 1
            for (unsigned k = jj & 1; k * 8192u < CODE_TOUCH; k += 2) touch4(srdC, lane * 128, k * 8192u, lds0 + MOFF + 4096u);
        }
        bf16x8 qf[3][8];
        {
            f32x16 acc[3][2];
#pragma unroll
            for (int i = 0; i < 3; ++i) { acc[i][0] = f32x16{}; acc[i][1] = f32x16{}; }
            const int xsw = (r32 >> 1) & 7;
            int xa[4];
#pragma unroll
            for (int ks = 0; ks < 4; ++ks) xa[ks] = (96 * qh + r32) * 128 + (((2 * ks + hh) ^ xsw) << 4);
            const int ps = ((p4 & 1) << 1) | (p4 >> 1);
            const int wa0 = RXB + 2048 * hh + 1024 * kh + 64 * q4 + (((2 * blk + (ps >> 1)) ^ (2 * hh)) << 4) + 8 * (ps & 1);
            const int wa1 = RXB + 2048 * hh + 1024 * kh + 64 * q4 + 256 + (((2 * blk + (ps >> 1)) ^ (2 * hh + 1)) << 4) + 8 * (ps & 1);
            bf16x8 xf[2][3]; s16x4 wl[2][2], wh[2][2];
#define RDX(buf, base, ks, i) xf[buf][i] = ldsr128(base + xa[ks] + i * 4096)
#define RDW(buf, base, ks, db) do { wl[buf][db] = ldstr(base + wa0 + ks * 4096 + db * 512); wh[buf][db] = ldstr(base + wa1 + ks * 4096 + db * 512); } while (0)
#define MM(buf, i, db) acc[i][db] = MFMA(cat8(wl[buf][db], wh[buf][db]), xf[buf][i], acc[i][db])
#define HALF(cb, base, ks) do { \
                MM(cb, 0, 0); RDW(cb ^ 1, base, ks, 0); SBAR(); \
                MM(cb, 0, 1); RDX(cb ^ 1, base, ks, 0); SBAR(); \
                MM(cb, 1, 0); RDW(cb ^ 1, base, ks, 1); SBAR(); \
                MM(cb, 1, 1); RDX(cb ^ 1, base, ks, 1); SBAR(); \
                MM(cb, 2, 0); RDX(cb ^ 1, base, ks, 2); SBAR(); \
                MM(cb, 2, 1); SBAR(); } while (0)
            BAR_L();
            { const lds_cptr base = L + RING0;
              RDW(0, base, 0, 0); RDX(0, base, 0, 0); RDW(0, base, 0, 1); RDX(0, base, 0, 1); RDX(0, base, 0, 2); SBAR();
              HALF(0, base, 1); HALF(1, base, 2); HALF(0, base, 3); }
            int rb = RSTG;
#pragma unroll 1
            for (int s = 1; s < 10; ++s) {
                SBAR(); BAR_L(); SBAR();
                const lds_cptr base = L + RING0 + rb;
                HALF(1, base, 0); HALF(0, base, 1); HALF(1, base, 2); HALF(0, base, 3);
                rb = (rb == 2 * RSTG) ? 0 : rb + RSTG;
            }
            BAR_L();
            MM(1, 0, 0); MM(1, 0, 1); MM(1, 1, 0); MM(1, 1, 1); MM(1, 2, 0); MM(1, 2, 1);
#pragma unroll
            for (int i = 0; i < 3; ++i)
#pragma unroll
                for (int db = 0; db < 2; ++db)
#pragma unroll
                    for (int s = 0; s < 2; ++s) {
                        u32x4 w;
#pragma unroll
                        for (int e = 0; e < 4; ++e) w[e] = cvtpk(acc[i][db][8 * s + 2 * e] * QSCALE, acc[i][db][8 * s + 2 * e + 1] * QSCALE);
                        *(LAS u32x4*)(LW + POFF + (w4 ^ 2) * 12288 + (i * 4 + db * 2 + s) * 1024 + lane * 16) = w;
                        qf[i][db * 2 + s] = __builtin_bit_cast(bf16x8, w);
                    }
            if (wid == 1) WAITV(0);
            BAR_L();
#pragma unroll
            for (int i = 0; i < 3; ++i)
#pragma unroll
                for (int j = 0; j < 4; ++j) qf[i][4 + j] = ldsr128(L + POFF + w4 * 12288 + (i * 4 + j) * 1024 + lane * 16);
        }
        const int krow = 32 * kh + r32;
        const int kb0 = 2048 * (krow >> 3) + 64 * (krow & 7) + ((hh ^ ((krow >> 2) & 3)) << 4);
        const int kb1 = 2048 * (krow >> 3) + 64 * (krow & 7) + (((2 + hh) ^ ((krow >> 2) & 3)) << 4);
        const int kown = kh * 1024, koth = (1 - kh) * 1024;
        bf16x8 kf[8];
        unsigned ka[4];
#define KRD(j) asm volatile("ds_read_b128 %0, %1 offset:%2" : "+v"(kf[j]) : "v"(ka[((j) >> 2) * 2 + ((j) & 1)]), "i"(512 * (((j) >> 1) & 1)) : "memory")
#define KADDR(t_) do { const unsigned kb_ = lds0 + kslot(t_); ka[0] = kb_ + kown + kb0; ka[1] = kb_ + kown + kb1; ka[2] = kb_ + koth + kb0; ka[3] = kb_ + koth + kb1; } while (0)
#define KRDALL() do { KRD(0); KRD(1); KRD(2); KRD(3); KRD(4); KRD(5); KRD(6); KRD(7); } while (0)
        f32x16 negm[3], sc;
        float l[3], m[3], lold[3];
#pragma unroll
        for (int j = 0; j < 8; ++j) kf[j] = bf16x8{};
        KADDR(0); KRDALL();
        asm volatile("s_waitcnt lgkmcnt(0)" ::: "memory"); SBAR();
        {
            const lds_ptr pw = LW + POFF + w4 * 12288 + lane * 16;
            f32x16 s0[3]; float d[3];
#pragma unroll
            for (int i = 0; i < 3; ++i) {
                s0[i] = f32x16{};
#pragma unroll
                for (int j = 0; j < 8; ++j) s0[i] = MFMA(kf[j], qf[i][j], s0[i]);
            }
            SBAR();
            KADDR(1); KRDALL();
#pragma unroll
            for (int i = 0; i < 3; ++i) {
                float pm = s0[i][0];
#pragma unroll
                for (int r = 1; r < 16; ++r) pm = fmaxf(pm, s0[i][r]);
                d[i] = xhalf_max(pm);
                prop[(w4 * 3 + i) * 32 + r32] = d[i];
            }
            BAR_L();
#pragma unroll
            for (int i = 0; i < 3; ++i) {
                m[i] = fmaxf(d[i], prop[((w4 ^ 2) * 3 + i) * 32 + r32]);
                float psum = 0.f;
#pragma unroll
                for (int r = 0; r < 16; ++r) { negm[i][r] = -m[i]; s0[i][r] -= m[i]; }
                if (i < 2) {
#pragma unroll
                    for (int r = 0; r < 16; ++r) { s0[i][r] = __builtin_amdgcn_exp2f(s0[i][r]); psum += s0[i][r]; }
#pragma unroll
                    for (int s = 0; s < 2; ++s) { u32x4 w;
#pragma unroll
                        for (int e = 0; e < 4; ++e) w[e] = cvtpk(s0[i][8 * s + 2 * e], s0[i][8 * s + 2 * e + 1]);
                        *(LAS u32x4*)(pw + (i * 2 + s) * 1024) = w; }
                }
                l[i] = psum; lold[i] = psum;
            }
            sc = s0[2];
            BAR_L(); SBAR();
        }
        __builtin_amdgcn_s_setprio(1);
#define EX2(X, g) do { X[2 * (g)] = __builtin_amdgcn_exp2f(X[2 * (g)]); X[2 * (g) + 1] = __builtin_amdgcn_exp2f(X[2 * (g) + 1]); } while (0)
#define AP(X, g) do { if ((g) == 0) pa = X[0] + X[1]; else { pa += X[2 * (g)]; pa += X[2 * (g) + 1]; } w[(g) & 3] = cvtpk(X[2 * (g)], X[2 * (g) + 1]); PIN(pa); PIN(w); } while (0)
#define PST(PW, i, s) *(LAS u32x4*)(PW + ((i) * 2 + (s)) * 1024) = w
#define SMS(X, PW, i, g) do { EX2(X, g); if ((g) > 0) AP(X, (g) - 1); if ((g) == 4) PST(PW, i, 0); PIN(X); } while (0)
#define SMFIN(X, PW, i) do { AP(X, 7); PST(PW, i, 1); bad |= !(pa <= PSLIM); lold[i] = l[i]; l[i] += pa; PIN(l[i]); } while (0)
#define QK(S, i, j) S = MFMA(kf[j], qf[i][j], (j) ? S : negm[i])
        auto echo_check = [&]() __attribute__((always_inline)) {
            if (wid == 0) {
                base = basew[lane < 12 ? lane : 0];
                const unsigned want = (lane < 8 ? basew[hl] : basew[8 + hg]) ^ ECHO_KEY;
                gu32* const pecho = E + PEER(lane) * 32 + (lane < 8 ? hl : 8 + hg);
#pragma unroll 1
                for (unsigned sp = 0; sp < SPIN_CAP; ++sp) {
                    const unsigned echo = __hip_atomic_load(pecho, RLX_AGENT);
                    if (!__any(lane < 12 && echo != want)) break;
                    __builtin_amdgcn_s_sleep(2);
                }
            }
        };
        u32x4 w = {};
        int tf = 9;
#pragma unroll 1
        for (int t = 1; t < 8; ++t) {
            const lds_ptr pw = LW + POFF + w4 * 12288 + (t & 1) * 6144 + lane * 16;
            const lds_ptr pc = LW + POFF + w4 * 12288 + ((t & 1) ^ 1) * 6144 + lane * 16;
            f32x16 sa, sb; float pa; bool bad = false;
            const unsigned flv = flagw[t - 1];
            SBAR();
            QK(sa, 0, 0); SMS(sc, pc, 2, 0); SBAR(); QK(sa, 0, 1); SMS(sc, pc, 2, 1); SBAR(); QK(sa, 0, 2); SMS(sc, pc, 2, 2); SBAR();
            if (__builtin_expect(__builtin_amdgcn_readfirstlane(flv) != 0u, 0)) { tf = t - 1; break; }
            QK(sa, 0, 3); SMS(sc, pc, 2, 3); SBAR(); QK(sa, 0, 4); SMS(sc, pc, 2, 4); SBAR(); QK(sa, 0, 5); SMS(sc, pc, 2, 5); SBAR();
            QK(sa, 0, 6); SMS(sc, pc, 2, 6); SBAR(); QK(sa, 0, 7); SMS(sc, pc, 2, 7); SBAR();
            QK(sb, 1, 0); SMFIN(sc, pc, 2); KADDR(t < 7 ? t + 1 : 7); SBAR();
            QK(sb, 1, 1); SBAR(); SMS(sa, pw, 0, 0); SBAR(); QK(sb, 1, 2); SMS(sa, pw, 0, 1); SBAR(); QK(sb, 1, 3); SMS(sa, pw, 0, 2); SBAR();
            QK(sb, 1, 4); SMS(sa, pw, 0, 3); SBAR(); QK(sb, 1, 5); SMS(sa, pw, 0, 4); SBAR(); QK(sb, 1, 6); SMS(sa, pw, 0, 5); SBAR(); QK(sb, 1, 7); SMS(sa, pw, 0, 6); SBAR();
            QK(sc, 2, 0); SBAR(); KRD(0); SMS(sa, pw, 0, 7); SBAR(); SMFIN(sa, pw, 0); SBAR();
            QK(sc, 2, 1); SBAR(); KRD(1); SMS(sb, pw, 1, 0); SBAR(); SMS(sb, pw, 1, 1); SBAR();
            QK(sc, 2, 2); SBAR(); KRD(2); SMS(sb, pw, 1, 2); SBAR(); QK(sc, 2, 3); SBAR(); KRD(3); SMS(sb, pw, 1, 3); SBAR();
            QK(sc, 2, 4); SBAR(); KRD(4); SMS(sb, pw, 1, 4); SBAR(); QK(sc, 2, 5); SBAR(); KRD(5); SMS(sb, pw, 1, 5); SBAR(); SMS(sb, pw, 1, 6); SBAR();
            QK(sc, 2, 6); SBAR(); KRD(6); SMS(sb, pw, 1, 7); SBAR(); QK(sc, 2, 7); SBAR(); KRD(7); SMFIN(sb, pw, 1); SBAR();
            if (__builtin_expect(__any(bad), 0)) { if (lane == 0) flagw[t] = 1u; }
            if (t == 7) { lsum[(w4 * 3 + 0) * 32 + r32] = xhalf_sum(l[0]); lsum[(w4 * 3 + 1) * 32 + r32] = xhalf_sum(l[1]); }
            BAR_L(); SBAR();
        }
        if (__builtin_expect(tf == 9, 1)) {
            if (__builtin_expect(__builtin_amdgcn_readfirstlane(flagw[7]) != 0u, 0)) tf = 7;
            else {
                const lds_ptr pc = LW + POFF + w4 * 12288 + 6144 + lane * 16;
                float pa; bool bad = false;
                lold[0] = l[0]; lold[1] = l[1];
                SMS(sc, pc, 2, 0); SMS(sc, pc, 2, 1); SMS(sc, pc, 2, 2); SMS(sc, pc, 2, 3); SMS(sc, pc, 2, 4); SMS(sc, pc, 2, 5); SMS(sc, pc, 2, 6); SMS(sc, pc, 2, 7); SMFIN(sc, pc, 2);
                if (__builtin_expect(__any(bad), 0)) { if (lane == 0) flagw[8] = 1u; }
                lsum[(w4 * 3 + 2) * 32 + r32] = xhalf_sum(l[2]);
                echo_check();
                BAR_L();
                if (__builtin_expect(__builtin_amdgcn_readfirstlane(flagw[8]) != 0u, 0)) tf = 8;
            }
        }
        slow = tf < 9;
        if (__builtin_expect(slow, 0)) {
#pragma unroll
            for (int i = 0; i < 3; ++i) l[i] = lold[i];
            BAR_L();
#pragma unroll 1
            for (int t = tf - 1; t < 8; ++t) {
                const bool all = t >= tf;
                const lds_ptr pw = LW + POFF + w4 * 12288 + (t & 1) * 6144 + lane * 16;
#pragma unroll
                for (int j = 0; j < 8; ++j) { const int s = (j < 4) ? 4 * kh + j : 4 * (1 - kh) + (j - 4);
                    kf[j] = *(const bf16x8*)(Kg + (size_t)(KB(t) * 64 + krow) * DH + s * 16 + hh * 8); }
                f32x16 s0[3]; float d[3];
#pragma unroll
                for (int i = 0; i < 3; ++i) {
                    s0[i] = f32x16{};
#pragma unroll
                    for (int j = 0; j < 8; ++j) s0[i] = MFMA(kf[j], qf[i][j], s0[i]);
                    float pm = s0[i][0];
#pragma unroll
                    for (int r = 1; r < 16; ++r) pm = fmaxf(pm, s0[i][r]);
                    d[i] = (all || i == 2) ? fmaxf(xhalf_max(pm) - m[i], 0.f) : 0.f;
                    prop[(w4 * 3 + i) * 32 + r32] = d[i];
                }
                BAR_L();
#pragma unroll
                for (int i = 0; i < 3; ++i) {
                    const float dl = fmaxf(d[i], prop[((w4 ^ 2) * 3 + i) * 32 + r32]);
                    m[i] += dl;
                    float psum = l[i] * __builtin_amdgcn_exp2f(-dl);
#pragma unroll
                    for (int r = 0; r < 16; ++r) { s0[i][r] = (all || i == 2) ? __builtin_amdgcn_exp2f(s0[i][r] - m[i]) : 0.f; psum += s0[i][r]; }
                    l[i] = psum;
#pragma unroll
                    for (int s = 0; s < 2; ++s) { u32x4 w2;
#pragma unroll
                        for (int e = 0; e < 4; ++e) w2[e] = cvtpk(s0[i][8 * s + 2 * e], s0[i][8 * s + 2 * e + 1]);
                        *(LAS u32x4*)(pw + (i * 2 + s) * 1024) = w2; }
                    lsum[(w4 * 3 + i) * 32 + r32] = xhalf_sum(psum);
                }
                BAR_L();
            }
            BAR_L();
        }
        if (__builtin_expect(slow, 0)) echo_check();
        __builtin_amdgcn_s_setprio(0);
        BAR_L();
        if (wid == 0 && lane == hl) __hip_atomic_store(PFLAG(lane), base + 1u, RLX_AGENT);
    } else {
        const srd_t srdK = __builtin_amdgcn_make_buffer_rsrc((void*)Kg, (short)0, SK * DH * 2, 0x00020000);
        const srd_t srdV = __builtin_amdgcn_make_buffer_rsrc((void*)Vg, (short)0, SK * DH * 2, 0x00020000);
        const int arow = 8 * (w4 >> 1) + ((lane >> 2) & 7), ach = 4 * (2 * (w4 & 1) + hh) + (p4 ^ ((2 * (w4 >> 1) + blk) & 3));
        const unsigned kvoff = arow * 256 + ach * 16;
        auto kv_unit = [&](bool isV, int t) {
#pragma unroll
            for (int i = 0; i < 4; ++i)
                dma16<true>(isV ? srdV : srdK, kvoff, (unsigned)(KB(t) * 16384 + i * 4096), lds0 + (isV ? vslot(t) : kslot(t)) + (4 * i + w4) * 1024);
        };
        u32x4 pre[8];
        {
            const int rot = (((hl + 2 * bl) & 7) * 9) / 8;
            const srd_t srdX = __builtin_amdgcn_make_buffer_rsrc((void*)(x + (size_t)b * SQ * DM), (short)0, SQ * DM * 2, 0x00020000);
            const srd_t srdW = __builtin_amdgcn_make_buffer_rsrc((void*)(wq + h * DH), (short)0, DM * HD * 2 - h * DH * 2, 0x00020000);
            const unsigned xvoff = (8 * w4 + (lane >> 3)) * (DM * 2) + (((lane & 7) ^ ((4 * (w4 & 1) + (lane >> 4)) & 7)) << 4);
            const unsigned wvoff = arow * (HD * 2) + ach * 16;
            auto issue = [&](int s, int buf) __attribute__((always_inline)) {
                int cs = s + rot; if (cs >= 10) cs -= 9; if (s == 0) cs = 0;
                const unsigned dst = lds0 + RING0 + buf;
#pragma unroll
                for (int i = 0; i < 4; ++i) dma16<false>(srdW, wvoff, (unsigned)((cs * 64 + i * 16) * HD * 2), dst + RXB + (4 * i + w4) * 1024);
#pragma unroll
                for (int i = 0; i < 6; ++i) dma16<false>(srdX, xvoff, (unsigned)(i * 32 * DM * 2 + cs * 128), dst + (4 * i + w4) * 1024);
            };
            if (wid == 4 && lane < 16) flagw[lane] = 0u;
            issue(0, 0); issue(1, RSTG);
            int rb = 2 * RSTG;
#pragma unroll 1
            for (int s = 0; s < 8; ++s) { WAITV(10); BAR_L(); issue(s + 2, rb); rb = (rb == 2 * RSTG) ? 0 : rb + RSTG; }
#define PRELD(n0, n1) _Pragma("unroll") for (int n = n0; n < n1; ++n) ld16_async<true>(pre[n], n < 4 ? srdV : srdK, kvoff, (unsigned)(KB(n < 4 ? 0 : 2) * 16384 + (n & 3) * 4096))
            kv_unit(false, 0);
            WAITV(14); BAR_L(); kv_unit(false, 1); PRELD(0, 4);
            WAITV(12); BAR_L(); PRELD(4, 8);
            BAR_L();
            kv_unit(true, 1);
            WAITV(12);
            BAR_L();
            kv_unit(false, 3);
        }
        f32x16 o[3][2];
#pragma unroll
        for (int i = 0; i < 3; ++i) { o[i][0] = f32x16{}; o[i][1] = f32x16{}; }
        const int vb0 = 1024 * kh + 64 * (4 * hh + q4) + (((2 * blk + (p4 >> 1)) ^ hh) << 4) + 8 * (p4 & 1);
        const int vb1 = 1024 * kh + 2048 + 64 * (4 * hh + q4) + (((2 * blk + (p4 >> 1)) ^ (2 + hh)) << 4) + 8 * (p4 & 1);
        const lds_cptr pb0 = L + POFF + qh * 12288 + lane * 16;
        s16x4 vl[2][4][2], vh[2][4][2];
        bf16x8 pf[2][3];
        unsigned wv[5];
#pragma unroll
        for (int p = 0; p < 5; ++p) { const int o_ = p * 1024 + lane * 16, row = o_ / 160, ch = (o_ % 160) >> 4; wv[p] = row * (DM * 2) + ch * 16; }
        const srd_t srdO = __builtin_amdgcn_make_buffer_rsrc((void*)(ws + WS_O + (size_t)(b * NH + hg * 8 + hl) * 49152), (short)0, 49152, 0x00020000);
        u32x4 wr[10];
        const int wg0 = 10 + w4, wg1 = w4 < 2 ? 14 + w4 : 4 + w4;
        u32x4 ow[3][4];
        auto orl = [&](int i) __attribute__((always_inline)) -> float { return 1.0f / (lsum[(qh * 3 + i) * 32 + r32] + lsum[((qh + 2) * 3 + i) * 32 + r32]); };
        auto opiece = [&](int i, int k, float rl) __attribute__((always_inline)) {
            const int db = k >> 1, s = k & 1;
#pragma unroll
            for (int e = 0; e < 4; ++e) { float a_ = o[i][db][8 * s + 2 * e] * rl; PIN(a_); ow[i][k][e] = cvtpk(a_, o[i][db][8 * s + 2 * e + 1] * rl); }
            st16_wt(ow[i][k], srdO, (r32 >> 4) * 4096 + hh * 256 + (r32 & 15) * 16, (3 * qh + i) * 8192 + (kh * 2 + db) * 1024 + s * 512);
        };
        auto opack = [&](int i) __attribute__((always_inline)) { const float rl = orl(i); opiece(i, 0, rl); opiece(i, 1, rl); opiece(i, 2, rl); opiece(i, 3, rl); };
        auto olds = [&](int i) __attribute__((always_inline)) {
#pragma unroll
            for (int db = 0; db < 2; ++db) { const lds_ptr p_ = LW + own_addr((3 * qh + i) * 4 + kh * 2 + db) + lane * 16;
                *(LAS u32x4*)p_ = ow[i][db * 2]; *(LAS u32x4*)(p_ + 1024) = ow[i][db * 2 + 1]; }
        };
#define RDV(X, vb, ks, db) do { vl[X][ks][db] = ldstr(vb + vb0 + (ks) * 4096 + (db) * 512); vh[X][ks][db] = ldstr(vb + vb1 + (ks) * 4096 + (db) * 512); } while (0)
#define RDP(buf, blk_, ks, i) pf[buf][i] = ldsr128(pb0 + ((blk_) & 1) * 6144 + ((ks) >> 1) * 24576 + ((i) * 2 + ((ks) & 1)) * 1024)
#define PVM(X, buf, ks, i, db) o[i][db] = MFMA(cat8(vl[X][ks][db], vh[X][ks][db]), pf[buf][i], o[i][db])
        auto epilogue = [&]() __attribute__((always_inline)) {
            opack(2); olds(2);
            WAITV(4);
            BAR_L();
#pragma unroll
            for (int p = 0; p < 5; ++p) *(LAS u32x4*)(LW + wg1 * 5120 + p * 1024 + lane * 16) = wr[5 + p];
        };
        auto period = [&](auto tag) __attribute__((always_inline)) -> bool {
            constexpr int P = decltype(tag)::value, X = P & 1;
            constexpr bool AB = P <= 8, C = P >= 2;
            const lds_cptr vb = L + vslot(AB ? P - 1 : 0);
            auto dm = [&](int n) __attribute__((always_inline)) {
                if constexpr (P <= 5) {
                    if (n < 4) dma16<true>(srdV, kvoff, (unsigned)(KB(P + 1) * 16384 + n * 4096), lds0 + vslot(P + 1) + (4 * n + w4) * 1024);
                    else if constexpr (P < 5) dma16<true>(srdK, kvoff, (unsigned)(KB(P + 3) * 16384 + (n - 4) * 4096), lds0 + kslot(P + 3) + (4 * (n - 4) + w4) * 1024);
                    else dma16<true>(srdV, kvoff, (unsigned)(KB(7) * 16384 + (n - 4) * 4096), lds0 + vslot(7) + (4 * (n - 4) + w4) * 1024);
                } else if constexpr (P <= 8) {
                    const int g = (P == 6) ? 16 + w4 : (P == 7) ? w4 : (w4 < 2 ? 4 + w4 : 6 + w4);
                    if (n < 5) dma16<false>(srdWo, wv[n], wo_soff(g), lds0 + g * 5120 + n * 1024);
                }
            };
            auto rd = [&](int buf, int ks, int part) __attribute__((always_inline)) {
                if (part == 0) { if (C) RDP(buf, P - 2, ks, 2); }
                if (part == 1) { if (AB) RDV(X, vb, ks, 0); }
                if (part == 2) { if (AB) RDP(buf, P - 1, ks, 0); }
                if (part == 3) { if (AB) RDV(X, vb, ks, 1); }
                if (part == 4) { if (AB) RDP(buf, P - 1, ks, 1); }
            };
            auto wrl = [&](int n) __attribute__((always_inline)) {
                if constexpr (P == 6 || P == 7) { if (n < 5) ld16_async<false>(wr[(P - 6) * 5 + n], srdWo, wv[n], wo_soff(P == 6 ? wg0 : wg1)); }
            };
            const unsigned flv = flagw[P - 1];
            rd(0, 0, 0); rd(0, 0, 1); rd(0, 0, 2); rd(0, 0, 3); rd(0, 0, 4); SBAR();
            if (__builtin_expect(__builtin_amdgcn_readfirstlane(flv) != 0u, 0)) {
                if constexpr (P == 7 || P == 8) asm volatile("s_waitcnt vmcnt(0)" :: "v"(wr[0]), "v"(wr[1]), "v"(wr[2]), "v"(wr[3]), "v"(wr[4]) : "memory");
                if constexpr (P == 8) asm volatile("" :: "v"(wr[5]), "v"(wr[6]), "v"(wr[7]), "v"(wr[8]), "v"(wr[9]) : "memory");
                return true;
            }
            if constexpr (P == 9) {
                bf16x8 pc[4]; pc[0] = pf[0][2];
#pragma unroll
                for (int ks = 1; ks < 4; ++ks) pc[ks] = ldsr128(pb0 + (6144 + (ks >> 1) * 24576 + (4 + (ks & 1)) * 1024));
                SBAR();
#pragma unroll
                for (int k = 0; k < 8; ++k) {
                    o[2][k & 1] = MFMA(cat8(vl[X ^ 1][k >> 1][k & 1], vh[X ^ 1][k >> 1][k & 1]), pc[k >> 1], o[2][k & 1]);
                    *(LAS u32x4*)(LW + own_addr((3 * qh + (k >> 2)) * 4 + kh * 2 + ((k >> 1) & 1)) + (k & 1) * 1024 + lane * 16) = ow[k >> 2][k & 3];
                    if (k < 5) *(LAS u32x4*)(LW + wg0 * 5120 + k * 1024 + lane * 16) = wr[k];
                    SBAR();
                }
            } else if constexpr (P == 8) {
                bf16x8 pc[4]; pc[0] = pf[0][2];
                const float rl0 = orl(0), rl1 = orl(1);
#pragma unroll
                for (int ks = 0; ks < 4; ++ks) {
                    const int cb = ks & 1; const bool more = ks < 3;
                    PVM(X, cb, ks, 0, 0); if (more) { rd(cb ^ 1, ks + 1, 1); rd(cb ^ 1, ks + 1, 2); } SBAR();
                    PVM(X, cb, ks, 0, 1); if (more) rd(cb ^ 1, ks + 1, 3); dm(2 * ks); SBAR();
                    PVM(X, cb, ks, 1, 0); if (more) { rd(cb ^ 1, ks + 1, 4); pc[ks + 1] = ldsr128(pb0 + (((ks + 1) >> 1) * 24576 + (4 + ((ks + 1) & 1)) * 1024)); } SBAR();
                    PVM(X, cb, ks, 1, 1); dm(2 * ks + 1); SBAR();
                }
                asm volatile("s_waitcnt vmcnt(5)" : "+v"(wr[0]), "+v"(wr[1]), "+v"(wr[2]), "+v"(wr[3]), "+v"(wr[4]), "+v"(wr[5]), "+v"(wr[6]), "+v"(wr[7]), "+v"(wr[8]), "+v"(wr[9]) :: "memory");
#pragma unroll
                for (int k = 0; k < 8; ++k) {
                    o[2][k & 1] = MFMA(cat8(vl[X ^ 1][k >> 1][k & 1], vh[X ^ 1][k >> 1][k & 1]), pc[k >> 1], o[2][k & 1]); SBAR();
                    opiece(k >> 2, k & 3, k < 4 ? rl0 : rl1); SBAR();
                }
            } else {
#pragma unroll
                for (int ks = 0; ks < 4; ++ks) {
                    const int cb = ks & 1; const bool more = ks < 3;
                    if (C) PVM(X ^ 1, cb, ks, 2, 0); if (more) rd(cb ^ 1, ks + 1, 0); SBAR();
                    if (C) PVM(X ^ 1, cb, ks, 2, 1); if (more) rd(cb ^ 1, ks + 1, 1); wrl(2 * ks); SBAR();
                    if (AB) PVM(X, cb, ks, 0, 0); if (more) rd(cb ^ 1, ks + 1, 2); SBAR();
                    if (AB) PVM(X, cb, ks, 0, 1); if (more) rd(cb ^ 1, ks + 1, 3); dm(2 * ks); SBAR();
                    if (AB) PVM(X, cb, ks, 1, 0); if (more) rd(cb ^ 1, ks + 1, 4); wrl(2 * ks + 1); SBAR();
                    if (AB) PVM(X, cb, ks, 1, 1); dm(2 * ks + 1); SBAR();
                }
            }
            if constexpr (P <= 5) WAITV(8); else if constexpr (P == 6) WAITV(14); else if constexpr (P == 7) WAITV(20);
            if constexpr (P <= 8) BAR_L(); else epilogue();
            return false;
        };
        BAR_L();
        asm volatile("s_waitcnt vmcnt(8)" : "+v"(pre[0]), "+v"(pre[1]), "+v"(pre[2]), "+v"(pre[3]), "+v"(pre[4]), "+v"(pre[5]), "+v"(pre[6]), "+v"(pre[7]) :: "memory");
#pragma unroll
        for (int n = 0; n < 8; ++n) *(LAS u32x4*)(LW + slot_addr(2 + (n >> 2)) + (4 * (n & 3) + w4) * 1024 + lane * 16) = pre[n];
        BAR_L();
        int tf = 9;
#define PERIOD(P) if (__builtin_expect(tf == 9, 1)) { if (period(std::integral_constant<int, P>{})) tf = P - 1; }
        PERIOD(1) PERIOD(2) PERIOD(3) PERIOD(4) PERIOD(5) PERIOD(6) PERIOD(7) PERIOD(8) PERIOD(9)
        slow = tf < 9;
        if (__builtin_expect(slow, 0)) {
            WAITV(0); BAR_L();
#pragma unroll 1
            for (int t = tf - 1; t < 8; ++t) {
                const int sl = slot_addr(2 + (t & 1));
#pragma unroll
                for (int n = 0; n < 4; ++n) dma16<true>(srdV, kvoff, (unsigned)(KB(t) * 16384 + n * 4096), lds0 + sl + (4 * n + w4) * 1024);
                BAR_L();
#pragma unroll
                for (int i = 0; i < 3; ++i) {
                    const float dl = fmaxf(prop[(qh * 3 + i) * 32 + r32], prop[((qh + 2) * 3 + i) * 32 + r32]);
                    const float f = __builtin_amdgcn_exp2f(-dl);
#pragma unroll
                    for (int r = 0; r < 16; ++r) { o[i][0][r] *= f; o[i][1][r] *= f; }
                }
                WAITV(0);
                BAR_L();
                const lds_cptr vb = L + sl;
#pragma unroll
                for (int ks = 0; ks < 4; ++ks) {
                    RDV(0, vb, ks, 0); RDV(0, vb, ks, 1); RDP(0, t, ks, 0); RDP(0, t, ks, 1); RDP(0, t, ks, 2);
#pragma unroll
                    for (int i = 0; i < 3; ++i) { PVM(0, 0, ks, i, 0); PVM(0, 0, ks, i, 1); }
                }
            }
            BAR_L();
#pragma unroll
            for (int p = 0; p < 5; ++p) { wr[p] = __builtin_amdgcn_raw_buffer_load_b128(srdWo, wv[p], wo_soff(wg0), 0); wr[5 + p] = __builtin_amdgcn_raw_buffer_load_b128(srdWo, wv[p], wo_soff(wg1), 0); }
#pragma unroll
            for (int p = 0; p < 10; ++p) PIN(wr[p]);
#pragma unroll
            for (int p = 0; p < 5; ++p) *(LAS u32x4*)(LW + wg0 * 5120 + p * 1024 + lane * 16) = wr[p];
            opack(0); opack(1); olds(0); olds(1);
            epilogue();
        }
    }
    int ln; asm volatile("v_mbcnt_lo_u32_b32 %0, -1, 0\n\tv_mbcnt_hi_u32_b32 %0, -1, %0" : "=v"(ln));
    const int qg = wid & 3, kh2 = wid >> 2;
    gu32* const pflag = PFLAG(ln);
    if (__builtin_expect(slow, 0)) { if (wid < 4) { wo_groups(w4, 4, 4); wo_groups(w4 < 2 ? 4 + w4 : 6 + w4, 10, 8); wo_groups(16 + w4, 20, 4); } WAITV(0); BAR_L(); }
    {
        const int g = ln >> 4, qq = ln & 15;
        const srd_t srdOg = __builtin_amdgcn_make_buffer_rsrc((void*)(ws + WS_O + (size_t)(b * NH + hg * 8) * 49152), (short)0, 8 * 49152, 0x00020000);
        const unsigned ov0 = (g >> 1) * 1024 + (g & 1) * 512 + qq * 16, ov = ln * 16 + kh2 * 2048;
#define T16(qg_, i) (6 * ((qg_) >> 1) + ((i) < 2 ? 2 * ((qg_) & 1) + (i) : 4 + ((qg_) & 1)))
#define JISC(j) ((j) >= 8 && (j) < 20 && ((j) & 1))
#define JSTEP(j) ((j) < 8 ? (j) : (j) >= 20 ? (j) - 6 : JISC(j) ? ((j) - 5) / 2 : 4 + (j) / 2)
#define JHAS(j, i) (JISC(j) ? (i) == 2 : ((j) >= 2 && (j) < 8) ? (i) < 2 : true)
        constexpr int XC = 6;
        constexpr int OW = 5;
        u32x4 of[OW][3], fo[4][3];
#define LDOG(st, i) __builtin_amdgcn_raw_buffer_load_b128(srdOg, ov, ((hl + hdir * ((st) >> 1)) & 7) * 49152 + T16(qg, i) * 4096 + ((st) & 1) * 1024, 16)
#define LDO1(j, i) do { if (JHAS(j, i)) of[(j) % OW][i] = LDOG(JSTEP(j), i); } while (0)
#pragma unroll
        for (int st = 0; st < 2; ++st)
#pragma unroll
            for (int i = 0; i < 3; ++i) { const int t16 = T16(qg, i);
                of[st][i] = *(const LAS u32x4*)(L + own_addr((t16 >> 1) * 4 + kh2 * 2 + st) + (t16 & 1) * 256 + ov0); }
        f32x4 acc[5][3];
#pragma unroll
        for (int n = 0; n < 5; ++n)
#pragma unroll
            for (int i = 0; i < 3; ++i) acc[n][i] = f32x4{};
        lds_cptr wb[3];
        wb[0] = L + kh2 * 5120 + (16 * (g >> 1) + 4 * (g & 1) + (qq >> 2)) * 160 + (ln & 3) * 8;
        wb[1] = wb[0] + 61440; wb[2] = wb[0] + 122880;
        PIN(wb[1]); PIN(wb[2]);
        s16x4 wl[2][5], wh[2][5];
#define RDWO(buf, st, n) do { const lds_cptr p_ = wb[PAIR_OF_STEP[st] / 6] + (PAIR_OF_STEP[st] % 6) * 10240 + (n) * 32; wl[buf][n] = ldstr(p_); wh[buf][n] = ldstr(p_ + 1280); } while (0)
#pragma unroll
        for (int n = 0; n < 5; ++n) RDWO(0, 0, n);
        if (wid >= 4) WAITV(0);
        BAR_L();
        unsigned fc = 0;
        if (wid == 0) { if (ln == hl) __hip_atomic_store(pflag, base + 2u, RLX_AGENT); fc = __hip_atomic_load(pflag, RLX_AGENT); }
        else if (wid < 4) wo_groups(19 + w4, 32, 3);
        SBAR();
#pragma unroll
        for (int j = 0; j < 20; ++j) {
            if (j == 1) {
                if (wid == 0) {
#pragma unroll 1
                    for (unsigned sp = 0; __any(ln < 8 && fc == base) && sp < SPIN_CAP; ++sp) fc = __hip_atomic_load(pflag, RLX_AGENT);
                }
                BAR_L();
#pragma unroll
                for (int j2 = 2; j2 < 2 + OW - 1; ++j2) { LDO1(j2, 0); LDO1(j2, 1); LDO1(j2, 2); }
                SBAR();
            }
            if (j == XC - 1 && wid == 0) fc = __hip_atomic_load(pflag, RLX_AGENT);
            if (j == XC) {
                if (wid == 0) {
#pragma unroll 1
                    for (unsigned sp = 0; __any(ln < 8 && fc - base < 2u) && sp < SPIN_CAP; ++sp) { __builtin_amdgcn_s_sleep(1); fc = __hip_atomic_load(pflag, RLX_AGENT); }
                }
                BAR_L();
            }
            if (j >= 2 && (j & 1) == 0) { if (kh2 == ((j >> 1) & 1)) __builtin_amdgcn_s_setprio(1); else __builtin_amdgcn_s_setprio(0); }
            if (j == 18) BAR_L();
#pragma unroll
            for (int n = 0; n < 5; ++n) {
#pragma unroll
                for (int i = 0; i < 3; ++i)
                    if (JHAS(j, i)) acc[n][i] = MFMA16(cat8(wl[j & 1][n], wh[j & 1][n]), __builtin_bit_cast(bf16x8, of[j % OW][i]), acc[n][i]);
                if (j < 19) RDWO((j + 1) & 1, JSTEP(j + 1), n);
                if (j >= 2 && j + OW - 1 < 20 && n < 3 && (n < 2 || j >= XC)) LDO1(j + OW - 1, n);
                if (j == XC && n >= 3 && n + 5 < XC + OW - 1) LDO1(n + 5, 2);
                if (j >= 16 && n < 3) fo[j - 16][n] = __builtin_amdgcn_raw_buffer_load_b128(srdOg, ln * 16, ((hl + hdir * 7) & 7) * 49152 + T16(qg, n) * 4096 + (j - 16) * 1024, 16);
                SBAR();
            }
        }
        const int dst = ((xcd & 4) | qg) + 8 * jj;
        const srd_t srdP = __builtin_amdgcn_make_buffer_rsrc((void*)(ws + WS_PART + (size_t)dst * 24576 + ((hg - qg - 1) & 3) * 8192), (short)0, 8192, 0x00020000);
        auto fin = [&](auto tag) __attribute__((always_inline)) {
            constexpr int H = decltype(tag)::value, N0 = H ? 3 : 0, N1 = H ? 5 : 3;
            const lds_cptr wf = wb[2] - kh2 * 5120 + 20480;
#define RDWF(buf, db, n) do { const lds_cptr p_ = wf + ((db) & 1) * 10240 + ((db) >> 1) * 5120 + (n) * 32; wl[buf][n] = ldstr(p_); wh[buf][n] = ldstr(p_ + 1280); } while (0)
#pragma unroll
            for (int n = N0; n < N1; ++n) RDWF(0, 0, n);
            f32x4 pa_[9];
#pragma unroll
            for (int db = 0; db < 4; ++db) {
                if (db == 2) { BAR_L(); }
#pragma unroll
                for (int n = N0; n < N1; ++n) {
#pragma unroll
                    for (int i = 0; i < 3; ++i) acc[n][i] = MFMA16(cat8(wl[db & 1][n], wh[db & 1][n]), __builtin_bit_cast(bf16x8, fo[db][i]), acc[n][i]);
                    if (db < 3) RDWF((db + 1) & 1, db + 1, n);
                    if (db < 2) {
                        constexpr int NO = H ? 9 : 6, PER = H ? 3 : 1;
#pragma unroll
                        for (int k = 0; k < PER; ++k) { const int q_ = (db * (N1 - N0) + (n - N0)) * PER + k, tl = (H ? 0 : 9) + q_;
                            if (q_ < NO) *(LAS f32x4*)(LW + ((qg * 15 + tl) * 64 + ln) * 16) = acc[tl / 3][tl % 3]; }
                    }
                    if (db == 2) {
#pragma unroll
                        for (int i = 0; i < 3; ++i) pa_[(n - N0) * 3 + i] = *(const LAS f32x4*)(L + ((qg * 15 + n * 3 + i) * 64 + ln) * 16);
                    }
                    SBAR();
                }
            }
#pragma unroll
            for (int u = (H ? 5 : 0); u < (H ? 8 : 5); ++u) {
                u32x4 w = {};
#pragma unroll
                for (int k = 0; k < 2; ++k) { const int tl = H ? 2 * u - 1 + k : 2 * u + k;
                    if (tl < 3 * N1) {
                        const f32x4 v = acc[tl / 3][tl % 3] + pa_[tl - 3 * N0];
                        if (qg == hg) *(LAS f32x4*)(LW + ((qg * 15 + tl) * 64 + ln) * 16) = v;
                        w[2 * k] = cvtpk(v[0], v[1]); w[2 * k + 1] = cvtpk(v[2], v[3]);
                    } }
                if (qg != hg) st16_wt(w, srdP, ln * 16, u * 1024);
            }
        };
        if (kh2) fin(std::integral_constant<int, 1>{}); else fin(std::integral_constant<int, 0>{});
        WAITV(0);
        BAR_L();
        if (wid == 0) {
            if (ln == 8 + hg) __hip_atomic_store(pflag, base + 1u, RLX_AGENT);
#pragma unroll 1
            for (unsigned sp = 0; sp < SPIN_CAP; ++sp) {
                const unsigned v = __hip_atomic_load(pflag, RLX_AGENT);
                if (!__any(ln >= 8 && ln < 12 && v == base)) break;
                __builtin_amdgcn_s_sleep(1);
            }
        }
        BAR_L();
        const srd_t srdI = __builtin_amdgcn_make_buffer_rsrc((void*)(ws + WS_PART + (size_t)blockIdx.x * 24576), (short)0, 24576, 0x00020000);
        const srd_t srdOut = __builtin_amdgcn_make_buffer_rsrc((void*)(out + (size_t)b * SQ * DM), (short)0, SQ * DM * 2, 0x00020000);
        u32x4 pv_[3];
#pragma unroll
        for (int j = 0; j < 3; ++j) pv_[j] = __builtin_amdgcn_raw_buffer_load_b128(srdI, ln * 16, j * 8192 + wid * 1024, 16);
#pragma unroll
        for (int k = 0; k < 2; ++k) {
            const int tl = wid < 5 ? 2 * wid + k : 2 * wid - 1 + k;
            if (!(wid == 4 && k == 1)) {
                f32x4 sum = *(const LAS f32x4*)(L + ((hg * 15 + tl) * 64 + ln) * 16);
#pragma unroll
                for (int j = 0; j < 3; ++j) {
                    sum[0] += __uint_as_float(pv_[j][2 * k] << 16); sum[1] += __uint_as_float(pv_[j][2 * k] & 0xffff0000u);
                    sum[2] += __uint_as_float(pv_[j][2 * k + 1] << 16); sum[3] += __uint_as_float(pv_[j][2 * k + 1] & 0xffff0000u);
                }
                const int n = tl / 3, i = tl % 3;
                const int q = T16(hg, i) * 16 + qq, col = hl * 80 + n * 16 + g * 4;
                u32x2 w; w[0] = cvtpk(sum[0], sum[1]); w[1] = cvtpk(sum[2], sum[3]);
                __builtin_amdgcn_raw_buffer_store_b64(w, srdOut, (q * DM + col) * 2, 0, 0);
            }
        }
    }
}

extern "C" void kernel_launch(void* const* d_in, const int* in_sizes, int n_in, void* d_out, int out_size, void* d_ws, size_t ws_size, hipStream_t stream) {
    const bf16* x = (const bf16*)d_in[0]; const bf16* kc = (const bf16*)d_in[1]; const bf16* vc = (const bf16*)d_in[2];
    const bf16* wq = (const bf16*)d_in[3]; const bf16* wo = (const bf16*)d_in[4];
    (void)hipFuncSetAttribute((const void*)k1_attn, hipFuncAttributeMaxDynamicSharedMemorySize, LDS_BYTES);
    k1_attn<<<256, 512, LDS_BYTES, stream>>>(x, kc, vc, wq, wo, (char*)d_ws, (bf16*)d_out);
}
```
